```python
import torch

def _softmax(x, axis=-1):
    return torch.softmax(x, dim=axis)
def _gelu(x):
    return torch.nn.functional.gelu(x, approximate="tanh")
def _silu(x):
    return torch.nn.functional.silu(x)
def _sigmoid(x):
    return torch.sigmoid(x)
def _layernorm(x, axis=-1, eps=1e-5):
    m = x.mean(dim=axis, keepdim=True)
    v = x.var(dim=axis, keepdim=True, unbiased=False)
    return (x - m) / torch.sqrt(v + eps)
def _scatter_add(tgt, idx, src, axis=0):
    return tgt.clone().index_add_(axis, idx, src)
def _segment_sum(src, seg, n):
    out = torch.zeros((n,) + src.shape[1:], dtype=src.dtype)
    return out.index_add_(0, seg, src)
def _pad(x, pw):
    flat = tuple(v for pair in reversed(pw) for v in pair)
    return torch.nn.functional.pad(x, flat)

kernel_name = 'recurrent_scan__ssm_conv_s5__glu_gated_swiglu__prompt'

def setup_inputs():

    _w = (lambda a: (lambda t: t.to(torch.bfloat16) if t.is_floating_point() else t)(torch.as_tensor(a)))
    def _A_setup():
        B, S, D = 1, 2048, 640
        P = D
        Lambda_re = -0.5 * (lambda _x: torch.exp(torch.as_tensor(_x)))((lambda *s: torch.randn(*s, dtype=torch.bfloat16) * 0.8)(P))
        Lambda_im = (lambda *s: torch.randn(*s, dtype=torch.bfloat16) * 0.8)(P) * 1.0
        B_re = (lambda *s: torch.randn(*s, dtype=torch.bfloat16) * 0.8)(P, D) * (D ** -0.5)
        B_im = (lambda *s: torch.randn(*s, dtype=torch.bfloat16) * 0.8)(P, D) * (D ** -0.5)
        C_re = (lambda *s: torch.randn(*s, dtype=torch.bfloat16) * 0.8)(D, P) * (P ** -0.5)
        C_im = (lambda *s: torch.randn(*s, dtype=torch.bfloat16) * 0.8)(D, P) * (P ** -0.5)
        log_dt = (lambda *s: torch.randn(*s, dtype=torch.bfloat16) * 0.8)(P) * 0.3 - 2.0
        Dskip = (lambda *s: torch.randn(*s, dtype=torch.bfloat16) * 0.8)(D)
        return {
            "x": _w((lambda *s: torch.randn(*s, dtype=torch.bfloat16) * 0.05)(B, S, D)),
            "Lambda_re": _w(Lambda_re),
            "Lambda_im": _w(Lambda_im),
            "B_re": _w(B_re),
            "B_im": _w(B_im),
            "C_re": _w(C_re),
            "C_im": _w(C_im),
            "log_dt": _w(log_dt),
            "Dskip": _w(Dskip),
        }
    _A_in = _A_setup()

    def _B_setup():
        B, S, D, F = 1, 2048, 640, 1728
        return {
            "x": _w((lambda *s: torch.randn(*s, dtype=torch.bfloat16) * 0.05)(B, S, D)),
            "w1": _w((lambda *s: torch.randn(*s, dtype=torch.bfloat16) * 0.2)(D, F) * (2.0 / D) ** 0.5),
            "w3": _w((lambda *s: torch.randn(*s, dtype=torch.bfloat16) * 0.2)(D, F) * (2.0 / D) ** 0.5),
            "w2": _w((lambda *s: torch.randn(*s, dtype=torch.bfloat16) * 0.2)(F, D) * (2.0 / F) ** 0.5),
        }
    _B_in = _B_setup()

    return {
        "A_x": _A_in["x"],
        "A_Lambda_re": _A_in["Lambda_re"],
        "A_Lambda_im": _A_in["Lambda_im"],
        "A_B_re": _A_in["B_re"],
        "A_B_im": _A_in["B_im"],
        "A_C_re": _A_in["C_re"],
        "A_C_im": _A_in["C_im"],
        "A_log_dt": _A_in["log_dt"],
        "A_Dskip": _A_in["Dskip"],
        "B_x": _B_in["x"],
        "B_w1": _B_in["w1"],
        "B_w3": _B_in["w3"],
        "B_w2": _B_in["w2"],
    }

def reference(A_x, A_Lambda_re, A_Lambda_im, A_B_re, A_B_im, A_C_re, A_C_im, A_log_dt, A_Dskip, B_x, B_w1, B_w3, B_w2):
    def _A_ref(x, Lambda_re, Lambda_im, B_re, B_im, C_re, C_im, log_dt, Dskip):
        Bsz, L, H = x.shape
        Lam = Lambda_re + 1j * Lambda_im
        Bmat = B_re + 1j * B_im
        Cmat = C_re + 1j * C_im
        dt = torch.exp(log_dt)
        Lam_bar = torch.exp(dt * Lam)
        B_bar = ((Lam_bar - 1.0) / Lam)[:, None] * Bmat
        Bu = torch.einsum("bld,pd->blp", x + 0j, B_bar)
        h = torch.zeros_like(Bu[:, 0, :])
        hs = []
        for t in range(L):
            h = Lam_bar[None, :] * h + Bu[:, t, :]
            hs.append(h)
        hseq = torch.stack(hs, 1)
        y = 2.0 * torch.einsum("blp,dp->bld", hseq, Cmat).real + x * Dskip[None, None, :]
        return y

    def _B_ref(x, w1, w3, w2):
        gate = torch.einsum("bsd,df->bsf", x, w1)
        up = torch.einsum("bsd,df->bsf", x, w3)
        h = _silu(gate) * up
        out = torch.einsum("bsf,fd->bsd", h, w2)
        return out

    x = A_x
    xn = _layernorm(x, axis=-1)
    s = _A_ref(xn, A_Lambda_re, A_Lambda_im, A_B_re, A_B_im, A_C_re, A_C_im, A_log_dt, A_Dskip)
    g = _silu(_B_ref(xn, B_w1, B_w3, B_w2))
    return x + s * g
```

```cpp
#include <hip/hip_runtime.h>
#include <hip/hip_bf16.h>
#include <stdint.h>
namespace naive {
constexpr int L = 2048, D = 640, P = 640, F = 1728;
typedef unsigned short u16;
__device__ __forceinline__ float bf2f(u16 v) { return __uint_as_float(((unsigned)v) << 16); }
__device__ __forceinline__ u16 f2bf(float f) { unsigned u = __float_as_uint(f); u += 0x7FFFu + ((u >> 16) & 1u); return (u16)(u >> 16); }
__device__ __forceinline__ float rb(float f) { return bf2f(f2bf(f)); }

__global__ void k_ln(const u16* x, float* xn) {
    int t = blockIdx.x; int lane = threadIdx.x;
    float s = 0; for (int i = lane; i < D; i += 64) s += bf2f(x[t * D + i]);
    for (int o = 32; o; o >>= 1) s += __shfl_xor(s, o);
    float m = s / D;
    float v = 0; for (int i = lane; i < D; i += 64) { float d = bf2f(x[t * D + i]) - m; v += d * d; }
    for (int o = 32; o; o >>= 1) v += __shfl_xor(v, o);
    v /= D;
    float mb = rb(m), vb = rb(v);
    float sd = rb(sqrtf(rb(vb + 1e-5f)));
    for (int i = lane; i < D; i += 64) xn[t * D + i] = rb(rb(bf2f(x[t * D + i]) - mb) / sd);
}
__global__ void k_param(const u16* lre, const u16* lim, const u16* ldt, float* lam, float* coef) {
    int p = blockIdx.x * blockDim.x + threadIdx.x; if (p >= P) return;
    float a = bf2f(lre[p]), b = bf2f(lim[p]);
    float dt = rb(expf(bf2f(ldt[p])));
    float er = expf(dt * a); float lr = er * cosf(dt * b), li = er * sinf(dt * b);
    lam[2 * p] = lr; lam[2 * p + 1] = li;
    float nr = lr - 1.0f, ni = li; float den = a * a + b * b;
    coef[2 * p] = (nr * a + ni * b) / den; coef[2 * p + 1] = (ni * a - nr * b) / den;
}
__global__ void k_bu(const float* xn, const u16* bre, const u16* bim, const float* coef, float* bu) {
    int p = blockIdx.x * blockDim.x + threadIdx.x; int l = blockIdx.y; if (p >= P) return;
    float cr = coef[2 * p], ci = coef[2 * p + 1];
    float ar = 0, ai = 0;
    for (int d = 0; d < D; ++d) { float xv = xn[l * D + d]; ar += xv * bf2f(bre[p * D + d]); ai += xv * bf2f(bim[p * D + d]); }
    bu[(l * P + p) * 2] = cr * ar - ci * ai; bu[(l * P + p) * 2 + 1] = cr * ai + ci * ar;
}
__global__ void k_scan(const float* lam, float* bu) {
    int p = blockIdx.x * blockDim.x + threadIdx.x; if (p >= P) return;
    float lr = lam[2 * p], li = lam[2 * p + 1]; float hr = 0, hi = 0;
    for (int l = 0; l < L; ++l) {
        float br = bu[(l * P + p) * 2], bi = bu[(l * P + p) * 2 + 1];
        float nr = lr * hr - li * hi + br, ni = lr * hi + li * hr + bi; hr = nr; hi = ni;
        bu[(l * P + p) * 2] = hr; bu[(l * P + p) * 2 + 1] = hi;
    }
}
__global__ void k_ffn1(const float* xn, const u16* w1, const u16* w3, float* h) {
    int f = blockIdx.x * blockDim.x + threadIdx.x; int l = blockIdx.y; if (f >= F) return;
    float g = 0, u = 0;
    for (int d = 0; d < D; ++d) { float xv = xn[l * D + d]; g += xv * bf2f(w1[d * F + f]); u += xv * bf2f(w3[d * F + f]); }
    g = rb(g); u = rb(u);
    float s = rb(g / (1.0f + expf(-g)));
    h[l * F + f] = rb(s * u);
}
__global__ void k_final(const u16* x, const float* xn, const float* hs, const float* h, const u16* cre, const u16* cim, const u16* w2, const u16* dsk, float* out) {
    int d = blockIdx.x * blockDim.x + threadIdx.x; int l = blockIdx.y; if (d >= D) return;
    float y = 0;
    for (int p = 0; p < P; ++p) y += hs[(l * P + p) * 2] * bf2f(cre[d * P + p]) - hs[(l * P + p) * 2 + 1] * bf2f(cim[d * P + p]);
    y = 2.0f * y + rb(xn[l * D + d] * bf2f(dsk[d]));
    float o = 0;
    for (int f = 0; f < F; ++f) o += h[l * F + f] * bf2f(w2[f * D + d]);
    o = rb(o);
    float g = rb(o / (1.0f + expf(-o)));
    out[l * D + d] = bf2f(x[l * D + d]) + y * g;
}
static void run(void* const* d_in, void* d_out, void* d_ws, hipStream_t stream) {
    const u16* x = (const u16*)d_in[0];
    float* xn = (float*)d_ws; float* lam = xn + L * D; float* coef = lam + 2 * P; float* bu = coef + 2 * P; float* h = bu + L * P * 2;
    k_ln<<<L, 64, 0, stream>>>(x, xn);
    k_param<<<(P + 63) / 64, 64, 0, stream>>>((const u16*)d_in[1], (const u16*)d_in[2], (const u16*)d_in[7], lam, coef);
    k_bu<<<dim3(P / 64, L), 64, 0, stream>>>(xn, (const u16*)d_in[3], (const u16*)d_in[4], coef, bu);
    k_scan<<<P / 64, 64, 0, stream>>>(lam, bu);
    k_ffn1<<<dim3(F / 64, L), 64, 0, stream>>>(xn, (const u16*)d_in[10], (const u16*)d_in[11], h);
    k_final<<<dim3(D / 64, L), 64, 0, stream>>>(x, xn, bu, h, (const u16*)d_in[5], (const u16*)d_in[6], (const u16*)d_in[12], (const u16*)d_in[8], (float*)d_out);
}
}
extern "C" void kernel_launch(void* const* d_in, const int* in_sizes, int n_in,
                              void* d_out, int out_size, void* d_ws, size_t ws_size,
                              hipStream_t stream) {
    naive::run(d_in, d_out, d_ws, stream);
}
```

```cpp
#include <hip/hip_runtime.h>
#include <stdint.h>

namespace s5 {
typedef unsigned short u16;
typedef short bf16x8 __attribute__((ext_vector_type(8)));
typedef short s16x4 __attribute__((ext_vector_type(4)));
typedef float f32x16 __attribute__((ext_vector_type(16)));
typedef float f32x4 __attribute__((ext_vector_type(4)));
typedef float f32x2 __attribute__((ext_vector_type(2)));
typedef unsigned u32x4 __attribute__((ext_vector_type(4)));
typedef unsigned u32x2 __attribute__((ext_vector_type(2)));
typedef __bf16 bf16x2 __attribute__((ext_vector_type(2)));
typedef __attribute__((address_space(3))) void* lds_vp;
typedef __attribute__((address_space(3))) s16x4* lds_s16x4p;
typedef __attribute__((address_space(1))) unsigned long long gu64;
typedef __attribute__((address_space(1))) unsigned gu32;
#define DI __device__ __forceinline__
#define RLX_AGENT __ATOMIC_RELAXED, __HIP_MEMORY_SCOPE_AGENT

constexpr int L = 2048, D = 640, P = 640, F = 1728;
constexpr int NSTEP = 94, NSTEP_F = 54;
constexpr size_t WS_EPOCH = 0;
constexpr size_t WS_GRAN = 4096;
constexpr size_t WS_STATS = WS_GRAN + 7 * 10 * 1024 + 2048;
constexpr size_t WS_ACT = 131072;
constexpr size_t ACT_BYTES = (size_t)32 * NSTEP * 4 * 1024;
constexpr size_t WS_W2T = WS_ACT + ACT_BYTES;
constexpr size_t W2T_BYTES = (size_t)8 * NSTEP * 5 * 1024;
static_assert(WS_STATS + 2048 * 8 <= WS_ACT, "ws map");
constexpr int LDS_XR = 0;
constexpr int LDS_WR = 65536;
constexpr int WSLOT = 20480;
constexpr int LDS_MISC = LDS_WR + 3 * WSLOT;
constexpr int LDS_RT = LDS_MISC;
constexpr int LDS_PAR = LDS_RT + 16384;
constexpr int LDS_CS = LDS_PAR + 2048;
constexpr int LDS_ST = LDS_CS + 5120 + 640;
constexpr int LDS_ET = LDS_ST + 2048;
constexpr int LDS1_BYTES = LDS_ET + 8192;
static_assert(LDS1_BYTES <= 163840, "lds");
constexpr int RSRC3 = 0x00020000;

struct Params {
  const u16 *x, *lre, *lim, *bre, *bim, *cre, *cim, *ldt, *dsk, *w1, *w3, *w2;
  float* out;
  char* ws;
};

DI float bf2f(u16 v) { return __uint_as_float(((unsigned)v) << 16); }
DI float rbf(float f) { unsigned u = __float_as_uint(f); u += 0x7FFFu + ((u >> 16) & 1u); return __uint_as_float(u & 0xFFFF0000u); }
DI unsigned pack2(float a, float b) { f32x2 p = {a, b}; return __builtin_bit_cast(unsigned, __builtin_convertvector(p, bf16x2)); }
DI float dot2(unsigned a, unsigned b, float c) { return __builtin_amdgcn_fdot2_f32_bf16(__builtin_bit_cast(bf16x2, a), __builtin_bit_cast(bf16x2, b), c, false); }
DI f32x16 mfma32(u32x4 a, u32x4 b, f32x16 c) { return __builtin_amdgcn_mfma_f32_32x32x16_bf16(__builtin_bit_cast(bf16x8, a), __builtin_bit_cast(bf16x8, b), c, 0, 0, 0); }
DI f32x4 mfma16(u32x4 a, u32x4 b, f32x4 c) { return __builtin_amdgcn_mfma_f32_16x16x32_bf16(__builtin_bit_cast(bf16x8, a), __builtin_bit_cast(bf16x8, b), c, 0, 0, 0); }
DI u32x4 tr_pair(const char* p0, const char* p1) {
  s16x4 lo = __builtin_amdgcn_ds_read_tr16_b64_v4i16((lds_s16x4p)p0);
  s16x4 hi = __builtin_amdgcn_ds_read_tr16_b64_v4i16((lds_s16x4p)p1);
  u32x2 a = __builtin_bit_cast(u32x2, lo), b = __builtin_bit_cast(u32x2, hi);
  return u32x4{a.x, a.y, b.x, b.y};
}
DI void dma16(__amdgpu_buffer_rsrc_t rs, char* lds, int voff, int soff) { __builtin_amdgcn_raw_ptr_buffer_load_lds(rs, (lds_vp)lds, 16, voff, soff, 0, 0); }
#define WAIT_V(n) asm volatile("s_waitcnt vmcnt(" #n ")" ::: "memory")
#define WAIT_L0() asm volatile("s_waitcnt lgkmcnt(0)" ::: "memory")
#define BAR() __builtin_amdgcn_s_barrier()
DI float silu_f(float g) { return g * __builtin_amdgcn_rcpf(1.0f + __builtin_amdgcn_exp2f(g * -1.44269504088896f)); }

template <bool SSM>
DI void phase1(const Params& p, char* smem, int mt, int cj  , int blk) {
  constexpr int NT = SSM ? 4 : 5;
  constexpr int NW = SSM ? 2 : 3;
  const int tid = threadIdx.x, lane = tid & 63, w = __builtin_amdgcn_readfirstlane(tid >> 6);
  const int r = lane & 31, h = lane >> 5;

  const __amdgpu_buffer_rsrc_t rx = __builtin_amdgcn_make_buffer_rsrc((void*)(p.x + (size_t)(256 * mt + 32 * w) * D), 0, 32 * D * 2, RSRC3);
  const int vx0 = (lane >> 3) * 1280 + (((lane & 7) ^ (lane >> 4)) << 4), vx1 = vx0 ^ 64;
  __amdgpu_buffer_rsrc_t rw;
  int vw0, vw1, vw2, sw_base, sw_step;
  if (SSM) {
    const int c = w >> 1;
    const u16* src = (c & 1) ? p.bim : p.bre;
    rw = __builtin_amdgcn_make_buffer_rsrc((void*)src, 0, P * D * 2, RSRC3);
    vw0 = vx0; vw1 = vx1; vw2 = 0;
    sw_base = (64 * cj + 32 * (c >> 1) + 16 * (w & 1)) * 1280; sw_step = 128;
  } else {
    const int u = w & 1;
    rw = __builtin_amdgcn_make_buffer_rsrc((void*)(u ? p.w3 : p.w1), 0, D * F * 2, RSRC3);
    int n0 = lane, n1 = 64 + lane, n2 = 128 + lane;
    const int x4 = u << 2;
    vw0 = ((n0 / 10) ^ x4) * 3456 + (n0 % 10) * 16 + cj * 160;
    vw1 = ((n1 / 10) ^ x4) * 3456 + (n1 % 10) * 16 + cj * 160;
    vw2 = (((n2 / 10) & 15) ^ x4) * 3456 + (n2 % 10) * 16 + cj * 160;
    sw_base = 16 * (w >> 1) * 3456; sw_step = 64 * 3456;
  }
  char* xr = smem + LDS_XR + w * 8192;
  auto issue_x = [&](int s) {
    char* d = xr + (s & 1) * 4096;
    dma16(rx, d, vx0, s * 128);
    dma16(rx, d + 1024, vx1, s * 128 + 10240);
    dma16(rx, d + 2048, vx0, s * 128 + 20480);
    dma16(rx, d + 3072, vx1, s * 128 + 30720);
  };
  auto issue_w = [&](int s, int slot) {
    const int so = sw_base + s * sw_step;
    if (SSM) {
      char* d = smem + LDS_WR + slot * WSLOT + w * 2048;
      dma16(rw, d, vw0, so);
      dma16(rw, d + 1024, vw1, so + 10240);
    } else {
      char* d = smem + LDS_WR + slot * WSLOT + (w >> 1) * 5120 + (w & 1) * 2560;
      dma16(rw, d, vw0, so);
      dma16(rw, d + 1024, vw1, so);
      if (lane < 32) dma16(rw, d + 2048, vw2, so);
    }
  };

  const int unit = w * 256 + blk;
  const bool has_unit = (w < 4) && (unit < 940);
  char* rt = smem + LDS_RT + (w & 3) * 4096;
  if (has_unit) {
    if (unit < 540) {
      const int s = unit / 10, dq = unit % 10;
      const __amdgpu_buffer_rsrc_t r2 = __builtin_amdgcn_make_buffer_rsrc((void*)p.w2, 0, F * D * 2, RSRC3);
      const int vo = (lane >> 3) * 1280 + (lane & 7) * 16;
#pragma unroll
      for (int i = 0; i < 4; ++i) dma16(r2, rt + i * 1024, vo, (32 * s + 8 * i) * 1280 + dq * 128);
    } else {
      const int u = unit - 540, c = u / 10, v = u % 10;
      const int vo = (lane & 15) * 1280 + (lane >> 4) * 16;
#pragma unroll
      for (int i = 0; i < 4; ++i) {
        const int sp = 4 * v + i, jg = sp >> 1, part = sp & 1;
        const __amdgpu_buffer_rsrc_t rc = __builtin_amdgcn_make_buffer_rsrc((void*)(part ? p.cim : p.cre), 0, D * P * 2, RSRC3);
        dma16(rc, rt + i * 1024, vo, 16 * c * 1280 + jg * 64);
      }
    }
  }
  issue_w(0, 0); issue_x(0); issue_w(1, 1); issue_x(1); issue_w(2, 2);

  if (SSM && w == 7) {
    const int ch = 64 * cj + lane;
    const float a = bf2f(p.lre[ch]), b = bf2f(p.lim[ch]);
    const float dt = rbf(expf(bf2f(p.ldt[ch])));
    const float er = expf(dt * a);
    float sn, cn; sincosf(dt * b, &sn, &cn);
    const float lr = er * cn, li = er * sn;
    const float nr = lr - 1.0f, ni = li, den = a * a + b * b;
    const float cr = (nr * a + ni * b) / den, ci = (ni * a - nr * b) / den;
    float pr = lr, pi = li;
#pragma unroll
    for (int i = 0; i < 4; ++i) { float t = pr * pr - pi * pi; pi = 2.f * pr * pi; pr = t; }
    const float l16r = pr, l16i = pi;
#pragma unroll
    for (int i = 0; i < 4; ++i) { float t = pr * pr - pi * pi; pi = 2.f * pr * pi; pr = t; }
    float* par = (float*)(smem + LDS_PAR) + lane * 8;
    *(f32x4*)par = f32x4{lr, li, cr, ci};
    *(f32x4*)(par + 4) = f32x4{l16r, l16i, pr, pi};
  }

  if (has_unit) {
    if (SSM) WAIT_V(14); else WAIT_V(17);
    char* dstb = p.ws + WS_W2T;
    if (unit < 540) {
      const int s = unit / 10, dq = unit % 10;
      const int g = lane >> 4, q = (lane >> 2) & 3, pp = lane & 3;
      const int row0 = 16 * (g >> 1) + 4 * (g & 1) + q;
#pragma unroll
      for (int i = 0; i < 4; ++i) {
        u32x4 v = tr_pair(rt + row0 * 128 + 32 * i + 8 * pp, rt + (row0 + 8) * 128 + 32 * i + 8 * pp);
        const int c = 4 * dq + i;
        *(u32x4*)(dstb + ((size_t)((c / 5) * NSTEP + s) * 5 + (c % 5)) * 1024 + lane * 16) = v;
      }
    } else {
      const int u = unit - 540, c = u / 10, v = u % 10;
#pragma unroll
      for (int i = 0; i < 4; ++i) {
        u32x4 val = *(const u32x4*)(rt + i * 1024 + lane * 16);
        const int s = NSTEP_F + 4 * v + i;
        *(u32x4*)(dstb + ((size_t)((c / 5) * NSTEP + s) * 5 + (c % 5)) * 1024 + lane * 16) = val;
      }
    }
  }

  const int xrow = SSM ? (16 * ((r >> 2) & 1) + 4 * (r >> 3) + (r & 3)) : r;
  const int xsw = (h ^ ((xrow >> 1) & 7)) << 4;
  int ax[4];
#pragma unroll
  for (int ks = 0; ks < 4; ++ks) ax[ks] = LDS_XR + w * 8192 + xrow * 128 + (xsw ^ (ks << 5));
  int aw[4];
  if (SSM) {
    const int sw = (h ^ ((r >> 1) & 7)) << 4;
#pragma unroll
    for (int ks = 0; ks < 4; ++ks) aw[ks] = LDS_WR + r * 128 + (sw ^ (ks << 5));
  } else {
    const int blkk = (lane >> 4) & 1, q = (lane >> 2) & 3, pp = lane & 3, up = (pp >> 1) & 1;
    aw[0] = LDS_WR + up * 2560 + (8 * h + 4 * (0 ^ up) + q) * 160 + (8 * blkk + 4 * (pp & 1)) * 2;
    aw[1] = LDS_WR + up * 2560 + (8 * h + 4 * (1 ^ up) + q) * 160 + (8 * blkk + 4 * (pp & 1)) * 2;
    aw[2] = aw[3] = 0;
  }
  auto read_w = [&](u32x4 (&wf)[NT], int ks, int slot_off) {
#pragma unroll
    for (int c = 0; c < NT; ++c) {
      if (SSM) wf[c] = *(const u32x4*)(smem + aw[ks] + slot_off + c * 4096);
      else wf[c] = tr_pair(smem + aw[0] + slot_off + ks * 5120 + c * 32, smem + aw[1] + slot_off + ks * 5120 + c * 32);
    }
  };
  auto read_x = [&](u32x4 (&xf)[4], int s) {
#pragma unroll
    for (int ks = 0; ks < 4; ++ks) xf[ks] = *(const u32x4*)(smem + ax[ks] + (s & 1) * 4096);
  };

  f32x16 acc[NT];
#pragma unroll
  for (int c = 0; c < NT; ++c)
#pragma unroll
    for (int i = 0; i < 16; ++i) acc[c][i] = 0.f;
  float sx = 0.f, sxx = 0.f, cs[NT];
#pragma unroll
  for (int c = 0; c < NT; ++c) cs[c] = 0.f;
  const unsigned ONES = 0x3F803F80u;

  if (SSM) WAIT_V(8); else WAIT_V(10);
  BAR();
  u32x4 xf[4], wf[NT], wn[NT];
  read_x(xf, 0);
  read_w(wf, 0, 0);
  WAIT_L0();
  issue_x(2);

  int slot_off = 0;
  for (int s = 0; s < 10; ++s) {
#pragma unroll
    for (int ks = 0; ks < 4; ++ks) {
      u32x4 xn[4];
      if (ks < 3) {
        read_w(wn, ks + 1, slot_off);
      } else if (s < 9) {
        WAIT_L0();
        if (s < 8) { if (SSM) WAIT_V(6); else WAIT_V(7); } else WAIT_V(0);
        BAR();
        const int nslot = (slot_off == 2 * WSLOT) ? 0 : slot_off + WSLOT;
        if (s + 3 < 10) issue_w(s + 3, slot_off / WSLOT);
        read_x(xn, s + 1);
        read_w(wn, 0, nslot);
        slot_off = nslot;
      }
      const u32x4 xv = xf[ks];
      sx = dot2(xv.x, ONES, sx); sxx = dot2(xv.x, xv.x, sxx);
      sx = dot2(xv.y, ONES, sx); sxx = dot2(xv.y, xv.y, sxx);
      sx = dot2(xv.z, ONES, sx); sxx = dot2(xv.z, xv.z, sxx);
      sx = dot2(xv.w, ONES, sx); sxx = dot2(xv.w, xv.w, sxx);
      if ((((s & 1) << 2) | ks) == w) {
#pragma unroll
        for (int c = 0; c < NT; ++c) {
          cs[c] = dot2(wf[c].x, ONES, cs[c]); cs[c] = dot2(wf[c].y, ONES, cs[c]);
          cs[c] = dot2(wf[c].z, ONES, cs[c]); cs[c] = dot2(wf[c].w, ONES, cs[c]);
        }
      }
#pragma unroll
      for (int c = 0; c < NT; ++c) acc[c] = SSM ? mfma32(xv, wf[c], acc[c]) : mfma32(wf[c], xv, acc[c]);
      if (ks == 3 && s < 9) {
        WAIT_L0();
        if (s + 3 < 10) issue_x(s + 3);
#pragma unroll
        for (int k2 = 0; k2 < 4; ++k2) xf[k2] = xn[k2];
      }
#pragma unroll
      for (int c = 0; c < NT; ++c) wf[c] = wn[c];
    }
  }

  sx += __shfl_xor(sx, 32); sxx += __shfl_xor(sxx, 32);
  const float mean = sx * (1.0f / D);
  const float var = fmaxf(sxx * (1.0f / D) - mean * mean, 0.f);
  const float rstd = 1.0f / sqrtf(var + 1e-5f);
  float* cst = (float*)(smem + LDS_CS);
#pragma unroll
  for (int c = 0; c < NT; ++c) { cs[c] += __shfl_xor(cs[c], 32); if (h == 0) cst[w * 160 + c * 32 + r] = cs[c]; }
  float* stt = (float*)(smem + LDS_ST);
  if (SSM) {
    if (h == 0) *(f32x2*)(stt + (w * 32 + xrow) * 2) = f32x2{rstd, -mean * rstd};
    if (cj == 0 && h == 0) *(f32x2*)(p.ws + WS_STATS + (size_t)(256 * mt + 32 * w + xrow) * 8) = f32x2{mean, rstd};
  }
  __syncthreads();
  if (tid < NT * 32) {
    float t = 0.f;
#pragma unroll
    for (int i = 0; i < 8; ++i) t += cst[i * 160 + tid];
    cst[8 * 160 + tid] = t;
  }
  __syncthreads();
  const float* csf = cst + 8 * 160;
  char* act = p.ws + WS_ACT;
  const int mt64 = 4 * mt + (w >> 1);

  if (!SSM) {
    const float mr = -mean * rstd;
#pragma unroll
    for (int c = 0; c < NT; ++c) {
      const int ft = 5 * cj + c;
      float hv[8];
#pragma unroll
      for (int q = 0; q < 2; ++q) {
        const f32x4 cg = *(const f32x4*)(csf + c * 32 + 16 * q + 4 * h);
        const f32x4 cu = *(const f32x4*)(csf + c * 32 + 16 * q + 8 + 4 * h);
#pragma unroll
        for (int e = 0; e < 4; ++e) {
          const float g = fmaf(acc[c][8 * q + e], rstd, mr * cg[e]);
          const float u = fmaf(acc[c][8 * q + 4 + e], rstd, mr * cu[e]);
          hv[4 * q + e] = silu_f(g) * u;
        }
      }
      if (ft < 108) {
        const u32x4 v = {pack2(hv[0], hv[1]), pack2(hv[2], hv[3]), pack2(hv[4], hv[5]), pack2(hv[6], hv[7])};
        const int s2 = ft >> 1, g = 2 * (ft & 1) + h, T = 2 * (w & 1) + (r >> 4);
        *(u32x4*)(act + ((size_t)(mt64 * NSTEP + s2) * 4 + T) * 1024 + (g * 16 + (r & 15)) * 16) = v;
      }
    }
  } else {
    const float* par = (const float*)(smem + LDS_PAR);
    float lr[2], li[2], cr[2], ci[2];
#pragma unroll
    for (int j = 0; j < 2; ++j) {
      const f32x4 pv = *(const f32x4*)(par + (32 * j + r) * 8);
      lr[j] = pv.x; li[j] = pv.y; cr[j] = pv.z; ci[j] = pv.w;
    }
    {
      float csl[4];
#pragma unroll
      for (int c = 0; c < 4; ++c) csl[c] = csf[c * 32 + r];
#pragma unroll
      for (int i = 0; i < 16; i += 2) {
        const f32x4 sv = *(const f32x4*)(stt + (w * 32 + 16 * h + i) * 2);
#pragma unroll
        for (int c = 0; c < 4; ++c) {
          acc[c][i] = fmaf(acc[c][i], sv.x, sv.y * csl[c]);
          acc[c][i + 1] = fmaf(acc[c][i + 1], sv.z, sv.w * csl[c]);
        }
      }
    }
    f32x2* et = (f32x2*)(smem + LDS_ET);
#pragma unroll
    for (int j = 0; j < 2; ++j) {
      float hr = 0.f, hi = 0.f;
#pragma unroll
      for (int i = 0; i < 16; ++i) {
        const float nr = fmaf(lr[j], hr, fmaf(-li[j], hi, acc[2 * j][i]));
        const float ni = fmaf(lr[j], hi, fmaf(li[j], hr, acc[2 * j + 1][i]));
        hr = nr; hi = ni;
      }
      et[(2 * w + h) * 64 + 32 * j + r] = f32x2{hr, hi};
    }
    __syncthreads();
    if (w == 0) {
      const f32x4 pv = *(const f32x4*)(par + lane * 8 + 4);
      gu32* ep = (gu32*)(p.ws + WS_EPOCH + cj * 128);
      const unsigned tag = __hip_atomic_load(ep, RLX_AGENT) + 1u;
      float xr_ = 0.f, xi_ = 0.f;
      f32x2 ev[16];
#pragma unroll
      for (int s = 0; s < 16; ++s) ev[s] = et[s * 64 + lane];
#pragma unroll
      for (int s = 0; s < 16; ++s) {
        const float nr = fmaf(pv.x, xr_, fmaf(-pv.y, xi_, ev[s].x));
        const float ni = fmaf(pv.x, xi_, fmaf(pv.y, xr_, ev[s].y));
        xr_ = nr; xi_ = ni;
      }
      gu64* gran = (gu64*)(p.ws + WS_GRAN);
      if (mt < 7) {
        gu64* g = gran + ((size_t)(mt * 10 + cj) * 64 + lane) * 2;
        __hip_atomic_store(g, ((unsigned long long)tag << 32) | __float_as_uint(xr_), RLX_AGENT);
        __hip_atomic_store(g + 1, ((unsigned long long)tag << 32) | __float_as_uint(xi_), RLX_AGENT);
      }
      float Hr = 0.f, Hi = 0.f;
      for (int m = 0; m < mt; ++m) {
        gu64* g = gran + ((size_t)(m * 10 + cj) * 64 + lane) * 2;
        unsigned long long a, b;
        unsigned spins = 0;
        while (true) {
          a = __hip_atomic_load(g, RLX_AGENT); b = __hip_atomic_load(g + 1, RLX_AGENT);
          const bool ok = ((unsigned)(a >> 32) == tag) && ((unsigned)(b >> 32) == tag);
          if (__all(ok)) break;
          if (++spins > (1u << 22)) break;
          __builtin_amdgcn_s_sleep(1);
        }
        const float sr = __uint_as_float((unsigned)a), si = __uint_as_float((unsigned)b);
        const float nr = fmaf(pv.z, Hr, fmaf(-pv.w, Hi, sr));
        const float ni = fmaf(pv.z, Hi, fmaf(pv.w, Hr, si));
        Hr = nr; Hi = ni;
      }
      if (mt == 7 && lane == 0) __hip_atomic_store(ep, tag, RLX_AGENT);
      xr_ = Hr; xi_ = Hi;
#pragma unroll
      for (int s = 0; s < 16; ++s) {
        et[s * 64 + lane] = f32x2{xr_, xi_};
        const float nr = fmaf(pv.x, xr_, fmaf(-pv.y, xi_, ev[s].x));
        const float ni = fmaf(pv.x, xi_, fmaf(pv.y, xr_, ev[s].y));
        xr_ = nr; xi_ = ni;
      }
    }
    __syncthreads();
    char* tp = smem + w * 10240;
#pragma unroll
    for (int j = 0; j < 2; ++j) {
      const f32x2 x0 = et[(2 * w + h) * 64 + 32 * j + r];
      float hr = x0.x, hi = x0.y;
      float ore[16], oim[16];
#pragma unroll
      for (int i = 0; i < 16; ++i) {
        const float nr = fmaf(lr[j], hr, fmaf(-li[j], hi, acc[2 * j][i]));
        const float ni = fmaf(lr[j], hi, fmaf(li[j], hr, acc[2 * j + 1][i]));
        hr = nr; hi = ni;
        ore[i] = cr[j] * hr - ci[j] * hi;
        oim[i] = -(cr[j] * hi + ci[j] * hr);
      }
      char* t0 = tp + (2 * j) * 2560 + r * 80 + 32 * h;
      char* t1 = tp + (2 * j + 1) * 2560 + r * 80 + 32 * h;
      *(u32x4*)t0 = u32x4{pack2(ore[0], ore[1]), pack2(ore[2], ore[3]), pack2(ore[4], ore[5]), pack2(ore[6], ore[7])};
      *(u32x4*)(t0 + 16) = u32x4{pack2(ore[8], ore[9]), pack2(ore[10], ore[11]), pack2(ore[12], ore[13]), pack2(ore[14], ore[15])};
      *(u32x4*)t1 = u32x4{pack2(oim[0], oim[1]), pack2(oim[2], oim[3]), pack2(oim[4], oim[5]), pack2(oim[6], oim[7])};
      *(u32x4*)(t1 + 16) = u32x4{pack2(oim[8], oim[9]), pack2(oim[10], oim[11]), pack2(oim[12], oim[13]), pack2(oim[14], oim[15])};
    }
    WAIT_L0();
    {
      const int g = lane >> 4, q = (lane >> 2) & 3, pp = lane & 3;
#pragma unroll
      for (int c = 0; c < 4; ++c) {
        const int s2 = NSTEP_F + 2 * (2 * cj + (c >> 1)) + (c & 1);
#pragma unroll
        for (int T = 0; T < 2; ++T) {
          const char* b0 = tp + c * 2560 + (8 * g + q) * 80 + (16 * T + 4 * pp) * 2;
          const u32x4 v = tr_pair(b0, b0 + 4 * 80);
          *(u32x4*)(act + ((size_t)(mt64 * NSTEP + s2) * 4 + 2 * (w & 1) + T) * 1024 + lane * 16) = v;
        }
      }
    }
  }
}

__global__ __launch_bounds__(512, 2) void k1(Params p) {
  extern __shared__ __attribute__((aligned(16))) char smem[];
  const int b = blockIdx.x, xcd = b & 7, i = b >> 3;
  const int mt = (xcd & 1) * 4 + (i & 3), cb = (xcd >> 1) * 8 + (i >> 2);
  if (cb < 22) phase1<false>(p, smem, mt, cb, b);
  else phase1<true>(p, smem, mt, cb - 22, b);
}

template <int N>
DI void p2_loop(__amdgpu_buffer_rsrc_t ra, __amdgpu_buffer_rsrc_t rw, int sa, int sw, int vo, f32x4 (&acc)[4][5]) {
  u32x4 a[3][4], wv[3][5];
  asm volatile("" : "+s"(sa), "+s"(sw));
  auto ld = [&](int s, int slot) {
#pragma unroll
    for (int T = 0; T < 4; ++T) a[slot][T] = __builtin_amdgcn_raw_buffer_load_b128(ra, vo + T * 1024, sa + s * 4096, 0);
#pragma unroll
    for (int c = 0; c < 4; ++c) wv[slot][c] = __builtin_amdgcn_raw_buffer_load_b128(rw, vo + c * 1024, sw + s * 5120, 0);
    wv[slot][4] = __builtin_amdgcn_raw_buffer_load_b128(rw, vo, sw + s * 5120 + 4096, 0);
  };
  ld(0, 0); ld(1, 1);
  __builtin_amdgcn_sched_barrier(0);
#pragma unroll
  for (int s = 0; s < N; ++s) {
    if (s + 2 < N) ld(s + 2, (s + 2) % 3);
    __builtin_amdgcn_sched_barrier(0);
#pragma unroll
    for (int T = 0; T < 4; ++T)
#pragma unroll
      for (int c = 0; c < 5; ++c) acc[T][c] = mfma16(wv[s % 3][c], a[s % 3][T], acc[T][c]);
    __builtin_amdgcn_sched_barrier(0);
  }
}

__global__ __launch_bounds__(512, 2) void k2(Params p) {
  extern __shared__ __attribute__((aligned(16))) char smem[];
  const int b = blockIdx.x, xcd = b & 7, i = b >> 3;
  const int mt64 = (xcd >> 1) * 8 + (i & 7), nt = (xcd & 1) * 4 + (i >> 3);
  const int tid = threadIdx.x, lane = tid & 63, w = __builtin_amdgcn_readfirstlane(tid >> 6);
  f32x4 acc[4][5];
#pragma unroll
  for (int T = 0; T < 4; ++T)
#pragma unroll
    for (int c = 0; c < 5; ++c) acc[T][c] = f32x4{0.f, 0.f, 0.f, 0.f};
  const int s0 = w < 5 ? 11 * w : (w == 5 ? 54 : (w == 6 ? 68 : 81));
  const __amdgpu_buffer_rsrc_t ra = __builtin_amdgcn_make_buffer_rsrc((void*)(p.ws + WS_ACT), 0, (int)ACT_BYTES, RSRC3);
  const __amdgpu_buffer_rsrc_t rw = __builtin_amdgcn_make_buffer_rsrc((void*)(p.ws + WS_W2T), 0, (int)W2T_BYTES, RSRC3);
  const int sa = (mt64 * NSTEP + s0) * 4096, sw = (nt * NSTEP + s0) * 5120, vo = lane * 16;
  if (w < 4) p2_loop<11>(ra, rw, sa, sw, vo, acc);
  else if (w == 4) p2_loop<10>(ra, rw, sa, sw, vo, acc);
  else if (w == 5) p2_loop<14>(ra, rw, sa, sw, vo, acc);
  else p2_loop<13>(ra, rw, sa, sw, vo, acc);
#pragma unroll
  for (int T = 0; T < 4; ++T)
#pragma unroll
    for (int c = 0; c < 5; ++c) *(f32x4*)(smem + w * 20480 + (T * 5 + c) * 1024 + lane * 16) = acc[T][c];
  __syncthreads();
  for (int item = tid; item < 1280; item += 512) {
    const int tok = item / 20, dg = item % 20, c = dg >> 2, g = dg & 3, T = tok >> 4, rr = tok & 15;
    const char* base = smem + (T * 5 + c) * 1024 + (g * 16 + rr) * 16;
    f32x4 o = *(const f32x4*)(base);
#pragma unroll
    for (int k = 1; k < 5; ++k) o += *(const f32x4*)(base + k * 20480);
    f32x4 y = *(const f32x4*)(base + 5 * 20480);
    y += *(const f32x4*)(base + 6 * 20480);
    y += *(const f32x4*)(base + 7 * 20480);
    const int gt = mt64 * 64 + tok, d0 = nt * 80 + dg * 4;
    const f32x2 st = *(const f32x2*)(p.ws + WS_STATS + (size_t)gt * 8);
    const u32x2 xv = *(const u32x2*)(p.x + (size_t)gt * D + d0);
    const u32x2 dv = *(const u32x2*)(p.dsk + d0);
    const float xs[4] = {__uint_as_float(xv.x << 16), __uint_as_float(xv.x & 0xFFFF0000u), __uint_as_float(xv.y << 16), __uint_as_float(xv.y & 0xFFFF0000u)};
    const float ds[4] = {__uint_as_float(dv.x << 16), __uint_as_float(dv.x & 0xFFFF0000u), __uint_as_float(dv.y << 16), __uint_as_float(dv.y & 0xFFFF0000u)};
    f32x4 res;
#pragma unroll
    for (int e = 0; e < 4; ++e) {
      const float xn = (xs[e] - st.x) * st.y;
      const float yy = fmaf(2.0f, y[e], xn * ds[e]);
      res[e] = fmaf(yy, silu_f(o[e]), xs[e]);
    }
    *(f32x4*)(p.out + (size_t)gt * D + d0) = res;
  }
}
}

extern "C" void kernel_launch(void* const* d_in, const int* in_sizes, int n_in, void* d_out, int out_size, void* d_ws, size_t ws_size, hipStream_t stream) {
  using namespace s5;
  Params p;
  p.x = (const u16*)d_in[0]; p.lre = (const u16*)d_in[1]; p.lim = (const u16*)d_in[2];
  p.bre = (const u16*)d_in[3]; p.bim = (const u16*)d_in[4]; p.cre = (const u16*)d_in[5]; p.cim = (const u16*)d_in[6];
  p.ldt = (const u16*)d_in[7]; p.dsk = (const u16*)d_in[8];
  p.w1 = (const u16*)d_in[10]; p.w3 = (const u16*)d_in[11]; p.w2 = (const u16*)d_in[12];
  p.out = (float*)d_out; p.ws = (char*)d_ws;
  (void)hipFuncSetAttribute((const void*)k1, hipFuncAttributeMaxDynamicSharedMemorySize, LDS1_BYTES);
  (void)hipFuncSetAttribute((const void*)k2, hipFuncAttributeMaxDynamicSharedMemorySize, 163840);
  k1<<<256, 512, LDS1_BYTES, stream>>>(p);
  k2<<<256, 512, 163840, stream>>>(p);
}
```

```cpp
#include <hip/hip_runtime.h>
#include <stdint.h>
#include <type_traits>

namespace s5 {
typedef unsigned short u16;
typedef short bf16x8 __attribute__((ext_vector_type(8)));
typedef float f32x4 __attribute__((ext_vector_type(4)));
typedef float f32x2 __attribute__((ext_vector_type(2)));
typedef unsigned u32x4 __attribute__((ext_vector_type(4)));
typedef unsigned u32x2 __attribute__((ext_vector_type(2)));
typedef __bf16 bf16x2 __attribute__((ext_vector_type(2)));
typedef __attribute__((address_space(3))) void* lds_vp;
typedef __attribute__((address_space(3))) char* lds_cp;
typedef __attribute__((address_space(1))) unsigned long long gu64;
typedef __attribute__((address_space(1))) unsigned gu32;
#define DI __device__ __forceinline__
#define RLX_AGENT __ATOMIC_RELAXED, __HIP_MEMORY_SCOPE_AGENT

constexpr int L = 2048, D = 640, P = 640, F = 1728;
constexpr int NSTEP = 94, NSTEP_F = 54;
constexpr size_t WS_EPOCH = 0;
constexpr size_t WS_FLAGW = 1024;
constexpr size_t WS_FLAGA = 4096;
constexpr size_t WS_GRAN = 8192;
constexpr size_t WS_STATS = WS_GRAN + 7 * 10 * 1024 + 2048;
constexpr size_t WS_ACT = 262144;
constexpr size_t ACT_BYTES = (size_t)32 * NSTEP * 4 * 1024;
constexpr size_t WS_W2T = WS_ACT + ACT_BYTES;
constexpr size_t W2T_BYTES = (size_t)8 * NSTEP * 5 * 1024;
constexpr size_t WS_STAMP = 32u << 20;
static_assert(WS_STATS + 2048 * 16 <= WS_ACT, "ws map");
constexpr int XSLOT = 32768, WSLOT = 20480, SLOT = XSLOT + WSLOT;
constexpr int LDS_PAR = 3 * SLOT;
constexpr int LDS1_BYTES = 163840;
constexpr int LDS_CS = 0;
constexpr int LDS_ST = 4096;
constexpr int LDS_ET = 8192;
constexpr int LDS_TP = 16384;
constexpr int LDS_RT = 2 * SLOT;
static_assert(LDS1_BYTES <= 163840, "lds");
constexpr int RSRC3 = 0x00020000;

struct Params {
  const u16 *x, *lre, *lim, *bre, *bim, *cre, *cim, *ldt, *dsk, *w1, *w3, *w2;
  float* out;
  char* ws;
};

DI float bf2f(u16 v) { return __uint_as_float(((unsigned)v) << 16); }
DI float rbf(float f) { unsigned u = __float_as_uint(f); u += 0x7FFFu + ((u >> 16) & 1u); return __uint_as_float(u & 0xFFFF0000u); }
DI unsigned pack2(float a, float b) { f32x2 p = {a, b}; return __builtin_bit_cast(unsigned, __builtin_convertvector(p, bf16x2)); }
DI f32x4 mfma16(u32x4 a, u32x4 b, f32x4 c) { return __builtin_amdgcn_mfma_f32_16x16x32_bf16(__builtin_bit_cast(bf16x8, a), __builtin_bit_cast(bf16x8, b), c, 0, 0, 0); }
template <int OFF> DI u32x2 trrd(unsigned addr) { u32x2 r; asm volatile("ds_read_b64_tr_b16 %0, %1 offset:%2" : "=v"(r) : "v"(addr), "i"(OFF) : "memory"); return r; }
template <int OFF> DI u32x4 rd128(unsigned addr) { u32x4 r; asm volatile("ds_read_b128 %0, %1 offset:%2" : "=v"(r) : "v"(addr), "i"(OFF) : "memory"); return r; }
DI u32x4 cat(u32x2 a, u32x2 b) { return u32x4{a.x, a.y, b.x, b.y}; }
DI void dma16(__amdgpu_buffer_rsrc_t rs, char* lds, int voff, int soff) { __builtin_amdgcn_raw_ptr_buffer_load_lds(rs, (lds_vp)lds, 16, voff, soff, 0, 0); }
#define SB() __builtin_amdgcn_sched_barrier(0)
#define WAIT_V(n) asm volatile("s_waitcnt vmcnt(" #n ")" ::: "memory")
#define WAIT_L0() asm volatile("s_waitcnt lgkmcnt(0)" ::: "memory")
#define BAR() __builtin_amdgcn_s_barrier()
#define LBAR() __builtin_amdgcn_s_barrier()
#define TS(i) do {} while (0)
DI float wave_sum(float v) {
  v += __builtin_bit_cast(float, __builtin_amdgcn_update_dpp(0, __builtin_bit_cast(int, v), 0x111, 0xf, 0xf, true));
  v += __builtin_bit_cast(float, __builtin_amdgcn_update_dpp(0, __builtin_bit_cast(int, v), 0x112, 0xf, 0xf, true));
  v += __builtin_bit_cast(float, __builtin_amdgcn_update_dpp(0, __builtin_bit_cast(int, v), 0x114, 0xf, 0xf, true));
  v += __builtin_bit_cast(float, __builtin_amdgcn_update_dpp(0, __builtin_bit_cast(int, v), 0x118, 0xf, 0xf, true));
  const int iv = __builtin_bit_cast(int, v);
  const float a = __builtin_bit_cast(float, __builtin_amdgcn_readlane(iv, 15)), b = __builtin_bit_cast(float, __builtin_amdgcn_readlane(iv, 31));
  const float c = __builtin_bit_cast(float, __builtin_amdgcn_readlane(iv, 47)), d = __builtin_bit_cast(float, __builtin_amdgcn_readlane(iv, 63));
  return (a + b) + (c + d);
}
DI float silu_f(float g) { return g * __builtin_amdgcn_rcpf(1.0f + __builtin_amdgcn_exp2f(g * -1.44269504088896f)); }

template <int MODE> struct Frags {
  static constexpr int NT = MODE == 0 ? 6 : 4;
  u32x4 x[4];
  u32x2 lo[MODE == 2 ? 1 : NT], hi[MODE == 2 ? 1 : NT];
  u32x4 v[MODE == 2 ? NT : 1];
  template <int KK, int C> DI void rdw(unsigned aw) {
    if constexpr (MODE == 2) v[C] = rd128<C * 2048>(aw);
    else { constexpr int O = KK * 10240 + (C & 1) * 2560 + ((MODE == 0 ? 0 : 3) + (C >> 1)) * 32; lo[C] = trrd<O>(aw); hi[C] = trrd<O + 1280>(aw); }
  }
  template <int KK, int C> DI void rdw_lo(unsigned aw) {
    if constexpr (MODE == 2) v[C] = rd128<C * 2048>(aw);
    else { constexpr int O = KK * 10240 + (C & 1) * 2560 + ((MODE == 0 ? 0 : 3) + (C >> 1)) * 32; lo[C] = trrd<O>(aw); }
  }
  template <int KK, int C> DI void rdw_hi(unsigned aw) {
    if constexpr (MODE != 2) { constexpr int O = KK * 10240 + (C & 1) * 2560 + ((MODE == 0 ? 0 : 3) + (C >> 1)) * 32; hi[C] = trrd<O + 1280>(aw); }
  }
  template <int T> DI void rdx(unsigned ax) { x[T] = rd128<(MODE == 2 ? 512 : 2048) * T>(ax); }
  DI u32x4 w(int c) const { if constexpr (MODE == 2) return v[c]; else return cat(lo[c], hi[c]); }
  DI void wait() {
    if constexpr (MODE == 0) asm volatile("s_waitcnt lgkmcnt(0)" : "+v"(x[0]), "+v"(x[1]), "+v"(x[2]), "+v"(x[3]), "+v"(lo[0]), "+v"(hi[0]), "+v"(lo[1]), "+v"(hi[1]), "+v"(lo[2]), "+v"(hi[2]),
                                          "+v"(lo[3 % NT]), "+v"(hi[3 % NT]), "+v"(lo[4 % NT]), "+v"(hi[4 % NT]), "+v"(lo[5 % NT]), "+v"(hi[5 % NT]) :: "memory");
    else if constexpr (MODE == 1) asm volatile("s_waitcnt lgkmcnt(0)" : "+v"(x[0]), "+v"(x[1]), "+v"(x[2]), "+v"(x[3]), "+v"(lo[0]), "+v"(hi[0]), "+v"(lo[1]), "+v"(hi[1]), "+v"(lo[2]), "+v"(hi[2]), "+v"(lo[3]), "+v"(hi[3]) :: "memory");
    else asm volatile("s_waitcnt lgkmcnt(0)" : "+v"(x[0]), "+v"(x[1]), "+v"(x[2]), "+v"(x[3]), "+v"(v[0]), "+v"(v[1 % NT]), "+v"(v[2 % NT]), "+v"(v[3 % NT]) :: "memory");
  }
};

struct Ctx {
  __amdgpu_buffer_rsrc_t rx, rw;
  int vx[4];
  int vw[3];
  int xso_base, wso_base, wso_step;
  int rot;
  char* smem;
  int xdst, wdst;
};

template <int MODE, class H0, class H7, class H9>
DI void main_loop(const Params& p, const Ctx& cx, unsigned lbase, int w, int lane, f32x4 (&acc)[4][Frags<MODE>::NT], f32x4& csa, H0 hook0, H7 hook7, H9 hook9) {
  constexpr bool SSM = MODE == 2;
  constexpr int NT = Frags<MODE>::NT;
  const int wr = w & 3, wc = w >> 2;
  const int G = lane >> 4, i16 = lane & 15;
  unsigned ax[2], aw[2];
  if (SSM) {
    const int row = 64 * wr + 16 * (i16 >> 2) + (i16 & 3), f = ((i16 & 3) >> 1) | ((i16 >> 2) << 1);
#pragma unroll
    for (int kk = 0; kk < 2; ++kk) ax[kk] = lbase + row * 128 + (((4 * kk + G) ^ f) << 4);
    const int f2 = (i16 >> 1) & 7;
#pragma unroll
    for (int kk = 0; kk < 2; ++kk) aw[kk] = lbase + XSLOT + (64 * wc + i16) * 128 + (((4 * kk + G) ^ f2) << 4);
  } else {
    const int row = 64 * wr + i16, f = (i16 >> 1) & 7;
#pragma unroll
    for (int kk = 0; kk < 2; ++kk) ax[kk] = lbase + row * 128 + (((4 * kk + G) ^ f) << 4);
    const int q = (lane >> 2) & 3, pp = lane & 3;
    aw[0] = aw[1] = lbase + XSLOT + (G >> 1) * 5120 + (4 * (G & 1) + q) * 160 + pp * 8;
  }
  auto kchunk = [&](int s) { int k = s + cx.rot; return k >= 10 ? k - 10 : k; };
  auto dma = [&](int slot, int sox, int sow, int i) {
    char* b = cx.smem + slot;
    if (i < 4) dma16(cx.rx, b + cx.xdst + i * 1024, cx.vx[i], sox + i * 10240);
    else if (SSM) { if (i < 6) dma16(cx.rw, b + XSLOT + cx.wdst + (i - 4) * 1024, cx.vw[i - 4], sow + (i - 4) * 10240); }
    else if (i < 6) dma16(cx.rw, b + XSLOT + cx.wdst + (i - 4) * 1024, cx.vw[i - 4], sow);
    else { if (lane < 32) dma16(cx.rw, b + XSLOT + cx.wdst + 2048, cx.vw[2], sow); }
  };
  Frags<MODE> fa, fb;
  fa.template rdx<0>(ax[0]); fa.template rdx<1>(ax[0]); fa.template rdx<2>(ax[0]); fa.template rdx<3>(ax[0]);
  fa.template rdw<0, 0>(aw[0]); fa.template rdw<0, 1>(aw[0]); fa.template rdw<0, 2>(aw[0]); fa.template rdw<0, 3>(aw[0]);
  if constexpr (NT > 4) { fa.template rdw<0, 4 % NT>(aw[0]); fa.template rdw<0, 5 % NT>(aw[0]); }

#define MM1(F_, C, T) acc[T][C] = SSM ? mfma16(F_.x[T], F_.w(C), acc[T][C]) : mfma16(F_.w(C), F_.x[T], acc[T][C])
#define MMG(F_, C) do { MM1(F_, C, 0); MM1(F_, C, 1); MM1(F_, C, 2); MM1(F_, C, 3); } while (0)
#define NREADS (SSM ? 8 : 4 + 2 * NT)
#define RD(N_, KK, I, WA, XA) do { \
    if constexpr ((I) < 4) N_.template rdx<(I) & 3>(XA); \
    else if constexpr (SSM) { if constexpr ((I) < 8) N_.template rdw_lo<KK, ((I) - 4) & 3>(WA); } \
    else if constexpr ((I) < 4 + 2 * NT) { if constexpr (((I) & 1) == 0) N_.template rdw_lo<KK, (((I) - 4) >> 1) % NT>(WA); else N_.template rdw_hi<KK, (((I) - 4) >> 1) % NT>(WA); } \
  } while (0)
#define GRP(F_, N_, KK, C, R0, WA, XA, EXTRA) do { \
    MM1(F_, C, 0); RD(N_, KK, (R0), WA, XA); SB(); \
    MM1(F_, C, 1); RD(N_, KK, (R0) + 1, WA, XA); SB(); \
    MM1(F_, C, 2); RD(N_, KK, (R0) + 2, WA, XA); SB(); \
    MM1(F_, C, 3); RD(N_, KK, (R0) + 3, WA, XA); EXTRA; SB(); } while (0)
#define CSEL1(F_, C) do { const unsigned v_ = (io_ == (C)) ? 0x3F803F80u : 0u; const u32x4 s_ = {v_, v_, v_, v_}; csa = SSM ? mfma16(s_, F_.w(C), csa) : mfma16(F_.w(C), s_, csa); } while (0)
#define CSELALL(F_, M) do { if (M) { int io_ = i16; asm volatile("" : "+v"(io_)); CSEL1(F_, 0); CSEL1(F_, 1); CSEL1(F_, 2); CSEL1(F_, 3); if constexpr (NT > 4) { CSEL1(F_, 4 % NT); CSEL1(F_, 5 % NT); } } SB(); } while (0)
  int slot = 0;
  auto stage = [&](const int s, auto dx_t, auto dw_t, auto bnd_t) __attribute__((always_inline)) {
    constexpr bool DX = decltype(dx_t)::value, DW = decltype(dw_t)::value;
    constexpr int BND = decltype(bnd_t)::value;
    const int nslot = slot == 2 * SLOT ? 0 : slot + SLOT;
    int sox = 0, sow = 0, pows = 0, pslot = 0;
    if constexpr (DX) { sox = cx.xso_base + kchunk(s + 3) * 128; asm volatile("" : "+s"(sox)); }
    if constexpr (DW) { pows = cx.wso_base + kchunk(s + 2) * cx.wso_step; pslot = nslot == 2 * SLOT ? 0 : nslot + SLOT; asm volatile("" : "+s"(pows), "+s"(pslot)); }
    fa.wait(); SB();
    {
      const unsigned X1 = ax[1] + slot, W1 = aw[1] + slot;
      GRP(fa, fb, 1, 0, 0, W1, X1, if constexpr (DW) dma(pslot, 0, pows, 4));
      GRP(fa, fb, 1, 1, 4, W1, X1, if constexpr (DW) dma(pslot, 0, pows, 5));
      GRP(fa, fb, 1, 2, 8, W1, X1, if constexpr (!SSM && DW) dma(pslot, 0, pows, 6));
      GRP(fa, fb, 1, 3, 12, W1, X1, );
      if constexpr (NT > 4) {
        GRP(fa, fb, 1, 4 % NT, 16, W1, X1, );
        GRP(fa, fb, 1, 5 % NT, 20, W1, X1, );
      }
      CSELALL(fa, ((2 * s) & 3) == wr);
      if constexpr (BND == 0) hook9();
    }
    fb.wait(); SB();
    const unsigned X0 = ax[0] + nslot, W0 = aw[0] + nslot;
    MMG(fb, 0); SB();
    if constexpr (BND == 1) { if (SSM) WAIT_V(6); else WAIT_V(7); LBAR(); }
    if constexpr (BND == 2) { WAIT_V(0); LBAR(); }
    GRP(fb, fa, 0, 1, 0, W0, X0, if constexpr (DX) dma(slot, sox, sow, 0));
    GRP(fb, fa, 0, 2, 4, W0, X0, if constexpr (DX) dma(slot, sox, sow, 1));
    GRP(fb, fa, 0, 3, 8, W0, X0, if constexpr (DX) dma(slot, sox, sow, 2));
    if constexpr (NT > 4) {
      GRP(fb, fa, 0, 4 % NT, 12, W0, X0, if constexpr (DX) dma(slot, sox, sow, 3));
      GRP(fb, fa, 0, 5 % NT, 16, W0, X0, );
    } else { if constexpr (DX) dma(slot, sox, sow, 3); }
    CSELALL(fb, ((2 * s + 1) & 3) == wr);
    slot = nslot;
  };
  using T_ = std::true_type; using F_ = std::false_type;
  using I0 = std::integral_constant<int, 0>; using I1 = std::integral_constant<int, 1>; using I2 = std::integral_constant<int, 2>;
  stage(0, T_{}, F_{}, I1{});
  hook0();
#pragma unroll 1
  for (int s = 1; s < 7; ++s) { stage(s, T_{}, T_{}, I1{}); if (s == 2) hook7(); }
  stage(7, F_{}, T_{}, I1{});
  stage(8, F_{}, F_{}, I2{});
  stage(9, F_{}, F_{}, I0{});
  fa.wait();
}

template <bool SSM>
DI unsigned phase1(const Params& p, char* smem, int mt, int cj  , int blk, int rot) {
  const int tid = threadIdx.x, lane = tid & 63, w = __builtin_amdgcn_readfirstlane(tid >> 6);
  const int wr = w & 3, wc = w >> 2, G = lane >> 4, i16 = lane & 15;
  const unsigned lbase = (unsigned)(size_t)(lds_cp)smem;
  TS(0);
  const int cbi = SSM ? 22 + cj : cj;
  u32x4 sr0, sr1; unsigned etag;
  {
    const __amdgpu_buffer_rsrc_t rs = __builtin_amdgcn_make_buffer_rsrc((void*)(p.x + (size_t)(256 * mt + 8 * cbi + w) * D), 0, D * 2, RSRC3);
    const int vo = lane * 16;
    asm volatile("buffer_load_dwordx4 %0, %3, %4, 0 offen\n\tbuffer_load_dwordx4 %1, %3, %4, 0 offen offset:1024\n\tglobal_load_dword %2, %5, off sc1"
                 : "=&v"(sr0), "=&v"(sr1), "=&v"(etag) : "v"(vo), "s"(rs), "v"(p.ws + WS_EPOCH) : "memory");
  }
  unsigned pa_ = 0, pb_ = 0, pc_ = 0;
  if (SSM) {
    const int ch = 64 * cj + lane;
    asm volatile("global_load_ushort %0, %3, off\n\tglobal_load_ushort %1, %4, off\n\tglobal_load_ushort %2, %5, off"
                 : "=&v"(pa_), "=&v"(pb_), "=&v"(pc_) : "v"(p.lre + ch), "v"(p.lim + ch), "v"(p.ldt + ch) : "memory");
  }
  Ctx cx;
  cx.smem = smem; cx.rot = rot;
  cx.rx = __builtin_amdgcn_make_buffer_rsrc((void*)(p.x + (size_t)(256 * mt + 32 * w) * D), 0, 32 * D * 2, RSRC3);
#pragma unroll
  for (int i = 0; i < 4; ++i) {
    int f;
    if (SSM) f = ((lane >> 4) & 1) | ((((2 * w + (i >> 1)) & 3)) << 1);
    else f = ((i & 1) << 2) | (lane >> 4);
    cx.vx[i] = (lane >> 3) * 1280 + (((lane & 7) ^ f) << 4);
  }
  cx.xso_base = 0; cx.xdst = w * 4096;
  if (SSM) {
    const int c = w & 3;
    cx.rw = __builtin_amdgcn_make_buffer_rsrc((void*)((c & 1) ? p.bim : p.bre), 0, P * D * 2, RSRC3);
    cx.vw[0] = (lane >> 3) * 1280 + (((lane & 7) ^ (lane >> 4)) << 4); cx.vw[1] = cx.vw[0] ^ 64; cx.vw[2] = 0;
    cx.wso_base = (64 * cj + 32 * (w >> 2) + 16 * (c >> 1)) * 1280; cx.wso_step = 128; cx.wdst = w * 2048;
  } else {
    const int u = w & 1;
    cx.rw = __builtin_amdgcn_make_buffer_rsrc((void*)(u ? p.w3 : p.w1), 0, D * F * 2, RSRC3);
#pragma unroll
    for (int i = 0; i < 3; ++i) {
      const int n = 64 * i + lane, pos = (n / 10) & 15, row = (pos & 3) | ((pos & 4) << 1) | ((pos & 8) >> 1);
      cx.vw[i] = row * 3456 + (n % 10) * 16 + cj * 160;
    }
    cx.wso_base = 16 * (w >> 1) * 3456; cx.wso_step = 64 * 3456; cx.wdst = (w >> 1) * 5120 + (w & 1) * 2560;
  }
  auto kchunk = [&](int s) { int k = s + rot; return k >= 10 ? k - 10 : k; };
  auto issue_stage = [&](int s, int slot) {
    const int sox = kchunk(s) * 128, sow = cx.wso_base + kchunk(s) * cx.wso_step;
    char* b = smem + slot;
#pragma unroll
    for (int i = 0; i < 4; ++i) dma16(cx.rx, b + cx.xdst + i * 1024, cx.vx[i], sox + i * 10240);
    dma16(cx.rw, b + XSLOT + cx.wdst, cx.vw[0], sow);
    dma16(cx.rw, b + XSLOT + cx.wdst + 1024, cx.vw[1], SSM ? sow + 10240 : sow);
    if (!SSM) { if (lane < 32) dma16(cx.rw, b + XSLOT + cx.wdst + 2048, cx.vw[2], sow); }
  };
  issue_stage(0, 0);
  TS(6);
  BAR();
  const int unit = w * 256 + blk;
  const bool has_unit = (w < 4) && (unit < 940);
  char* rt = smem + LDS_RT + (w & 3) * 4096;
  if (has_unit) {
    if (unit < 540) {
      const int s = unit / 10, dq = unit % 10;
      const __amdgpu_buffer_rsrc_t r2 = __builtin_amdgcn_make_buffer_rsrc((void*)p.w2, 0, F * D * 2, RSRC3);
      const int vo = (lane >> 3) * 1280 + (lane & 7) * 16;
#pragma unroll
      for (int i = 0; i < 4; ++i) dma16(r2, rt + i * 1024, vo, (32 * s + 8 * i) * 1280 + dq * 128);
    } else {
      const int u = unit - 540, c = u / 10, v = u % 10;
      const int vo = (lane & 15) * 1280 + (lane >> 4) * 16;
#pragma unroll
      for (int i = 0; i < 4; ++i) {
        const int sp = 4 * v + i, jg = sp >> 1, part = sp & 1;
        const __amdgpu_buffer_rsrc_t rc = __builtin_amdgcn_make_buffer_rsrc((void*)(part ? p.cim : p.cre), 0, D * P * 2, RSRC3);
        dma16(rc, rt + i * 1024, vo, 16 * c * 1280 + jg * 64);
      }
    }
  }
  issue_stage(1, SLOT);
  if (SSM) WAIT_V(6); else WAIT_V(7);
  TS(13);
  const __amdgpu_buffer_rsrc_t rW2T = __builtin_amdgcn_make_buffer_rsrc((void*)(p.ws + WS_W2T), 0, (int)W2T_BYTES, RSRC3);
  {
    u32x4 vv[4];
    const unsigned rtl = lbase + LDS_RT + (w & 3) * 4096;
    if (has_unit) {
      if (unit < 540) {
        const int g = lane >> 4, q = (lane >> 2) & 3, pp = lane & 3;
        const unsigned a0 = rtl + (8 * g + q) * 128 + 8 * pp;
        u32x2 l0 = trrd<0>(a0), h0 = trrd<512>(a0), l1 = trrd<32>(a0), h1 = trrd<512 + 32>(a0), l2 = trrd<64>(a0), h2 = trrd<512 + 64>(a0), l3 = trrd<96>(a0), h3 = trrd<512 + 96>(a0);
        asm volatile("s_waitcnt lgkmcnt(0)" : "+v"(l0), "+v"(h0), "+v"(l1), "+v"(h1), "+v"(l2), "+v"(h2), "+v"(l3), "+v"(h3) :: "memory");
        vv[0] = cat(l0, h0); vv[1] = cat(l1, h1); vv[2] = cat(l2, h2); vv[3] = cat(l3, h3);
      } else {
        u32x4 v0 = rd128<0>(rtl + lane * 16), v1 = rd128<1024>(rtl + lane * 16), v2 = rd128<2048>(rtl + lane * 16), v3 = rd128<3072>(rtl + lane * 16);
        asm volatile("s_waitcnt lgkmcnt(0)" : "+v"(v0), "+v"(v1), "+v"(v2), "+v"(v3) :: "memory");
        vv[0] = v0; vv[1] = v1; vv[2] = v2; vv[3] = v3;
      }
    }
    BAR();
    issue_stage(2, 2 * SLOT);
    if (has_unit) {
#pragma unroll
      for (int i = 0; i < 4; ++i) {
        int c, s;
        if (unit < 540) { s = unit / 10; c = 4 * (unit % 10) + i; } else { const int u = unit - 540; c = u / 10; s = NSTEP_F + 4 * (u % 10) + i; }
        __builtin_amdgcn_raw_buffer_store_b128(vv[i], rW2T, lane * 16, (((c / 5) * NSTEP + s) * 5 + (c % 5)) * 1024, 16);
      }
    }
  }
  TS(1);
  auto hook0 = [&]() {
    asm volatile("" : "+v"(sr0), "+v"(sr1), "+v"(etag), "+v"(pa_), "+v"(pb_), "+v"(pc_));
    etag += 1u;
    float v[16];
#pragma unroll
    for (int i = 0; i < 4; ++i) { v[2 * i] = __uint_as_float(sr0[i] << 16); v[2 * i + 1] = __uint_as_float(sr0[i] & 0xFFFF0000u); v[8 + 2 * i] = __uint_as_float(sr1[i] << 16); v[9 + 2 * i] = __uint_as_float(sr1[i] & 0xFFFF0000u); }
    float sum = 0.f;
#pragma unroll
    for (int i = 0; i < 16; ++i) sum += v[i];
    sum = wave_sum(sum);
    const float mean = sum * (1.0f / D);
    float sq = 0.f;
#pragma unroll
    for (int i = 0; i < 8; ++i) { const float d = v[i] - mean; sq = fmaf(d, d, sq); }
    float sq2 = 0.f;
#pragma unroll
    for (int i = 8; i < 16; ++i) { const float d = v[i] - mean; sq2 = fmaf(d, d, sq2); }
    sq += lane < 16 ? sq2 : 0.f;
    sq = wave_sum(sq);
    const float rstd = 1.0f / sqrtf(sq * (1.0f / D) + 1e-5f);
    if (lane == 0) {
      gu64* g = (gu64*)(p.ws + WS_STATS + (size_t)(256 * mt + 8 * cbi + w) * 16);
      __hip_atomic_store(g, ((unsigned long long)etag << 32) | __float_as_uint(mean), RLX_AGENT);
      __hip_atomic_store(g + 1, ((unsigned long long)etag << 32) | __float_as_uint(rstd), RLX_AGENT);
    }
    if (SSM && w == 5) {
      const float a = bf2f((u16)pa_), b = bf2f((u16)pb_);
      const float dt = rbf(expf(bf2f((u16)pc_)));
      const float er = expf(dt * a);
      float sn, cn; sincosf(dt * b, &sn, &cn);
      const float lr = er * cn, li = er * sn;
      const float nr = lr - 1.0f, ni = li, den = a * a + b * b;
      const float cr = (nr * a + ni * b) / den, ci = (ni * a - nr * b) / den;
      float pr = lr, pi = li;
#pragma unroll
      for (int i = 0; i < 4; ++i) { float t = pr * pr - pi * pi; pi = 2.f * pr * pi; pr = t; }
      const float l16r = pr, l16i = pi;
#pragma unroll
      for (int i = 0; i < 4; ++i) { float t = pr * pr - pi * pi; pi = 2.f * pr * pi; pr = t; }
      float* par = (float*)(smem + LDS_PAR) + lane * 8;
      *(f32x4*)par = f32x4{lr, li, cr, ci};
      *(f32x4*)(par + 4) = f32x4{l16r, l16i, pr, pi};
    }
  };
  auto hook7 = [&]() { if (tid == 0) __hip_atomic_store((gu32*)(p.ws + WS_FLAGW) + blk, etag, RLX_AGENT); };
  const __amdgpu_buffer_rsrc_t rACT = __builtin_amdgcn_make_buffer_rsrc((void*)(p.ws + WS_ACT), 0, (int)ACT_BYTES, RSRC3);
  const __amdgpu_buffer_rsrc_t rFW = __builtin_amdgcn_make_buffer_rsrc((void*)(p.ws + WS_FLAGW), 0, 1024, RSRC3);
  const int mt64 = 4 * mt + wr;
  gu64* stg = (gu64*)(p.ws + WS_STATS + (size_t)(256 * mt + 64 * wr + lane) * 16);
  unsigned long long sta = 0, stb = 0;
  u32x4 fw = {0u, 0u, 0u, 0u};
  auto hook9 = [&]() {
    sta = __hip_atomic_load(stg, RLX_AGENT); stb = __hip_atomic_load(stg + 1, RLX_AGENT);
    if (w == 0) fw = __builtin_amdgcn_raw_buffer_load_b128(rFW, lane * 16, 0, 16);
  };
  f32x4 csa = {0.f, 0.f, 0.f, 0.f};
  float* cst = (float*)(smem + LDS_CS);
  float* stt = (float*)(smem + LDS_ST);
  auto finish_common = [&](int ntw  , int tile0  ) {
    unsigned spins = 0;
    while (!__all((unsigned)(sta >> 32) == etag && (unsigned)(stb >> 32) == etag)) {
      if (++spins > (1u << 16)) break;
      __builtin_amdgcn_s_sleep(1);
      sta = __hip_atomic_load(stg, RLX_AGENT); stb = __hip_atomic_load(stg + 1, RLX_AGENT);
    }
    if (w == 0) {
      spins = 0;
      while (!__all(fw.x == etag && fw.y == etag && fw.z == etag && fw.w == etag)) {
        if (++spins > (1u << 16)) break;
        __builtin_amdgcn_s_sleep(1);
        fw = __builtin_amdgcn_raw_buffer_load_b128(rFW, lane * 16, 0, 16);
      }
      if (blk == 0 && lane == 0) __hip_atomic_store((gu32*)(p.ws + WS_EPOCH), etag, RLX_AGENT);
    }
    __syncthreads();
    const float mean = __uint_as_float((unsigned)sta), rstd = __uint_as_float((unsigned)stb);
    if (wc == 0) *(f32x2*)(stt + (64 * wr + lane) * 2) = f32x2{rstd, -mean * rstd};
    if (SSM) { if (G == 0) {
#pragma unroll
        for (int c = 0; c < 4; ++c) cst[wr * 160 + (tile0 + c) * 16 + i16] = csa[c]; } }
    else { if (i16 < ntw) *(f32x4*)(cst + wr * 160 + (tile0 + i16) * 16 + 4 * G) = csa; }
    __syncthreads();
    if (tid < 160) cst[4 * 160 + tid] = (cst[tid] + cst[160 + tid]) + (cst[320 + tid] + cst[480 + tid]);
    __syncthreads();
  };
  const float* csf = cst + 4 * 160;

  if (!SSM) {
    auto ffn = [&](auto mode_tag) {
      constexpr int MODE = decltype(mode_tag)::value;
      constexpr int NT = Frags<MODE>::NT, P0 = MODE == 0 ? 0 : 3;
      f32x4 acc[4][NT];
#pragma unroll
      for (int T = 0; T < 4; ++T)
#pragma unroll
        for (int c = 0; c < NT; ++c) acc[T][c] = f32x4{0.f, 0.f, 0.f, 0.f};
      main_loop<MODE>(p, cx, lbase, w, lane, acc, csa, hook0, hook7, hook9);
      TS(2);
      finish_common(NT, 2 * P0);
      f32x2 st4[4];
#pragma unroll
      for (int T = 0; T < 4; ++T) st4[T] = *(const f32x2*)(stt + (64 * wr + 16 * T + i16) * 2);
#pragma unroll
      for (int Pq = 0; Pq < NT / 2; ++Pq) {
        const int ft = 5 * cj + P0 + Pq;
        const f32x4 cg = *(const f32x4*)(csf + (2 * P0 + 2 * Pq) * 16 + 4 * G), cu = *(const f32x4*)(csf + (2 * P0 + 2 * Pq + 1) * 16 + 4 * G);
        unsigned pk[4][2];
#pragma unroll
        for (int T = 0; T < 4; ++T) {
          float hv[4];
#pragma unroll
          for (int e = 0; e < 4; ++e) {
            const float g = fmaf(acc[T][2 * Pq][e], st4[T].x, st4[T].y * cg[e]);
            const float u = fmaf(acc[T][2 * Pq + 1][e], st4[T].x, st4[T].y * cu[e]);
            hv[e] = silu_f(g) * u;
          }
          pk[T][0] = pack2(hv[0], hv[1]); pk[T][1] = pack2(hv[2], hv[3]);
        }
        if (ft < 108) {
#pragma unroll
          for (int T2 = 0; T2 < 4; T2 += 2) {
            const auto a = __builtin_amdgcn_permlane16_swap(pk[T2][0], pk[T2 + 1][0], false, false);
            const auto b = __builtin_amdgcn_permlane16_swap(pk[T2][1], pk[T2 + 1][1], false, false);
            const u32x4 v = {a[0], b[0], a[1], b[1]};
            const int s2 = ft >> 1, gp = 2 * (ft & 1) + (G >> 1), T = T2 + (G & 1);
            __builtin_amdgcn_raw_buffer_store_b128(v, rACT, T * 1024 + (gp * 16 + i16) * 16, (mt64 * NSTEP + s2) * 4096, 16);
          }
        }
      }
    };
    if (wc == 0) ffn(std::integral_constant<int, 0>{}); else ffn(std::integral_constant<int, 1>{});
  } else {
    f32x4 acc[4][4];
#pragma unroll
    for (int T = 0; T < 4; ++T)
#pragma unroll
      for (int c = 0; c < 4; ++c) acc[T][c] = f32x4{0.f, 0.f, 0.f, 0.f};
    main_loop<2>(p, cx, lbase, w, lane, acc, csa, hook0, hook7, hook9);
    TS(2);
    finish_common(4, 4 * wc);
    const float* par = (const float*)(smem + LDS_PAR);
    float lr[2], li[2], cr[2], ci[2];
#pragma unroll
    for (int j = 0; j < 2; ++j) {
      const f32x4 pv = *(const f32x4*)(par + (32 * wc + 16 * j + i16) * 8);
      lr[j] = pv.x; li[j] = pv.y; cr[j] = pv.z; ci[j] = pv.w;
    }
    {
      float csl[4];
#pragma unroll
      for (int c = 0; c < 4; ++c) csl[c] = csf[(4 * wc + c) * 16 + i16];
#pragma unroll
      for (int T = 0; T < 4; ++T)
#pragma unroll
        for (int e = 0; e < 4; e += 2) {
          const f32x4 sv = *(const f32x4*)(stt + (64 * wr + 16 * G + 4 * T + e) * 2);
#pragma unroll
          for (int c = 0; c < 4; ++c) {
            acc[T][c][e] = fmaf(acc[T][c][e], sv.x, sv.y * csl[c]);
            acc[T][c][e + 1] = fmaf(acc[T][c][e + 1], sv.z, sv.w * csl[c]);
          }
        }
    }
    f32x2* et = (f32x2*)(smem + LDS_ET);
    const int seg = 4 * wr + G;
#pragma unroll
    for (int j = 0; j < 2; ++j) {
      float hr = 0.f, hi = 0.f;
#pragma unroll
      for (int i = 0; i < 16; ++i) {
        const float nr = fmaf(lr[j], hr, fmaf(-li[j], hi, acc[i >> 2][2 * j][i & 3]));
        const float ni = fmaf(lr[j], hi, fmaf(li[j], hr, acc[i >> 2][2 * j + 1][i & 3]));
        hr = nr; hi = ni;
      }
      et[seg * 64 + 32 * wc + 16 * j + i16] = f32x2{hr, hi};
    }
    __syncthreads();
    TS(3);
    if (w == 0) {
      const f32x4 pv = *(const f32x4*)(par + lane * 8 + 4);
      const unsigned tag = etag;
      float xr_ = 0.f, xi_ = 0.f;
      f32x2 ev[16];
#pragma unroll
      for (int s = 0; s < 16; ++s) ev[s] = et[s * 64 + lane];
#pragma unroll
      for (int s = 0; s < 16; ++s) {
        const float nr = fmaf(pv.x, xr_, fmaf(-pv.y, xi_, ev[s].x));
        const float ni = fmaf(pv.x, xi_, fmaf(pv.y, xr_, ev[s].y));
        xr_ = nr; xi_ = ni;
      }
      gu64* gran = (gu64*)(p.ws + WS_GRAN);
      if (mt < 7) {
        gu64* g = gran + ((size_t)(mt * 10 + cj) * 64 + lane) * 2;
        __hip_atomic_store(g, ((unsigned long long)tag << 32) | __float_as_uint(xr_), RLX_AGENT);
        __hip_atomic_store(g + 1, ((unsigned long long)tag << 32) | __float_as_uint(xi_), RLX_AGENT);
      }
      float Hr = 0.f, Hi = 0.f;
      if (mt > 0) {
        unsigned long long ga[7], gb[7];
        unsigned spins = 0;
        while (true) {
          bool ok = true;
#pragma unroll
          for (int m = 0; m < 7; ++m) {
            if (m < mt) {
              gu64* g = gran + ((size_t)(m * 10 + cj) * 64 + lane) * 2;
              ga[m] = __hip_atomic_load(g, RLX_AGENT); gb[m] = __hip_atomic_load(g + 1, RLX_AGENT);
            } else { ga[m] = gb[m] = (unsigned long long)tag << 32; }
          }
#pragma unroll
          for (int m = 0; m < 7; ++m) ok = ok && ((unsigned)(ga[m] >> 32) == tag) && ((unsigned)(gb[m] >> 32) == tag);
          if (__all(ok)) break;
          if (++spins > (1u << 16)) break;
          __builtin_amdgcn_s_sleep(1);
        }
#pragma unroll
        for (int m = 0; m < 7; ++m) {
          if (m < mt) {
            const float sr = __uint_as_float((unsigned)ga[m]), si = __uint_as_float((unsigned)gb[m]);
            const float nr = fmaf(pv.z, Hr, fmaf(-pv.w, Hi, sr));
            const float ni = fmaf(pv.z, Hi, fmaf(pv.w, Hr, si));
            Hr = nr; Hi = ni;
          }
        }
      }
      xr_ = Hr; xi_ = Hi;
#pragma unroll
      for (int s = 0; s < 16; ++s) {
        et[s * 64 + lane] = f32x2{xr_, xi_};
        const float nr = fmaf(pv.x, xr_, fmaf(-pv.y, xi_, ev[s].x));
        const float ni = fmaf(pv.x, xi_, fmaf(pv.y, xr_, ev[s].y));
        xr_ = nr; xi_ = ni;
      }
    }
    __syncthreads();
    TS(4);
    char* tp = smem + LDS_TP + w * 9216;
#pragma unroll
    for (int j = 0; j < 2; ++j) {
      const f32x2 x0 = et[seg * 64 + 32 * wc + 16 * j + i16];
      float hr = x0.x, hi = x0.y;
      float ore[16], oim[16];
#pragma unroll
      for (int i = 0; i < 16; ++i) {
        const float nr = fmaf(lr[j], hr, fmaf(-li[j], hi, acc[i >> 2][2 * j][i & 3]));
        const float ni = fmaf(lr[j], hi, fmaf(li[j], hr, acc[i >> 2][2 * j + 1][i & 3]));
        hr = nr; hi = ni;
        ore[i] = cr[j] * hr - ci[j] * hi;
        oim[i] = -(cr[j] * hi + ci[j] * hr);
      }
      char* t0 = tp + (16 * j + i16) * 144 + 32 * G;
      char* t1 = t0 + 4608;
      *(u32x4*)t0 = u32x4{pack2(ore[0], ore[1]), pack2(ore[2], ore[3]), pack2(ore[4], ore[5]), pack2(ore[6], ore[7])};
      *(u32x4*)(t0 + 16) = u32x4{pack2(ore[8], ore[9]), pack2(ore[10], ore[11]), pack2(ore[12], ore[13]), pack2(ore[14], ore[15])};
      *(u32x4*)t1 = u32x4{pack2(oim[0], oim[1]), pack2(oim[2], oim[3]), pack2(oim[4], oim[5]), pack2(oim[6], oim[7])};
      *(u32x4*)(t1 + 16) = u32x4{pack2(oim[8], oim[9]), pack2(oim[10], oim[11]), pack2(oim[12], oim[13]), pack2(oim[14], oim[15])};
    }
    WAIT_L0();
    {
      const int q = (lane >> 2) & 3, pp = lane & 3;
      const unsigned tb = lbase + LDS_TP + w * 9216 + (8 * G + q) * 144 + 8 * pp;
      const int jg = 2 * cj + wc;
#pragma unroll
      for (int part = 0; part < 2; ++part) {
        u32x2 lo[4], hi[4];
        if (part == 0) { lo[0] = trrd<0>(tb); hi[0] = trrd<576>(tb); lo[1] = trrd<32>(tb); hi[1] = trrd<576 + 32>(tb); lo[2] = trrd<64>(tb); hi[2] = trrd<576 + 64>(tb); lo[3] = trrd<96>(tb); hi[3] = trrd<576 + 96>(tb); }
        else { lo[0] = trrd<4608>(tb); hi[0] = trrd<4608 + 576>(tb); lo[1] = trrd<4608 + 32>(tb); hi[1] = trrd<4608 + 576 + 32>(tb); lo[2] = trrd<4608 + 64>(tb); hi[2] = trrd<4608 + 576 + 64>(tb); lo[3] = trrd<4608 + 96>(tb); hi[3] = trrd<4608 + 576 + 96>(tb); }
        asm volatile("s_waitcnt lgkmcnt(0)" : "+v"(lo[0]), "+v"(hi[0]), "+v"(lo[1]), "+v"(hi[1]), "+v"(lo[2]), "+v"(hi[2]), "+v"(lo[3]), "+v"(hi[3]) :: "memory");
        const int s2 = NSTEP_F + 2 * jg + part;
#pragma unroll
        for (int T = 0; T < 4; ++T)
          __builtin_amdgcn_raw_buffer_store_b128(cat(lo[T], hi[T]), rACT, T * 1024 + lane * 16, (mt64 * NSTEP + s2) * 4096, 16);
      }
    }
  }
  TS(5);
  WAIT_V(0);
  __syncthreads();
  if (tid == 0) __hip_atomic_store((gu32*)(p.ws + WS_FLAGA + mt * 128) + cbi, etag, RLX_AGENT);
  return etag;
}

template <int N>
DI void p2_run(const Params& p, __amdgpu_buffer_rsrc_t ra, __amdgpu_buffer_rsrc_t rw, int sa, int sw, int vo, int w, int lane, int mt2, unsigned etag, f32x4 (&acc)[4][5]) {
  constexpr int DW = 5, DA = 3;
  u32x4 a[DA][4], wv[DW][5];
  asm volatile("" : "+s"(sa), "+s"(sw));
  auto ldw = [&](int k, int slot) {
#pragma unroll
    for (int c = 0; c < 4; ++c) wv[slot][c] = __builtin_amdgcn_raw_buffer_load_b128(rw, vo + c * 1024, sw + k * 5120, 16);
    wv[slot][4] = __builtin_amdgcn_raw_buffer_load_b128(rw, vo, sw + k * 5120 + 4096, 16);
  };
  auto lda = [&](int k, int slot) {
#pragma unroll
    for (int T = 0; T < 4; ++T) a[slot][T] = __builtin_amdgcn_raw_buffer_load_b128(ra, vo + T * 1024, sa + k * 4096, 16);
  };
#pragma unroll
  for (int s = 0; s < DW; ++s) ldw(s, s);
  SB();
  {
    const int fi = w < 5 ? (lane < 22 ? lane : 0) : 22 + (lane < 10 ? lane : 0);
    gu32* fl = (gu32*)(p.ws + WS_FLAGA + mt2 * 128) + fi;
    unsigned spins = 0;
    while (!__all(__hip_atomic_load(fl, RLX_AGENT) == etag)) {
      if (++spins > (1u << 16)) break;
      __builtin_amdgcn_s_sleep(1);
    }
  }
  __builtin_amdgcn_fence(__ATOMIC_ACQUIRE, "wavefront");
  TS(9);
#pragma unroll
  for (int s = 0; s < DA - 1; ++s) lda(s, s);
  SB();
#pragma unroll
  for (int s = 0; s < N; ++s) {
    if (s + DA - 1 < N) lda(s + DA - 1, (s + DA - 1) % DA);
    SB();
#pragma unroll
    for (int T = 0; T < 4; ++T)
#pragma unroll
      for (int c = 0; c < 5; ++c) acc[T][c] = mfma16(wv[s % DW][c], a[s % DA][T], acc[T][c]);
    SB();
    if (s + DW < N) ldw(s + DW, s % DW);
    SB();
  }
}

DI void phase2(const Params& p, char* smem, unsigned etag) {
  const int b = blockIdx.x, xcd = b & 7, i = b >> 3;
  const int al = i & 7, bl = i >> 3;
  const int mt64 = (xcd >> 1) * 8 + al, nt = (xcd & 1) * 4 + bl;
  const int tid = threadIdx.x, lane = tid & 63, w = __builtin_amdgcn_readfirstlane(tid >> 6);
  TS(8);
  f32x4 acc[4][5];
#pragma unroll
  for (int T = 0; T < 4; ++T)
#pragma unroll
    for (int c = 0; c < 5; ++c) acc[T][c] = f32x4{0.f, 0.f, 0.f, 0.f};
  const int s0 = w < 5 ? 11 * w : (w == 5 ? 54 : (w == 6 ? 68 : 81));
  const __amdgpu_buffer_rsrc_t ra = __builtin_amdgcn_make_buffer_rsrc((void*)(p.ws + WS_ACT), 0, (int)ACT_BYTES, RSRC3);
  const __amdgpu_buffer_rsrc_t rw = __builtin_amdgcn_make_buffer_rsrc((void*)(p.ws + WS_W2T), 0, (int)W2T_BYTES, RSRC3);
  const int sa = (mt64 * NSTEP + s0) * 4096, sw = (nt * NSTEP + s0) * 5120, vo = lane * 16;
  if (w < 4) p2_run<11>(p, ra, rw, sa, sw, vo, w, lane, mt64 >> 2, etag, acc);
  else if (w == 4) p2_run<10>(p, ra, rw, sa, sw, vo, w, lane, mt64 >> 2, etag, acc);
  else if (w == 5) p2_run<14>(p, ra, rw, sa, sw, vo, w, lane, mt64 >> 2, etag, acc);
  else p2_run<13>(p, ra, rw, sa, sw, vo, w, lane, mt64 >> 2, etag, acc);
  {
    const int g = lane >> 4, r = lane & 15;
    const int pos = g * 16 + ((r + 4 * g) & 15);
#pragma unroll
    for (int T = 0; T < 4; ++T)
#pragma unroll
      for (int c = 0; c < 5; ++c) *(f32x4*)(smem + w * 20480 + (T * 5 + c) * 1024 + pos * 16) = acc[T][c];
  }
  __syncthreads();
  TS(10);
  for (int item = tid; item < 1280; item += 512) {
    const int tok = item / 20, dg = item % 20, c = dg >> 2, g = dg & 3, T = tok >> 4, rr = tok & 15;
    const char* base = smem + (T * 5 + c) * 1024 + (g * 16 + ((rr + 4 * g) & 15)) * 16;
    const int gt = mt64 * 64 + tok, d0 = nt * 80 + dg * 4;
    gu64* sg = (gu64*)(p.ws + WS_STATS + (size_t)gt * 16);
    const unsigned long long ga = __hip_atomic_load(sg, RLX_AGENT), gb = __hip_atomic_load(sg + 1, RLX_AGENT);
    const float mean = __uint_as_float((unsigned)ga), rstd = __uint_as_float((unsigned)gb);
    const u32x2 xv = *(const u32x2*)(p.x + (size_t)gt * D + d0);
    const u32x2 dv = *(const u32x2*)(p.dsk + d0);
    f32x4 o = *(const f32x4*)(base);
#pragma unroll
    for (int k = 1; k < 5; ++k) o += *(const f32x4*)(base + k * 20480);
    f32x4 y = *(const f32x4*)(base + 5 * 20480);
    y += *(const f32x4*)(base + 6 * 20480);
    y += *(const f32x4*)(base + 7 * 20480);
    const float xs[4] = {__uint_as_float(xv.x << 16), __uint_as_float(xv.x & 0xFFFF0000u), __uint_as_float(xv.y << 16), __uint_as_float(xv.y & 0xFFFF0000u)};
    const float ds[4] = {__uint_as_float(dv.x << 16), __uint_as_float(dv.x & 0xFFFF0000u), __uint_as_float(dv.y << 16), __uint_as_float(dv.y & 0xFFFF0000u)};
    f32x4 res;
#pragma unroll
    for (int e = 0; e < 4; ++e) {
      const float xn = (xs[e] - mean) * rstd;
      const float yy = fmaf(2.0f, y[e], xn * ds[e]);
      res[e] = fmaf(yy, silu_f(o[e]), xs[e]);
    }
    *(f32x4*)(p.out + (size_t)gt * D + d0) = res;
  }
  TS(11);
}

__global__ __launch_bounds__(512, 2) void k_fused(Params p) {
  extern __shared__ __attribute__((aligned(16))) char smem[];
  const int b = blockIdx.x, xcd = b & 7, i = b >> 3;
  const int ml = i & 3, cl = i >> 2;
  const int mt = (xcd & 1) * 4 + ml, cb = (xcd >> 1) * 8 + cl;
  const int rot = (cl + 3 * ml) % 10;
  unsigned etag;
  if (cb < 22) etag = phase1<false>(p, smem, mt, cb, b, rot);
  else etag = phase1<true>(p, smem, mt, cb - 22, b, rot);
  phase2(p, smem, etag);
}
}

extern "C" void kernel_launch(void* const* d_in, const int* in_sizes, int n_in, void* d_out, int out_size, void* d_ws, size_t ws_size, hipStream_t stream) {
  using namespace s5;
  Params p;
  p.x = (const u16*)d_in[0]; p.lre = (const u16*)d_in[1]; p.lim = (const u16*)d_in[2];
  p.bre = (const u16*)d_in[3]; p.bim = (const u16*)d_in[4]; p.cre = (const u16*)d_in[5]; p.cim = (const u16*)d_in[6];
  p.ldt = (const u16*)d_in[7]; p.dsk = (const u16*)d_in[8];
  p.w1 = (const u16*)d_in[10]; p.w3 = (const u16*)d_in[11]; p.w2 = (const u16*)d_in[12];
  p.out = (float*)d_out; p.ws = (char*)d_ws;
  (void)hipFuncSetAttribute((const void*)k_fused, hipFuncAttributeMaxDynamicSharedMemorySize, LDS1_BYTES);
  k_fused<<<256, 512, LDS1_BYTES, stream>>>(p);
}
```

```cpp
#include <hip/hip_runtime.h>
#include <stdint.h>
#include <type_traits>

#define OUT_AUX 2
namespace s5 {
typedef unsigned short u16;
typedef short bf16x8 __attribute__((ext_vector_type(8)));
typedef float f32x4 __attribute__((ext_vector_type(4)));
typedef float f32x2 __attribute__((ext_vector_type(2)));
typedef unsigned u32x4 __attribute__((ext_vector_type(4)));
typedef unsigned u32x2 __attribute__((ext_vector_type(2)));
typedef __bf16 bf16x2 __attribute__((ext_vector_type(2)));
typedef __attribute__((address_space(3))) void* lds_vp;
typedef __attribute__((address_space(3))) char* lds_cp;
typedef __attribute__((address_space(1))) unsigned long long gu64;
typedef __attribute__((address_space(1))) unsigned gu32;
#define DI __device__ __forceinline__
#define RLX_AGENT __ATOMIC_RELAXED, __HIP_MEMORY_SCOPE_AGENT

constexpr int L = 2048, D = 640, P = 640, F = 1728;
constexpr int NSTEP = 94, NSTEP_F = 54;
constexpr size_t WS_EPOCH = 0;
constexpr size_t WS_FLAGW = 1024;
constexpr size_t WS_FLAGA = 4096;
constexpr size_t WS_GRAN = 8192;
constexpr size_t WS_STATS = WS_GRAN + 7 * 10 * 1024 + 2048;
constexpr size_t WS_ACT = 262144;
constexpr size_t ACT_BYTES = (size_t)32 * NSTEP * 4 * 1024;
constexpr size_t WS_W2T = WS_ACT + ACT_BYTES;
constexpr size_t W2T_BYTES = (size_t)8 * NSTEP * 5 * 1024;
constexpr size_t WS_STAMP = 32u << 20;
static_assert(WS_STATS + 2048 * 16 <= WS_ACT, "ws map");
constexpr int XSLOT = 32768, WSLOT = 20480, SLOT = XSLOT + WSLOT;
constexpr int LDS_PAR = 3 * SLOT;
constexpr int LDS1_BYTES = 163840;
constexpr int LDS_CS = 0;
constexpr int LDS_ST = 4096;
constexpr int LDS_ET = 8192;
constexpr int LDS_TP = 4096;
constexpr int LDS_W2 = 40960;
constexpr int LDS_RT = 2 * SLOT;
static_assert(LDS1_BYTES <= 163840, "lds");
constexpr int RSRC3 = 0x00020000;

struct Params {
  const u16 *x, *lre, *lim, *bre, *bim, *cre, *cim, *ldt, *dsk, *w1, *w3, *w2;
  float* out;
  char* ws;
};

DI float bf2f(u16 v) { return __uint_as_float(((unsigned)v) << 16); }
DI float rbf(float f) { unsigned u = __float_as_uint(f); u += 0x7FFFu + ((u >> 16) & 1u); return __uint_as_float(u & 0xFFFF0000u); }
DI unsigned pack2(float a, float b) { f32x2 p = {a, b}; return __builtin_bit_cast(unsigned, __builtin_convertvector(p, bf16x2)); }
DI f32x4 mfma16(u32x4 a, u32x4 b, f32x4 c) { return __builtin_amdgcn_mfma_f32_16x16x32_bf16(__builtin_bit_cast(bf16x8, a), __builtin_bit_cast(bf16x8, b), c, 0, 0, 0); }
DI void mfma16_acc(f32x4& c, u32x4 a, u32x4 b) { asm volatile("v_mfma_f32_16x16x32_bf16 %0, %1, %2, %0" : "+v"(c) : "v"(a), "v"(b)); }
template <int OFF> DI u32x2 trrd(unsigned addr) { u32x2 r; asm volatile("ds_read_b64_tr_b16 %0, %1 offset:%2" : "=v"(r) : "v"(addr), "i"(OFF) : "memory"); return r; }
template <int OFF> DI u32x4 rd128(unsigned addr) { u32x4 r; asm volatile("ds_read_b128 %0, %1 offset:%2" : "=v"(r) : "v"(addr), "i"(OFF) : "memory"); return r; }
DI u32x4 cat(u32x2 a, u32x2 b) { return u32x4{a.x, a.y, b.x, b.y}; }
DI void dma16(__amdgpu_buffer_rsrc_t rs, char* lds, int voff, int soff) { __builtin_amdgcn_raw_ptr_buffer_load_lds(rs, (lds_vp)lds, 16, voff, soff, 0, 0); }
#define SB() __builtin_amdgcn_sched_barrier(0)
#define WAIT_V(n) asm volatile("s_waitcnt vmcnt(" #n ")" ::: "memory")
#define WAIT_L0() asm volatile("s_waitcnt lgkmcnt(0)" ::: "memory")
#define BAR() __builtin_amdgcn_s_barrier()
#define LBAR() __builtin_amdgcn_s_barrier()
#define TS(i) do {} while (0)
DI float wave_sum(float v) {
  v += __builtin_bit_cast(float, __builtin_amdgcn_update_dpp(0, __builtin_bit_cast(int, v), 0x111, 0xf, 0xf, true));
  v += __builtin_bit_cast(float, __builtin_amdgcn_update_dpp(0, __builtin_bit_cast(int, v), 0x112, 0xf, 0xf, true));
  v += __builtin_bit_cast(float, __builtin_amdgcn_update_dpp(0, __builtin_bit_cast(int, v), 0x114, 0xf, 0xf, true));
  v += __builtin_bit_cast(float, __builtin_amdgcn_update_dpp(0, __builtin_bit_cast(int, v), 0x118, 0xf, 0xf, true));
  const int iv = __builtin_bit_cast(int, v);
  const float a = __builtin_bit_cast(float, __builtin_amdgcn_readlane(iv, 15)), b = __builtin_bit_cast(float, __builtin_amdgcn_readlane(iv, 31));
  const float c = __builtin_bit_cast(float, __builtin_amdgcn_readlane(iv, 47)), d = __builtin_bit_cast(float, __builtin_amdgcn_readlane(iv, 63));
  return (a + b) + (c + d);
}
DI float silu_f(float g) { return g * __builtin_amdgcn_rcpf(1.0f + __builtin_amdgcn_exp2f(g * -1.44269504088896f)); }

template <int MODE> struct Frags {
  static constexpr int NT = MODE == 0 ? 6 : 4;
  u32x4 x[4];
  u32x2 lo[MODE == 2 ? 1 : NT], hi[MODE == 2 ? 1 : NT];
  u32x4 v[MODE == 2 ? NT : 1];
  template <int KK, int C> DI void rdw(unsigned aw) {
    if constexpr (MODE == 2) v[C] = rd128<C * 2048>(aw);
    else { constexpr int O = KK * 10240 + (C & 1) * 2560 + ((MODE == 0 ? 0 : 3) + (C >> 1)) * 32; lo[C] = trrd<O>(aw); hi[C] = trrd<O + 1280>(aw); }
  }
  template <int KK, int C> DI void rdw_lo(unsigned aw) {
    if constexpr (MODE == 2) v[C] = rd128<C * 2048>(aw);
    else { constexpr int O = KK * 10240 + (C & 1) * 2560 + ((MODE == 0 ? 0 : 3) + (C >> 1)) * 32; lo[C] = trrd<O>(aw); }
  }
  template <int KK, int C> DI void rdw_hi(unsigned aw) {
    if constexpr (MODE != 2) { constexpr int O = KK * 10240 + (C & 1) * 2560 + ((MODE == 0 ? 0 : 3) + (C >> 1)) * 32; hi[C] = trrd<O + 1280>(aw); }
  }
  template <int T> DI void rdx(unsigned ax) { x[T] = rd128<(MODE == 2 ? 512 : 2048) * T>(ax); }
  DI u32x4 w(int c) const { if constexpr (MODE == 2) return v[c]; else return cat(lo[c], hi[c]); }
  DI void wait() {
    if constexpr (MODE == 0) asm volatile("s_waitcnt lgkmcnt(0)" : "+v"(x[0]), "+v"(x[1]), "+v"(x[2]), "+v"(x[3]), "+v"(lo[0]), "+v"(hi[0]), "+v"(lo[1]), "+v"(hi[1]), "+v"(lo[2]), "+v"(hi[2]),
                                          "+v"(lo[3 % NT]), "+v"(hi[3 % NT]), "+v"(lo[4 % NT]), "+v"(hi[4 % NT]), "+v"(lo[5 % NT]), "+v"(hi[5 % NT]) :: "memory");
    else if constexpr (MODE == 1) asm volatile("s_waitcnt lgkmcnt(0)" : "+v"(x[0]), "+v"(x[1]), "+v"(x[2]), "+v"(x[3]), "+v"(lo[0]), "+v"(hi[0]), "+v"(lo[1]), "+v"(hi[1]), "+v"(lo[2]), "+v"(hi[2]), "+v"(lo[3]), "+v"(hi[3]) :: "memory");
    else asm volatile("s_waitcnt lgkmcnt(0)" : "+v"(x[0]), "+v"(x[1]), "+v"(x[2]), "+v"(x[3]), "+v"(v[0]), "+v"(v[1 % NT]), "+v"(v[2 % NT]), "+v"(v[3 % NT]) :: "memory");
  }
};

struct Ctx {
  __amdgpu_buffer_rsrc_t rx, rw;
  int vx[4];
  int vw[3];
  int xso_base, wso_base, wso_step;
  int rot;
  char* smem;
  int xdst, wdst;
};

template <int MODE, class H0, class H7, class HA, class H8, class H9>
DI void main_loop(const Params& p, const Ctx& cx, unsigned lbase, int w, int lane, f32x4 (&acc)[4][Frags<MODE>::NT], f32x4& csa, H0 hook0, H7 hook7, HA hookA, H8 hook8, H9 hook9) {
  constexpr bool SSM = MODE == 2;
  constexpr int NT = Frags<MODE>::NT;
  const int wr = w & 3, wc = w >> 2;
  const int G = lane >> 4, i16 = lane & 15;
  unsigned ax[2], aw[2];
  if (SSM) {
    const int row = 64 * wr + 16 * (i16 >> 2) + (i16 & 3), f = ((i16 & 3) >> 1) | ((i16 >> 2) << 1);
#pragma unroll
    for (int kk = 0; kk < 2; ++kk) ax[kk] = lbase + row * 128 + (((4 * kk + G) ^ f) << 4);
    const int f2 = (i16 >> 1) & 7;
#pragma unroll
    for (int kk = 0; kk < 2; ++kk) aw[kk] = lbase + XSLOT + (64 * wc + i16) * 128 + (((4 * kk + G) ^ f2) << 4);
  } else {
    const int row = 64 * wr + i16, f = (i16 >> 1) & 7;
#pragma unroll
    for (int kk = 0; kk < 2; ++kk) ax[kk] = lbase + row * 128 + (((4 * kk + G) ^ f) << 4);
    const int q = (lane >> 2) & 3, pp = lane & 3;
    aw[0] = aw[1] = lbase + XSLOT + (G >> 1) * 5120 + (4 * (G & 1) + q) * 160 + pp * 8;
  }
  auto kchunk = [&](int s) { int k = s + cx.rot; return k >= 10 ? k - 10 : k; };
  auto dma = [&](int slot, int sox, int sow, int i) {
    char* b = cx.smem + slot;
    if (i < 4) dma16(cx.rx, b + cx.xdst + i * 1024, cx.vx[i], sox + i * 10240);
    else if (SSM) { if (i < 6) dma16(cx.rw, b + XSLOT + cx.wdst + (i - 4) * 1024, cx.vw[i - 4], sow + (i - 4) * 10240); }
    else if (i < 6) dma16(cx.rw, b + XSLOT + cx.wdst + (i - 4) * 1024, cx.vw[i - 4], sow);
    else { if (lane < 32) dma16(cx.rw, b + XSLOT + cx.wdst + 2048, cx.vw[2], sow); }
  };
  Frags<MODE> fa, fb;
  fa.template rdx<0>(ax[0]); fa.template rdx<1>(ax[0]); fa.template rdx<2>(ax[0]); fa.template rdx<3>(ax[0]);
  fa.template rdw<0, 0>(aw[0]); fa.template rdw<0, 1>(aw[0]); fa.template rdw<0, 2>(aw[0]); fa.template rdw<0, 3>(aw[0]);
  if constexpr (NT > 4) { fa.template rdw<0, 4 % NT>(aw[0]); fa.template rdw<0, 5 % NT>(aw[0]); }

#define MM1(F_, C, T) acc[T][C] = SSM ? mfma16(F_.x[T], F_.w(C), acc[T][C]) : mfma16(F_.w(C), F_.x[T], acc[T][C])
#define MMG(F_, C) do { MM1(F_, C, 0); MM1(F_, C, 1); MM1(F_, C, 2); MM1(F_, C, 3); } while (0)
#define NREADS (SSM ? 8 : 4 + 2 * NT)
#define RD(N_, KK, I, WA, XA) do { \
    if constexpr ((I) < 4) N_.template rdx<(I) & 3>(XA); \
    else if constexpr (SSM) { if constexpr ((I) < 8) N_.template rdw_lo<KK, ((I) - 4) & 3>(WA); } \
    else if constexpr ((I) < 4 + 2 * NT) { if constexpr (((I) & 1) == 0) N_.template rdw_lo<KK, (((I) - 4) >> 1) % NT>(WA); else N_.template rdw_hi<KK, (((I) - 4) >> 1) % NT>(WA); } \
  } while (0)
#define GRP(F_, N_, KK, C, R0, WA, XA, EXTRA) do { \
    MM1(F_, C, 0); RD(N_, KK, (R0), WA, XA); SB(); \
    MM1(F_, C, 1); RD(N_, KK, (R0) + 1, WA, XA); SB(); \
    MM1(F_, C, 2); RD(N_, KK, (R0) + 2, WA, XA); SB(); \
    MM1(F_, C, 3); RD(N_, KK, (R0) + 3, WA, XA); EXTRA; SB(); } while (0)
#define CSEL1(F_, C) do { const unsigned v_ = (io_ == (C)) ? 0x3F803F80u : 0u; const u32x4 s_ = {v_, v_, v_, v_}; csa = SSM ? mfma16(s_, F_.w(C), csa) : mfma16(F_.w(C), s_, csa); } while (0)
#define CSELALL(F_, M) do { if (M) { int io_ = i16; asm volatile("" : "+v"(io_)); CSEL1(F_, 0); CSEL1(F_, 1); CSEL1(F_, 2); CSEL1(F_, 3); if constexpr (NT > 4) { CSEL1(F_, 4 % NT); CSEL1(F_, 5 % NT); } } SB(); } while (0)
  int slot = 0;
  auto stage = [&](const int s, auto dx_t, auto dw_t, auto bnd_t) __attribute__((always_inline)) {
    constexpr bool DX = decltype(dx_t)::value, DW = decltype(dw_t)::value;
    constexpr int BND = decltype(bnd_t)::value;
    const int nslot = slot == 2 * SLOT ? 0 : slot + SLOT;
    int sox = 0, sow = 0, pows = 0, pslot = 0;
    if constexpr (DX) { sox = cx.xso_base + kchunk(s + 3) * 128; asm volatile("" : "+s"(sox)); }
    if constexpr (DW) { pows = cx.wso_base + kchunk(s + 2) * cx.wso_step; pslot = nslot == 2 * SLOT ? 0 : nslot + SLOT; asm volatile("" : "+s"(pows), "+s"(pslot)); }
    fa.wait(); SB();
    {
      const unsigned X1 = ax[1] + slot, W1 = aw[1] + slot;
      GRP(fa, fb, 1, 0, 0, W1, X1, if constexpr (DW) dma(pslot, 0, pows, 4));
      GRP(fa, fb, 1, 1, 4, W1, X1, if constexpr (DW) dma(pslot, 0, pows, 5));
      GRP(fa, fb, 1, 2, 8, W1, X1, if constexpr (!SSM && DW) dma(pslot, 0, pows, 6));
      GRP(fa, fb, 1, 3, 12, W1, X1, );
      if constexpr (NT > 4) {
        GRP(fa, fb, 1, 4 % NT, 16, W1, X1, );
        GRP(fa, fb, 1, 5 % NT, 20, W1, X1, );
      }
      CSELALL(fa, ((2 * s) & 3) == wr);
      if constexpr (BND == 0) hook9();
    }
    fb.wait(); SB();
    const unsigned X0 = ax[0] + nslot, W0 = aw[0] + nslot;
    MMG(fb, 0); SB();
    if constexpr (BND == 1) { if (SSM) WAIT_V(6); else WAIT_V(7); LBAR(); }
    if constexpr (BND == 2) { hook8(); WAIT_V(0); LBAR(); }
    GRP(fb, fa, 0, 1, 0, W0, X0, if constexpr (DX) dma(slot, sox, sow, 0));
    GRP(fb, fa, 0, 2, 4, W0, X0, if constexpr (DX) dma(slot, sox, sow, 1));
    GRP(fb, fa, 0, 3, 8, W0, X0, if constexpr (DX) dma(slot, sox, sow, 2));
    if constexpr (NT > 4) {
      GRP(fb, fa, 0, 4 % NT, 12, W0, X0, if constexpr (DX) dma(slot, sox, sow, 3));
      GRP(fb, fa, 0, 5 % NT, 16, W0, X0, );
    } else { if constexpr (DX) dma(slot, sox, sow, 3); }
    CSELALL(fb, ((2 * s + 1) & 3) == wr);
    slot = nslot;
  };
  using T_ = std::true_type; using F_ = std::false_type;
  using I0 = std::integral_constant<int, 0>; using I1 = std::integral_constant<int, 1>; using I2 = std::integral_constant<int, 2>;
  stage(0, T_{}, F_{}, I1{});
  hook0();
#pragma unroll 1
  for (int s = 1; s < 7; ++s) { stage(s, T_{}, T_{}, I1{}); if (s == 2) hook7(); }
  stage(7, F_{}, T_{}, I1{});
  hookA();
  stage(8, F_{}, F_{}, I2{});
  stage(9, F_{}, F_{}, I0{});
  fa.wait();
}

constexpr int LWS = 3;
#define DWF_ 0
constexpr int DWF = DWF_;
constexpr int DW = 5;
struct P2Ctx { __amdgpu_buffer_rsrc_t rw; int sw, vo; };
DI void p2_ldw(const P2Ctx& c, u32x4 (&wv)[5], int k) {
#pragma unroll
  for (int j = 0; j < 4; ++j) wv[j] = __builtin_amdgcn_raw_buffer_load_b128(c.rw, c.vo + j * 1024, c.sw + k * 5120, 16);
  wv[4] = __builtin_amdgcn_raw_buffer_load_b128(c.rw, c.vo, c.sw + k * 5120 + 4096, 16);
}
template <bool SSM>
DI unsigned phase1(const Params& p, char* smem, int mt, int cj  , int blk, int rot, const P2Ctx& p2c, u32x4 (&wv)[DW + 1][5]) {
  const int tid = threadIdx.x, lane = tid & 63, w = __builtin_amdgcn_readfirstlane(tid >> 6);
  const int wr = w & 3, wc = w >> 2, G = lane >> 4, i16 = lane & 15;
  const unsigned lbase = (unsigned)(size_t)(lds_cp)smem;
  TS(0);
  const int cbi = SSM ? 22 + cj : cj;
  u32x4 sr0, sr1; unsigned etag;
  {
    const __amdgpu_buffer_rsrc_t rs = __builtin_amdgcn_make_buffer_rsrc((void*)(p.x + (size_t)(256 * mt + 8 * cbi + w) * D), 0, D * 2, RSRC3);
    const int vo = lane * 16;
    asm volatile("buffer_load_dwordx4 %0, %3, %4, 0 offen\n\tbuffer_load_dwordx4 %1, %3, %4, 0 offen offset:1024\n\tglobal_load_dword %2, %5, off sc1"
                 : "=&v"(sr0), "=&v"(sr1), "=&v"(etag) : "v"(vo), "s"(rs), "v"(p.ws + WS_EPOCH) : "memory");
  }
  unsigned pa_ = 0, pb_ = 0, pc_ = 0;
  if (SSM) {
    const int ch = 64 * cj + lane;
    asm volatile("global_load_ushort %0, %3, off\n\tglobal_load_ushort %1, %4, off\n\tglobal_load_ushort %2, %5, off"
                 : "=&v"(pa_), "=&v"(pb_), "=&v"(pc_) : "v"(p.lre + ch), "v"(p.lim + ch), "v"(p.ldt + ch) : "memory");
  }
  Ctx cx;
  cx.smem = smem; cx.rot = rot;
  cx.rx = __builtin_amdgcn_make_buffer_rsrc((void*)(p.x + (size_t)(256 * mt + 32 * w) * D), 0, 32 * D * 2, RSRC3);
#pragma unroll
  for (int i = 0; i < 4; ++i) {
    int f;
    if (SSM) f = ((lane >> 4) & 1) | ((((2 * w + (i >> 1)) & 3)) << 1);
    else f = ((i & 1) << 2) | (lane >> 4);
    cx.vx[i] = (lane >> 3) * 1280 + (((lane & 7) ^ f) << 4);
  }
  cx.xso_base = 0; cx.xdst = w * 4096;
  if (SSM) {
    const int c = w & 3;
    cx.rw = __builtin_amdgcn_make_buffer_rsrc((void*)((c & 1) ? p.bim : p.bre), 0, P * D * 2, RSRC3);
    cx.vw[0] = (lane >> 3) * 1280 + (((lane & 7) ^ (lane >> 4)) << 4); cx.vw[1] = cx.vw[0] ^ 64; cx.vw[2] = 0;
    cx.wso_base = (64 * cj + 32 * (w >> 2) + 16 * (c >> 1)) * 1280; cx.wso_step = 128; cx.wdst = w * 2048;
  } else {
    const int u = w & 1;
    cx.rw = __builtin_amdgcn_make_buffer_rsrc((void*)(u ? p.w3 : p.w1), 0, D * F * 2, RSRC3);
#pragma unroll
    for (int i = 0; i < 3; ++i) {
      const int n = 64 * i + lane, pos = (n / 10) & 15, row = (pos & 3) | ((pos & 4) << 1) | ((pos & 8) >> 1);
      cx.vw[i] = row * 3456 + (n % 10) * 16 + cj * 160;
    }
    cx.wso_base = 16 * (w >> 1) * 3456; cx.wso_step = 64 * 3456; cx.wdst = (w >> 1) * 5120 + (w & 1) * 2560;
  }
  auto kchunk = [&](int s) { int k = s + rot; return k >= 10 ? k - 10 : k; };
  auto issue_stage = [&](int s, int slot) {
    const int sox = kchunk(s) * 128, sow = cx.wso_base + kchunk(s) * cx.wso_step;
    char* b = smem + slot;
#pragma unroll
    for (int i = 0; i < 4; ++i) dma16(cx.rx, b + cx.xdst + i * 1024, cx.vx[i], sox + i * 10240);
    dma16(cx.rw, b + XSLOT + cx.wdst, cx.vw[0], sow);
    dma16(cx.rw, b + XSLOT + cx.wdst + 1024, cx.vw[1], SSM ? sow + 10240 : sow);
    if (!SSM) { if (lane < 32) dma16(cx.rw, b + XSLOT + cx.wdst + 2048, cx.vw[2], sow); }
  };
  issue_stage(0, 0);
  TS(6);
  BAR();
  const int unit = w * 256 + blk;
  const bool has_unit = (w < 4) && (unit < 940);
  char* rt = smem + LDS_RT + (w & 3) * 4096;
  if (has_unit) {
    if (unit < 540) {
      const int s = unit / 10, dq = unit % 10;
      const __amdgpu_buffer_rsrc_t r2 = __builtin_amdgcn_make_buffer_rsrc((void*)p.w2, 0, F * D * 2, RSRC3);
      const int vo = (lane >> 3) * 1280 + (lane & 7) * 16;
#pragma unroll
      for (int i = 0; i < 4; ++i) dma16(r2, rt + i * 1024, vo, (32 * s + 8 * i) * 1280 + dq * 128);
    } else {
      const int u = unit - 540, c = u / 10, v = u % 10;
      const int vo = (lane & 15) * 1280 + (lane >> 4) * 16;
#pragma unroll
      for (int i = 0; i < 4; ++i) {
        const int sp = 4 * v + i, jg = sp >> 1, part = sp & 1;
        const __amdgpu_buffer_rsrc_t rc = __builtin_amdgcn_make_buffer_rsrc((void*)(part ? p.cim : p.cre), 0, D * P * 2, RSRC3);
        dma16(rc, rt + i * 1024, vo, 16 * c * 1280 + jg * 64);
      }
    }
  }
  issue_stage(1, SLOT);
  if (SSM) WAIT_V(6); else WAIT_V(7);
  TS(13);
  const __amdgpu_buffer_rsrc_t rW2T = __builtin_amdgcn_make_buffer_rsrc((void*)(p.ws + WS_W2T), 0, (int)W2T_BYTES, RSRC3);
  {
    u32x4 vv[4];
    const unsigned rtl = lbase + LDS_RT + (w & 3) * 4096;
    if (has_unit) {
      if (unit < 540) {
        const int g = lane >> 4, q = (lane >> 2) & 3, pp = lane & 3;
        const unsigned a0 = rtl + (8 * g + q) * 128 + 8 * pp;
        u32x2 l0 = trrd<0>(a0), h0 = trrd<512>(a0), l1 = trrd<32>(a0), h1 = trrd<512 + 32>(a0), l2 = trrd<64>(a0), h2 = trrd<512 + 64>(a0), l3 = trrd<96>(a0), h3 = trrd<512 + 96>(a0);
        asm volatile("s_waitcnt lgkmcnt(0)" : "+v"(l0), "+v"(h0), "+v"(l1), "+v"(h1), "+v"(l2), "+v"(h2), "+v"(l3), "+v"(h3) :: "memory");
        vv[0] = cat(l0, h0); vv[1] = cat(l1, h1); vv[2] = cat(l2, h2); vv[3] = cat(l3, h3);
      } else {
        u32x4 v0 = rd128<0>(rtl + lane * 16), v1 = rd128<1024>(rtl + lane * 16), v2 = rd128<2048>(rtl + lane * 16), v3 = rd128<3072>(rtl + lane * 16);
        asm volatile("s_waitcnt lgkmcnt(0)" : "+v"(v0), "+v"(v1), "+v"(v2), "+v"(v3) :: "memory");
        vv[0] = v0; vv[1] = v1; vv[2] = v2; vv[3] = v3;
      }
    }
    BAR();
    issue_stage(2, 2 * SLOT);
    if (has_unit) {
#pragma unroll
      for (int i = 0; i < 4; ++i) {
        int c, s;
        if (unit < 540) { s = unit / 10; c = 4 * (unit % 10) + i; } else { const int u = unit - 540; c = u / 10; s = NSTEP_F + 4 * (u % 10) + i; }
        __builtin_amdgcn_raw_buffer_store_b128(vv[i], rW2T, lane * 16, (((c / 5) * NSTEP + s) * 5 + (c % 5)) * 1024, 16);
      }
    }
  }
  TS(1);
  auto hook0 = [&]() {
    asm volatile("" : "+v"(sr0), "+v"(sr1), "+v"(etag), "+v"(pa_), "+v"(pb_), "+v"(pc_));
    etag += 1u;
    float v[16];
#pragma unroll
    for (int i = 0; i < 4; ++i) { v[2 * i] = __uint_as_float(sr0[i] << 16); v[2 * i + 1] = __uint_as_float(sr0[i] & 0xFFFF0000u); v[8 + 2 * i] = __uint_as_float(sr1[i] << 16); v[9 + 2 * i] = __uint_as_float(sr1[i] & 0xFFFF0000u); }
    float sum = 0.f;
#pragma unroll
    for (int i = 0; i < 16; ++i) sum += v[i];
    sum = wave_sum(sum);
    const float mean = sum * (1.0f / D);
    float sq = 0.f;
#pragma unroll
    for (int i = 0; i < 8; ++i) { const float d = v[i] - mean; sq = fmaf(d, d, sq); }
    float sq2 = 0.f;
#pragma unroll
    for (int i = 8; i < 16; ++i) { const float d = v[i] - mean; sq2 = fmaf(d, d, sq2); }
    sq += lane < 16 ? sq2 : 0.f;
    sq = wave_sum(sq);
    const float rstd = 1.0f / sqrtf(sq * (1.0f / D) + 1e-5f);
    if (lane == 0) {
      gu64* g = (gu64*)(p.ws + WS_STATS + (size_t)(256 * mt + 8 * cbi + w) * 16);
      __hip_atomic_store(g, ((unsigned long long)etag << 32) | __float_as_uint(mean), RLX_AGENT);
      __hip_atomic_store(g + 1, ((unsigned long long)etag << 32) | __float_as_uint(rstd), RLX_AGENT);
    }
    if (SSM && w == 5) {
      const float a = bf2f((u16)pa_), b = bf2f((u16)pb_);
      const float dt = rbf(expf(bf2f((u16)pc_)));
      const float er = expf(dt * a);
      float sn, cn; sincosf(dt * b, &sn, &cn);
      const float lr = er * cn, li = er * sn;
      const float nr = lr - 1.0f, ni = li, den = a * a + b * b;
      const float cr = (nr * a + ni * b) / den, ci = (ni * a - nr * b) / den;
      float pr = lr, pi = li;
#pragma unroll
      for (int i = 0; i < 4; ++i) { float t = pr * pr - pi * pi; pi = 2.f * pr * pi; pr = t; }
      const float l16r = pr, l16i = pi;
#pragma unroll
      for (int i = 0; i < 4; ++i) { float t = pr * pr - pi * pi; pi = 2.f * pr * pi; pr = t; }
      float* par = (float*)(smem + LDS_PAR) + lane * 8;
      *(f32x4*)par = f32x4{lr, li, cr, ci};
      *(f32x4*)(par + 4) = f32x4{l16r, l16i, pr, pi};
    }
  };
  auto hook7 = [&]() { if (tid == 0) __hip_atomic_store((gu32*)(p.ws + WS_FLAGW) + blk, etag, RLX_AGENT); };
  const __amdgpu_buffer_rsrc_t rACT = __builtin_amdgcn_make_buffer_rsrc((void*)(p.ws + WS_ACT), 0, (int)ACT_BYTES, RSRC3);
  const __amdgpu_buffer_rsrc_t rFW = __builtin_amdgcn_make_buffer_rsrc((void*)(p.ws + WS_FLAGW), 0, 1024, RSRC3);
  const int mt64 = 4 * mt + wr;
  gu64* stg = (gu64*)(p.ws + WS_STATS + (size_t)(256 * mt + 64 * wr + lane) * 16);
  unsigned long long sta = 0, stb = 0;
  u32x4 fw = {0u, 0u, 0u, 0u};
  auto hookA = [&]() { if (w == 0) fw = __builtin_amdgcn_raw_buffer_load_b128(rFW, lane * 16, 0, 16); };
  auto hook8 = [&]() {
    if (w == 0) {
      unsigned spins = 0;
      while (!__all(fw.x == etag && fw.y == etag && fw.z == etag && fw.w == etag)) {
        if (++spins > (1u << 16)) break;
        __builtin_amdgcn_s_sleep(1);
        fw = __builtin_amdgcn_raw_buffer_load_b128(rFW, lane * 16, 0, 16);
      }
      if (blk == 0 && lane == 0) __hip_atomic_store((gu32*)(p.ws + WS_EPOCH), etag, RLX_AGENT);
      __builtin_amdgcn_fence(__ATOMIC_ACQUIRE, "agent");
    }
  };
  auto hook9 = [&]() { sta = __hip_atomic_load(stg, RLX_AGENT); stb = __hip_atomic_load(stg + 1, RLX_AGENT); };
  auto w2_to_lds = [&]() {
#pragma unroll
    for (int k = 0; k < LWS * 5; ++k) dma16(p2c.rw, smem + LDS_W2 + w * (LWS * 5120) + k * 1024, p2c.vo, p2c.sw + k * 1024);
  };
  f32x4 csa = {0.f, 0.f, 0.f, 0.f};
  float* cst = (float*)(smem + LDS_CS);
  float* stt = (float*)(smem + LDS_ST);
  auto finish_common = [&](int ntw  , int tile0  ) {
    unsigned spins = 0;
    while (!__all((unsigned)(sta >> 32) == etag && (unsigned)(stb >> 32) == etag)) {
      if (++spins > (1u << 16)) break;
      __builtin_amdgcn_s_sleep(1);
      sta = __hip_atomic_load(stg, RLX_AGENT); stb = __hip_atomic_load(stg + 1, RLX_AGENT);
    }
    __syncthreads();
    const float mean = __uint_as_float((unsigned)sta), rstd = __uint_as_float((unsigned)stb);
    if (wc == 0) *(f32x2*)(stt + (64 * wr + lane) * 2) = f32x2{rstd, -mean * rstd};
    if (SSM) { if (G == 0) {
#pragma unroll
        for (int c = 0; c < 4; ++c) cst[wr * 160 + (tile0 + c) * 16 + i16] = csa[c]; } }
    else { if (i16 < ntw) *(f32x4*)(cst + wr * 160 + (tile0 + i16) * 16 + 4 * G) = csa; }
    __syncthreads();
    if (tid < 160) cst[4 * 160 + tid] = (cst[tid] + cst[160 + tid]) + (cst[320 + tid] + cst[480 + tid]);
    __syncthreads();
  };
  const float* csf = cst + 4 * 160;

  if (!SSM) {
    auto ffn = [&](auto mode_tag) {
      constexpr int MODE = decltype(mode_tag)::value;
      constexpr int NT = Frags<MODE>::NT, P0 = MODE == 0 ? 0 : 3;
      f32x4 acc[4][NT];
#pragma unroll
      for (int T = 0; T < 4; ++T)
#pragma unroll
        for (int c = 0; c < NT; ++c) acc[T][c] = f32x4{0.f, 0.f, 0.f, 0.f};
      main_loop<MODE>(p, cx, lbase, w, lane, acc, csa, hook0, hook7, hookA, hook8, hook9);
      TS(2);
      finish_common(NT, 2 * P0);
      w2_to_lds();
#pragma unroll
      for (int k = 0; k < DWF; ++k) p2_ldw(p2c, wv[k], LWS + k);
      SB();
      f32x2 st4[4];
#pragma unroll
      for (int T = 0; T < 4; ++T) st4[T] = *(const f32x2*)(stt + (64 * wr + 16 * T + i16) * 2);
#pragma unroll
      for (int Pq = 0; Pq < NT / 2; ++Pq) {
        const int ft = 5 * cj + P0 + Pq;
        const f32x4 cg = *(const f32x4*)(csf + (2 * P0 + 2 * Pq) * 16 + 4 * G), cu = *(const f32x4*)(csf + (2 * P0 + 2 * Pq + 1) * 16 + 4 * G);
        unsigned pk[4][2];
#pragma unroll
        for (int T = 0; T < 4; ++T) {
          float hv[4];
#pragma unroll
          for (int e = 0; e < 4; ++e) {
            const float g = fmaf(acc[T][2 * Pq][e], st4[T].x, st4[T].y * cg[e]);
            const float u = fmaf(acc[T][2 * Pq + 1][e], st4[T].x, st4[T].y * cu[e]);
            hv[e] = silu_f(g) * u;
          }
          pk[T][0] = pack2(hv[0], hv[1]); pk[T][1] = pack2(hv[2], hv[3]);
        }
        if (ft < 108) {
#pragma unroll
          for (int T2 = 0; T2 < 4; T2 += 2) {
            const auto a = __builtin_amdgcn_permlane16_swap(pk[T2][0], pk[T2 + 1][0], false, false);
            const auto b = __builtin_amdgcn_permlane16_swap(pk[T2][1], pk[T2 + 1][1], false, false);
            const u32x4 v = {a[0], b[0], a[1], b[1]};
            const int s2 = ft >> 1, gp = 2 * (ft & 1) + (G >> 1), T = T2 + (G & 1);
            __builtin_amdgcn_raw_buffer_store_b128(v, rACT, T * 1024 + (gp * 16 + i16) * 16, (mt64 * NSTEP + s2) * 4096, 16);
          }
        }
      }
    };
    if (wc == 0) ffn(std::integral_constant<int, 0>{}); else ffn(std::integral_constant<int, 1>{});
  } else {
    f32x4 acc[4][4];
#pragma unroll
    for (int T = 0; T < 4; ++T)
#pragma unroll
      for (int c = 0; c < 4; ++c) acc[T][c] = f32x4{0.f, 0.f, 0.f, 0.f};
    main_loop<2>(p, cx, lbase, w, lane, acc, csa, hook0, hook7, hookA, hook8, hook9);
    TS(2);
    const float* par = (const float*)(smem + LDS_PAR);
    float lr[2], li[2], cr[2], ci[2];
#pragma unroll
    for (int j = 0; j < 2; ++j) {
      const f32x4 pv = *(const f32x4*)(par + (32 * wc + 16 * j + i16) * 8);
      lr[j] = pv.x; li[j] = pv.y; cr[j] = pv.z; ci[j] = pv.w;
    }
    f32x4 pv = *(const f32x4*)(par + lane * 8 + 4);
    asm volatile("" : "+v"(pv), "+v"(lr[0]), "+v"(li[0]), "+v"(cr[0]), "+v"(ci[0]), "+v"(lr[1]), "+v"(li[1]), "+v"(cr[1]), "+v"(ci[1]));
    finish_common(4, 4 * wc);
    w2_to_lds();
    SB();
    {
      float csl[4];
#pragma unroll
      for (int c = 0; c < 4; ++c) csl[c] = csf[(4 * wc + c) * 16 + i16];
#pragma unroll
      for (int T = 0; T < 4; ++T)
#pragma unroll
        for (int e = 0; e < 4; e += 2) {
          const f32x4 sv = *(const f32x4*)(stt + (64 * wr + 16 * G + 4 * T + e) * 2);
#pragma unroll
          for (int c = 0; c < 4; ++c) {
            acc[T][c][e] = fmaf(acc[T][c][e], sv.x, sv.y * csl[c]);
            acc[T][c][e + 1] = fmaf(acc[T][c][e + 1], sv.z, sv.w * csl[c]);
          }
        }
    }
    f32x2* et = (f32x2*)(smem + LDS_ET);
    const int seg = 4 * wr + G;
#pragma unroll
    for (int j = 0; j < 2; ++j) {
      float hr = 0.f, hi = 0.f;
#pragma unroll
      for (int i = 0; i < 16; ++i) {
        const float nr = fmaf(lr[j], hr, fmaf(-li[j], hi, acc[i >> 2][2 * j][i & 3]));
        const float ni = fmaf(lr[j], hi, fmaf(li[j], hr, acc[i >> 2][2 * j + 1][i & 3]));
        hr = nr; hi = ni;
      }
      et[seg * 64 + 32 * wc + 16 * j + i16] = f32x2{hr, hi};
    }
    __syncthreads();
    TS(3);
    if (w == 0) {
      const unsigned tag = etag;
      float xr_ = 0.f, xi_ = 0.f;
      f32x2 ev[16];
#pragma unroll
      for (int s = 0; s < 16; ++s) ev[s] = et[s * 64 + lane];
#pragma unroll
      for (int s = 0; s < 16; ++s) {
        const float nr = fmaf(pv.x, xr_, fmaf(-pv.y, xi_, ev[s].x));
        const float ni = fmaf(pv.x, xi_, fmaf(pv.y, xr_, ev[s].y));
        xr_ = nr; xi_ = ni;
      }
      gu64* gran = (gu64*)(p.ws + WS_GRAN);
      if (mt < 7) {
        gu64* g = gran + ((size_t)(mt * 10 + cj) * 64 + lane) * 2;
        __hip_atomic_store(g, ((unsigned long long)tag << 32) | __float_as_uint(xr_), RLX_AGENT);
        __hip_atomic_store(g + 1, ((unsigned long long)tag << 32) | __float_as_uint(xi_), RLX_AGENT);
      }
      float Hr = 0.f, Hi = 0.f;
      if (mt > 0) {
        unsigned long long ga[7], gb[7];
        unsigned spins = 0;
        while (true) {
          bool ok = true;
#pragma unroll
          for (int m = 0; m < 7; ++m) {
            if (m < mt) {
              gu64* g = gran + ((size_t)(m * 10 + cj) * 64 + lane) * 2;
              ga[m] = __hip_atomic_load(g, RLX_AGENT); gb[m] = __hip_atomic_load(g + 1, RLX_AGENT);
            } else { ga[m] = gb[m] = (unsigned long long)tag << 32; }
          }
#pragma unroll
          for (int m = 0; m < 7; ++m) ok = ok && ((unsigned)(ga[m] >> 32) == tag) && ((unsigned)(gb[m] >> 32) == tag);
          if (__all(ok)) break;
          if (++spins > (1u << 16)) break;
          __builtin_amdgcn_s_sleep(1);
        }
#pragma unroll
        for (int m = 0; m < 7; ++m) {
          if (m < mt) {
            const float sr = __uint_as_float((unsigned)ga[m]), si = __uint_as_float((unsigned)gb[m]);
            const float nr = fmaf(pv.z, Hr, fmaf(-pv.w, Hi, sr));
            const float ni = fmaf(pv.z, Hi, fmaf(pv.w, Hr, si));
            Hr = nr; Hi = ni;
          }
        }
      }
      xr_ = Hr; xi_ = Hi;
#pragma unroll
      for (int s = 0; s < 16; ++s) {
        et[s * 64 + lane] = f32x2{xr_, xi_};
        const float nr = fmaf(pv.x, xr_, fmaf(-pv.y, xi_, ev[s].x));
        const float ni = fmaf(pv.x, xi_, fmaf(pv.y, xr_, ev[s].y));
        xr_ = nr; xi_ = ni;
      }
    }
    __syncthreads();
    TS(4);
    f32x2 x0[2];
#pragma unroll
    for (int j = 0; j < 2; ++j) x0[j] = et[seg * 64 + 32 * wc + 16 * j + i16];
    WAIT_L0();
    BAR();
    unsigned pre[2][8], pim[2][8];
#pragma unroll
    for (int j = 0; j < 2; ++j) {
      float hr = x0[j].x, hi = x0[j].y;
      float ore[16], oim[16];
#pragma unroll
      for (int i = 0; i < 16; ++i) {
        const float nr = fmaf(lr[j], hr, fmaf(-li[j], hi, acc[i >> 2][2 * j][i & 3]));
        const float ni = fmaf(lr[j], hi, fmaf(li[j], hr, acc[i >> 2][2 * j + 1][i & 3]));
        hr = nr; hi = ni;
        ore[i] = cr[j] * hr - ci[j] * hi;
        oim[i] = -(cr[j] * hi + ci[j] * hr);
      }
#pragma unroll
      for (int i = 0; i < 8; ++i) { pre[j][i] = pack2(ore[2 * i], ore[2 * i + 1]); pim[j][i] = pack2(oim[2 * i], oim[2 * i + 1]); }
    }
    {
      char* tp = smem + LDS_TP + w * 4608;
      const int q = (lane >> 2) & 3, pp = lane & 3;
      const unsigned tb = lbase + LDS_TP + w * 4608 + (8 * G + q) * 144 + 8 * pp;
      const int jg = 2 * cj + wc;
#pragma unroll
      for (int part = 0; part < 2; ++part) {
#pragma unroll
        for (int j = 0; j < 2; ++j) {
          char* t0 = tp + (16 * j + i16) * 144 + 32 * G;
          const unsigned* src = part ? pim[j] : pre[j];
          *(u32x4*)t0 = u32x4{src[0], src[1], src[2], src[3]};
          *(u32x4*)(t0 + 16) = u32x4{src[4], src[5], src[6], src[7]};
        }
        WAIT_L0();
        u32x2 lo[4], hi[4];
        lo[0] = trrd<0>(tb); hi[0] = trrd<576>(tb); lo[1] = trrd<32>(tb); hi[1] = trrd<576 + 32>(tb); lo[2] = trrd<64>(tb); hi[2] = trrd<576 + 64>(tb); lo[3] = trrd<96>(tb); hi[3] = trrd<576 + 96>(tb);
        asm volatile("s_waitcnt lgkmcnt(0)" : "+v"(lo[0]), "+v"(hi[0]), "+v"(lo[1]), "+v"(hi[1]), "+v"(lo[2]), "+v"(hi[2]), "+v"(lo[3]), "+v"(hi[3]) :: "memory");
        const int s2 = NSTEP_F + 2 * jg + part;
#pragma unroll
        for (int T = 0; T < 4; ++T)
          __builtin_amdgcn_raw_buffer_store_b128(cat(lo[T], hi[T]), rACT, T * 1024 + lane * 16, (mt64 * NSTEP + s2) * 4096, 16);
      }
    }
  }
  TS(5);
  WAIT_V(0);
  BAR();
  if (tid == 0) __hip_atomic_store((gu32*)(p.ws + WS_FLAGA + mt * 128) + cbi, etag, RLX_AGENT);
  asm volatile("" ::: "memory"); SB();
#pragma unroll
  for (int k = (SSM ? 0 : DWF); k < DW; ++k) p2_ldw(p2c, wv[k], LWS + k);
  SB();
  return etag;
}

DI void p2_run(const Params& p, unsigned lw  , __amdgpu_buffer_rsrc_t ra, const P2Ctx& p2c, u32x4 (&wv)[DW + 1][5],
               int sa, int N, int w, int lane, int mt2, int shA, int shW, unsigned etag, f32x4 (&acc)[4][5]) {
  constexpr int DA = 2, NMIN = 10, NMAX = 14, NPRE = LWS + DW, NS = DW + 1;
  u32x4 a[DA][4];
  const int vo = p2c.vo;
  auto lda = [&](int k, int slot) {
#pragma unroll
    for (int T = 0; T < 4; ++T) a[slot][T] = __builtin_amdgcn_raw_buffer_load_b128(ra, vo + T * 1024, sa + k * 4096, 16);
  };
  unsigned tw0;
  {
    const int NL = (N - NPRE) * 10;
    tw0 = __builtin_amdgcn_raw_buffer_load_b32(p2c.rw, lane < NL ? (shW * NL + lane) * 128 : 0, p2c.sw + NPRE * 5120, 16);
  }
  {
    const int fi = w < 4 ? (lane < 22 ? lane : 0) : 22 + (lane < 10 ? lane : 0);
    gu32* fl = (gu32*)(p.ws + WS_FLAGA + mt2 * 128) + fi;
    unsigned spins = 0;
    while (!__all(__hip_atomic_load(fl, RLX_AGENT) == etag)) {
      if (++spins > (1u << 16)) break;
      __builtin_amdgcn_s_sleep(1);
    }
  }
  __builtin_amdgcn_fence(__ATOMIC_ACQUIRE, "wavefront");
  TS(9);
#pragma unroll
  for (int s = 0; s < DA - 1; ++s) lda(s, s);
  unsigned ta;
  {
    const int NL = (N - (DA - 1)) * 4;
    ta = __builtin_amdgcn_raw_buffer_load_b32(ra, lane < NL ? (shA * NL + lane) * 128 : 0, sa + (DA - 1) * 4096, 16);
  }
#pragma unroll
  for (int T = 0; T < 4; ++T)
#pragma unroll
    for (int c = 0; c < 5; ++c) asm volatile("" : "+v"(acc[T][c]));
  asm volatile("s_nop 7" ::: "memory");
  SB();
#pragma unroll
  for (int s = 0; s < NMAX; ++s) {
    if (s < NMIN || s < N) {
      const int slot = s < LWS ? DW : (s - LWS) % NS;
      if (s + DA - 1 < NMIN || s + DA - 1 < N) lda(s + DA - 1, (s + DA - 1) % DA);
      if (s < LWS) {
        u32x4 (&t)[5] = wv[DW];
        if (s == 0) { t[0] = rd128<0>(lw); t[1] = rd128<1024>(lw); t[2] = rd128<2048>(lw); t[3] = rd128<3072>(lw); t[4] = rd128<4096>(lw); }
        if (s == 1) { t[0] = rd128<5120>(lw); t[1] = rd128<5120 + 1024>(lw); t[2] = rd128<5120 + 2048>(lw); t[3] = rd128<5120 + 3072>(lw); t[4] = rd128<5120 + 4096>(lw); }
        if (s == 2) { t[0] = rd128<10240>(lw); t[1] = rd128<10240 + 1024>(lw); t[2] = rd128<10240 + 2048>(lw); t[3] = rd128<10240 + 3072>(lw); t[4] = rd128<10240 + 4096>(lw); }
        asm volatile("s_waitcnt lgkmcnt(0)" : "+v"(t[0]), "+v"(t[1]), "+v"(t[2]), "+v"(t[3]), "+v"(t[4]) :: "memory");
      }
      SB();
#pragma unroll
      for (int T = 0; T < 4; ++T)
#pragma unroll
        for (int c = 0; c < 5; ++c) mfma16_acc(acc[T][c], wv[slot][c], a[s % DA][T]);
      SB();
      if (s >= LWS - 1 && s + NS < NMAX) { if (s + NS < NMIN || s + NS < N) p2_ldw(p2c, wv[slot], s + NS); }
      SB();
    }
  }
  asm volatile("s_nop 15\n\ts_nop 7" ::: "memory");
  asm volatile("" :: "v"(tw0), "v"(ta));
}

DI void phase2(const Params& p, char* smem, unsigned etag, const P2Ctx& p2c, u32x4 (&wv)[DW + 1][5], int mt64, int nt, int s0) {
  const int tid = threadIdx.x, lane = tid & 63, w = __builtin_amdgcn_readfirstlane(tid >> 6);
  TS(8);
  f32x4 acc[4][5];
#pragma unroll
  for (int T = 0; T < 4; ++T)
#pragma unroll
    for (int c = 0; c < 5; ++c) acc[T][c] = f32x4{0.f, 0.f, 0.f, 0.f};
  const __amdgpu_buffer_rsrc_t ra = __builtin_amdgcn_make_buffer_rsrc((void*)(p.ws + WS_ACT), 0, (int)ACT_BYTES, RSRC3);
  const int sa = (mt64 * NSTEP + s0) * 4096;
  const int shA = nt, shW = mt64 & 3;
  const int N = w < 2 ? 14 : (w < 4 ? 13 : 10);
  const unsigned lw = (unsigned)(size_t)(lds_cp)smem + LDS_W2 + w * (LWS * 5120) + lane * 16;
  p2_run(p, lw, ra, p2c, wv, sa, N, w, lane, mt64 >> 2, shA, shW, etag, acc);
  auto part_addr = [&](int wv_, int t) { return t < 15 ? LDS_W2 + wv_ * (LWS * 5120) + t * 1024 : wv_ * 5120 + (t - 15) * 1024; };
  {
    const int g = lane >> 4, r = lane & 15;
    const int pos = g * 16 + ((r + 4 * g) & 15);
#pragma unroll
    for (int T = 0; T < 4; ++T)
#pragma unroll
      for (int c = 0; c < 5; ++c) *(f32x4*)(smem + part_addr(w, T * 5 + c) + pos * 16) = acc[T][c];
  }
  const __amdgpu_buffer_rsrc_t rout = __builtin_amdgcn_make_buffer_rsrc((void*)p.out, 0, L * D * 4, RSRC3);
  f32x4 res[3]; u32x2 xv[3], dv[3]; unsigned long long ga[3], gb[3];
#pragma unroll
  for (int it = 0; it < 3; ++it) {
    const int item = tid + 512 * it;
    if (item < 1280) {
      const int tok = item / 20, dg = item % 20;
      const int gt = mt64 * 64 + tok, d0 = nt * 80 + dg * 4;
      gu64* sg = (gu64*)(p.ws + WS_STATS + (size_t)gt * 16);
      ga[it] = __hip_atomic_load(sg, RLX_AGENT); gb[it] = __hip_atomic_load(sg + 1, RLX_AGENT);
      xv[it] = *(const u32x2*)(p.x + (size_t)gt * D + d0);
      dv[it] = *(const u32x2*)(p.dsk + d0);
    }
  }
  __syncthreads();
  TS(10);
#pragma unroll
  for (int it = 0; it < 3; ++it) {
    const int item = tid + 512 * it;
    if (item < 1280) {
      const int tok = item / 20, dg = item % 20, c = dg >> 2, g = dg & 3, T = tok >> 4, rr = tok & 15;
      const int t = T * 5 + c, off = (g * 16 + ((rr + 4 * g) & 15)) * 16;
      const int gt = mt64 * 64 + tok, d0 = nt * 80 + dg * 4;
      const int ta = t < 15 ? LDS_W2 + t * 1024 : (t - 15) * 1024, st = t < 15 ? LWS * 5120 : 5120;
      const char* base = smem + ta + off;
      f32x4 o = *(const f32x4*)(base);
#pragma unroll
      for (int k = 1; k < 4; ++k) o += *(const f32x4*)(base + k * st);
      f32x4 y = *(const f32x4*)(base + 4 * st);
#pragma unroll
      for (int k = 5; k < 8; ++k) y += *(const f32x4*)(base + k * st);
      const float mean = __uint_as_float((unsigned)ga[it]), rstd = __uint_as_float((unsigned)gb[it]);
      const float xs[4] = {__uint_as_float(xv[it].x << 16), __uint_as_float(xv[it].x & 0xFFFF0000u), __uint_as_float(xv[it].y << 16), __uint_as_float(xv[it].y & 0xFFFF0000u)};
      const float ds[4] = {__uint_as_float(dv[it].x << 16), __uint_as_float(dv[it].x & 0xFFFF0000u), __uint_as_float(dv[it].y << 16), __uint_as_float(dv[it].y & 0xFFFF0000u)};
#pragma unroll
      for (int e = 0; e < 4; ++e) {
        const float xn = (xs[e] - mean) * rstd;
        const float yy = fmaf(2.0f, y[e], xn * ds[e]);
        res[it][e] = fmaf(yy, silu_f(o[e]), xs[e]);
      }
      __builtin_amdgcn_raw_buffer_store_b128(__builtin_bit_cast(u32x4, res[it]), rout, (gt * D + d0) * 4, 0, OUT_AUX);
    }
  }
  TS(11);
}

__global__ __launch_bounds__(512, 2) void k_fused(Params p) {
  extern __shared__ __attribute__((aligned(16))) char smem[];
  const int b = blockIdx.x, xcd = b & 7, i = b >> 3;
  const int ml = i & 3, cl = i >> 2;
  const int mt = (xcd & 1) * 4 + ml, cb = (xcd >> 1) * 8 + cl;
  const int rot = (cl + 3 * ml) % 10;
  const int w = __builtin_amdgcn_readfirstlane(threadIdx.x >> 6), lane = threadIdx.x & 63;
  const int mt64 = xcd * 4 + (i & 3), nt = i >> 2;
  const int s0 = w < 4 ? (w < 2 ? 14 * w : 28 + 13 * (w - 2)) : 54 + 10 * (w - 4);
  P2Ctx p2c;
  p2c.rw = __builtin_amdgcn_make_buffer_rsrc((void*)(p.ws + WS_W2T), 0, (int)W2T_BYTES, RSRC3);
  p2c.sw = (nt * NSTEP + s0) * 5120; p2c.vo = lane * 16;
  u32x4 wv[DW + 1][5];
  unsigned etag;
  if (cb < 22) etag = phase1<false>(p, smem, mt, cb, b, rot, p2c, wv);
  else etag = phase1<true>(p, smem, mt, cb - 22, b, rot, p2c, wv);
  phase2(p, smem, etag, p2c, wv, mt64, nt, s0);
}
}

extern "C" void kernel_launch(void* const* d_in, const int* in_sizes, int n_in, void* d_out, int out_size, void* d_ws, size_t ws_size, hipStream_t stream) {
  using namespace s5;
  Params p;
  p.x = (const u16*)d_in[0]; p.lre = (const u16*)d_in[1]; p.lim = (const u16*)d_in[2];
  p.bre = (const u16*)d_in[3]; p.bim = (const u16*)d_in[4]; p.cre = (const u16*)d_in[5]; p.cim = (const u16*)d_in[6];
  p.ldt = (const u16*)d_in[7]; p.dsk = (const u16*)d_in[8];
  p.w1 = (const u16*)d_in[10]; p.w3 = (const u16*)d_in[11]; p.w2 = (const u16*)d_in[12];
  p.out = (float*)d_out; p.ws = (char*)d_ws;
  (void)hipFuncSetAttribute((const void*)k_fused, hipFuncAttributeMaxDynamicSharedMemorySize, LDS1_BYTES);
  k_fused<<<256, 512, LDS1_BYTES, stream>>>(p);
}
```

```cpp
#include <hip/hip_runtime.h>
#include <stdint.h>
#include <type_traits>

namespace s5 {
typedef unsigned short u16;
typedef short bf16x8 __attribute__((ext_vector_type(8)));
typedef float f32x4 __attribute__((ext_vector_type(4)));
typedef float f32x2 __attribute__((ext_vector_type(2)));
typedef unsigned u32x4 __attribute__((ext_vector_type(4)));
typedef unsigned u32x2 __attribute__((ext_vector_type(2)));
typedef __bf16 bf16x2 __attribute__((ext_vector_type(2)));
typedef __attribute__((address_space(3))) void* lds_vp;
typedef __attribute__((address_space(3))) char* lds_cp;
typedef __attribute__((address_space(1))) unsigned long long gu64;
typedef __attribute__((address_space(1))) unsigned gu32;
#define DI __device__ __forceinline__
#define RLX_AGENT __ATOMIC_RELAXED, __HIP_MEMORY_SCOPE_AGENT

constexpr int L = 2048, D = 640, P = 640, F = 1728;
constexpr int NSTEP = 94, NSTEP_F = 54;
constexpr size_t WS_EPOCH = 0;
constexpr size_t WS_FLAGW = 1024;
constexpr size_t WS_FLAGA = 4096;
constexpr size_t WS_GRAN = 8192;
constexpr size_t WS_STATS = WS_GRAN + 7 * 10 * 1024 + 2048;
constexpr size_t WS_ACT = 262144;
constexpr size_t ACT_BYTES = (size_t)32 * NSTEP * 4 * 1024;
constexpr size_t WS_W2T = WS_ACT + ACT_BYTES;
constexpr size_t W2T_BYTES = (size_t)8 * NSTEP * 5 * 1024;
constexpr size_t WS_STAMP = 32u << 20;
static_assert(WS_STATS + 2048 * 16 <= WS_ACT, "ws map");
constexpr int XSLOT = 32768, WSLOT = 20480, SLOT = XSLOT + WSLOT;
constexpr int LDS_PAR = 3 * SLOT;
constexpr int LDS1_BYTES = 163840;
constexpr int LDS_CS = SLOT;
constexpr int LDS_ST = SLOT + 4096;
constexpr int LDS_ET = SLOT + 8192;
constexpr int LDS_TP = SLOT + 16384;
constexpr int LDS_W2 = 40960;
constexpr int LDS_RT = 2 * SLOT;
static_assert(LDS1_BYTES <= 163840, "lds");
constexpr int RSRC3 = 0x00020000;

struct Params {
  const u16 *x, *lre, *lim, *bre, *bim, *cre, *cim, *ldt, *dsk, *w1, *w3, *w2;
  float* out;
  char* ws;
};

DI float bf2f(u16 v) { return __uint_as_float(((unsigned)v) << 16); }
DI float rbf(float f) { unsigned u = __float_as_uint(f); u += 0x7FFFu + ((u >> 16) & 1u); return __uint_as_float(u & 0xFFFF0000u); }
DI unsigned pack2(float a, float b) { f32x2 p = {a, b}; return __builtin_bit_cast(unsigned, __builtin_convertvector(p, bf16x2)); }
DI f32x4 mfma16(u32x4 a, u32x4 b, f32x4 c) { return __builtin_amdgcn_mfma_f32_16x16x32_bf16(__builtin_bit_cast(bf16x8, a), __builtin_bit_cast(bf16x8, b), c, 0, 0, 0); }
DI void mfma16_acc(f32x4& c, u32x4 a, u32x4 b) { asm volatile("v_mfma_f32_16x16x32_bf16 %0, %1, %2, %0" : "+v"(c) : "v"(a), "v"(b)); }
template <int OFF> DI u32x2 trrd(unsigned addr) { u32x2 r; asm volatile("ds_read_b64_tr_b16 %0, %1 offset:%2" : "=v"(r) : "v"(addr), "i"(OFF) : "memory"); return r; }
template <int OFF> DI u32x4 rd128(unsigned addr) { u32x4 r; asm volatile("ds_read_b128 %0, %1 offset:%2" : "=v"(r) : "v"(addr), "i"(OFF) : "memory"); return r; }
DI u32x4 cat(u32x2 a, u32x2 b) { return u32x4{a.x, a.y, b.x, b.y}; }
DI void dma16(__amdgpu_buffer_rsrc_t rs, char* lds, int voff, int soff) { __builtin_amdgcn_raw_ptr_buffer_load_lds(rs, (lds_vp)lds, 16, voff, soff, 0, 0); }
#define SB() __builtin_amdgcn_sched_barrier(0)
#define WAIT_V(n) asm volatile("s_waitcnt vmcnt(" #n ")" ::: "memory")
#define WAIT_L0() asm volatile("s_waitcnt lgkmcnt(0)" ::: "memory")
#define BAR() __builtin_amdgcn_s_barrier()
#define LBAR() __builtin_amdgcn_s_barrier()
#define TS(i) do {} while (0)
DI float wave_sum(float v) {
  v += __builtin_bit_cast(float, __builtin_amdgcn_update_dpp(0, __builtin_bit_cast(int, v), 0x111, 0xf, 0xf, true));
  v += __builtin_bit_cast(float, __builtin_amdgcn_update_dpp(0, __builtin_bit_cast(int, v), 0x112, 0xf, 0xf, true));
  v += __builtin_bit_cast(float, __builtin_amdgcn_update_dpp(0, __builtin_bit_cast(int, v), 0x114, 0xf, 0xf, true));
  v += __builtin_bit_cast(float, __builtin_amdgcn_update_dpp(0, __builtin_bit_cast(int, v), 0x118, 0xf, 0xf, true));
  const int iv = __builtin_bit_cast(int, v);
  const float a = __builtin_bit_cast(float, __builtin_amdgcn_readlane(iv, 15)), b = __builtin_bit_cast(float, __builtin_amdgcn_readlane(iv, 31));
  const float c = __builtin_bit_cast(float, __builtin_amdgcn_readlane(iv, 47)), d = __builtin_bit_cast(float, __builtin_amdgcn_readlane(iv, 63));
  return (a + b) + (c + d);
}
DI float silu_f(float g) { return g * __builtin_amdgcn_rcpf(1.0f + __builtin_amdgcn_exp2f(g * -1.44269504088896f)); }

template <int MODE> struct Frags {
  static constexpr int NT = MODE == 0 ? 6 : 4;
  u32x4 x[4];
  u32x2 lo[MODE == 2 ? 1 : NT], hi[MODE == 2 ? 1 : NT];
  u32x4 v[MODE == 2 ? NT : 1];
  template <int KK, int C> DI void rdw(unsigned aw) {
    if constexpr (MODE == 2) v[C] = rd128<C * 2048>(aw);
    else { constexpr int O = KK * 10240 + (C & 1) * 2560 + ((MODE == 0 ? 0 : 3) + (C >> 1)) * 32; lo[C] = trrd<O>(aw); hi[C] = trrd<O + 1280>(aw); }
  }
  template <int KK, int C> DI void rdw_lo(unsigned aw) {
    if constexpr (MODE == 2) v[C] = rd128<C * 2048>(aw);
    else { constexpr int O = KK * 10240 + (C & 1) * 2560 + ((MODE == 0 ? 0 : 3) + (C >> 1)) * 32; lo[C] = trrd<O>(aw); }
  }
  template <int KK, int C> DI void rdw_hi(unsigned aw) {
    if constexpr (MODE != 2) { constexpr int O = KK * 10240 + (C & 1) * 2560 + ((MODE == 0 ? 0 : 3) + (C >> 1)) * 32; hi[C] = trrd<O + 1280>(aw); }
  }
  template <int T> DI void rdx(unsigned ax) { x[T] = rd128<(MODE == 2 ? 512 : 2048) * T>(ax); }
  DI u32x4 w(int c) const { if constexpr (MODE == 2) return v[c]; else return cat(lo[c], hi[c]); }
  DI void wait() {
    if constexpr (MODE == 0) asm volatile("s_waitcnt lgkmcnt(0)" : "+v"(x[0]), "+v"(x[1]), "+v"(x[2]), "+v"(x[3]), "+v"(lo[0]), "+v"(hi[0]), "+v"(lo[1]), "+v"(hi[1]), "+v"(lo[2]), "+v"(hi[2]),
                                          "+v"(lo[3 % NT]), "+v"(hi[3 % NT]), "+v"(lo[4 % NT]), "+v"(hi[4 % NT]), "+v"(lo[5 % NT]), "+v"(hi[5 % NT]) :: "memory");
    else if constexpr (MODE == 1) asm volatile("s_waitcnt lgkmcnt(0)" : "+v"(x[0]), "+v"(x[1]), "+v"(x[2]), "+v"(x[3]), "+v"(lo[0]), "+v"(hi[0]), "+v"(lo[1]), "+v"(hi[1]), "+v"(lo[2]), "+v"(hi[2]), "+v"(lo[3]), "+v"(hi[3]) :: "memory");
    else asm volatile("s_waitcnt lgkmcnt(0)" : "+v"(x[0]), "+v"(x[1]), "+v"(x[2]), "+v"(x[3]), "+v"(v[0]), "+v"(v[1 % NT]), "+v"(v[2 % NT]), "+v"(v[3 % NT]) :: "memory");
  }
};

struct Ctx {
  __amdgpu_buffer_rsrc_t rx, rw;
  int vx[4];
  int vw[3];
  int xso_base, wso_base, wso_step;
  int rot;
  char* smem;
  int xdst, wdst;
};

template <int MODE, class H0, class H7, class HA, class H8, class H9>
DI void main_loop(const Params& p, const Ctx& cx, unsigned lbase, int w, int lane, f32x4 (&acc)[4][Frags<MODE>::NT], f32x4& csa, H0 hook0, H7 hook7, HA hookA, H8 hook8, H9 hook9) {
  constexpr bool SSM = MODE == 2;
  constexpr int NT = Frags<MODE>::NT;
  const int wr = w & 3, wc = w >> 2;
  const int G = lane >> 4, i16 = lane & 15;
  unsigned ax[2], aw[2];
  if (SSM) {
    const int row = 64 * wr + 16 * (i16 >> 2) + (i16 & 3), f = ((i16 & 3) >> 1) | ((i16 >> 2) << 1);
#pragma unroll
    for (int kk = 0; kk < 2; ++kk) ax[kk] = lbase + row * 128 + (((4 * kk + G) ^ f) << 4);
    const int f2 = (i16 >> 1) & 7;
#pragma unroll
    for (int kk = 0; kk < 2; ++kk) aw[kk] = lbase + XSLOT + (64 * wc + i16) * 128 + (((4 * kk + G) ^ f2) << 4);
  } else {
    const int row = 64 * wr + i16, f = (i16 >> 1) & 7;
#pragma unroll
    for (int kk = 0; kk < 2; ++kk) ax[kk] = lbase + row * 128 + (((4 * kk + G) ^ f) << 4);
    const int q = (lane >> 2) & 3, pp = lane & 3;
    aw[0] = aw[1] = lbase + XSLOT + (G >> 1) * 5120 + (4 * (G & 1) + q) * 160 + pp * 8;
  }
  auto kchunk = [&](int s) { int k = s + cx.rot; return k >= 10 ? k - 10 : k; };
  auto dma = [&](int slot, int sox, int sow, int i) {
    char* b = cx.smem + slot;
    if (i < 4) dma16(cx.rx, b + cx.xdst + i * 1024, cx.vx[i], sox + i * 10240);
    else if (SSM) { if (i < 6) dma16(cx.rw, b + XSLOT + cx.wdst + (i - 4) * 1024, cx.vw[i - 4], sow + (i - 4) * 10240); }
    else if (i < 6) dma16(cx.rw, b + XSLOT + cx.wdst + (i - 4) * 1024, cx.vw[i - 4], sow);
    else { if (lane < 32) dma16(cx.rw, b + XSLOT + cx.wdst + 2048, cx.vw[2], sow); }
  };
  Frags<MODE> fa, fb;
  fa.template rdx<0>(ax[0]); fa.template rdx<1>(ax[0]); fa.template rdx<2>(ax[0]); fa.template rdx<3>(ax[0]);
  fa.template rdw<0, 0>(aw[0]); fa.template rdw<0, 1>(aw[0]); fa.template rdw<0, 2>(aw[0]); fa.template rdw<0, 3>(aw[0]);
  if constexpr (NT > 4) { fa.template rdw<0, 4 % NT>(aw[0]); fa.template rdw<0, 5 % NT>(aw[0]); }

#define MM1(F_, C, T) acc[T][C] = SSM ? mfma16(F_.x[T], F_.w(C), acc[T][C]) : mfma16(F_.w(C), F_.x[T], acc[T][C])
#define MMG(F_, C) do { MM1(F_, C, 0); MM1(F_, C, 1); MM1(F_, C, 2); MM1(F_, C, 3); } while (0)
#define NREADS (SSM ? 8 : 4 + 2 * NT)
#define RD(N_, KK, I, WA, XA) do { \
    if constexpr ((I) < 4) N_.template rdx<(I) & 3>(XA); \
    else if constexpr (SSM) { if constexpr ((I) < 8) N_.template rdw_lo<KK, ((I) - 4) & 3>(WA); } \
    else if constexpr ((I) < 4 + 2 * NT) { if constexpr (((I) & 1) == 0) N_.template rdw_lo<KK, (((I) - 4) >> 1) % NT>(WA); else N_.template rdw_hi<KK, (((I) - 4) >> 1) % NT>(WA); } \
  } while (0)
#define GRP(F_, N_, KK, C, R0, WA, XA, EXTRA) do { \
    MM1(F_, C, 0); RD(N_, KK, (R0), WA, XA); SB(); \
    MM1(F_, C, 1); RD(N_, KK, (R0) + 1, WA, XA); SB(); \
    MM1(F_, C, 2); RD(N_, KK, (R0) + 2, WA, XA); SB(); \
    MM1(F_, C, 3); RD(N_, KK, (R0) + 3, WA, XA); EXTRA; SB(); } while (0)
#define CSEL1(F_, C) do { const unsigned v_ = (io_ == (C)) ? 0x3F803F80u : 0u; const u32x4 s_ = {v_, v_, v_, v_}; csa = SSM ? mfma16(s_, F_.w(C), csa) : mfma16(F_.w(C), s_, csa); } while (0)
#define CSELALL(F_, M) do { if (M) { int io_ = i16; asm volatile("" : "+v"(io_)); CSEL1(F_, 0); CSEL1(F_, 1); CSEL1(F_, 2); CSEL1(F_, 3); if constexpr (NT > 4) { CSEL1(F_, 4 % NT); CSEL1(F_, 5 % NT); } } SB(); } while (0)
  int slot = 0;
  auto stage = [&](const int s, auto dx_t, auto dw_t, auto bnd_t) __attribute__((always_inline)) {
    constexpr bool DX = decltype(dx_t)::value, DW = decltype(dw_t)::value;
    constexpr int BND = decltype(bnd_t)::value;
    const int nslot = slot == 2 * SLOT ? 0 : slot + SLOT;
    int sox = 0, sow = 0, pows = 0, pslot = 0;
    if constexpr (DX) { sox = cx.xso_base + kchunk(s + 3) * 128; asm volatile("" : "+s"(sox)); }
    if constexpr (DW) { pows = cx.wso_base + kchunk(s + 2) * cx.wso_step; pslot = nslot == 2 * SLOT ? 0 : nslot + SLOT; asm volatile("" : "+s"(pows), "+s"(pslot)); }
    fa.wait(); SB();
    {
      const unsigned X1 = ax[1] + slot, W1 = aw[1] + slot;
      GRP(fa, fb, 1, 0, 0, W1, X1, if constexpr (DW) dma(pslot, 0, pows, 4));
      GRP(fa, fb, 1, 1, 4, W1, X1, if constexpr (DW) dma(pslot, 0, pows, 5));
      GRP(fa, fb, 1, 2, 8, W1, X1, if constexpr (!SSM && DW) dma(pslot, 0, pows, 6));
      GRP(fa, fb, 1, 3, 12, W1, X1, );
      if constexpr (NT > 4) {
        GRP(fa, fb, 1, 4 % NT, 16, W1, X1, );
        GRP(fa, fb, 1, 5 % NT, 20, W1, X1, );
      }
      CSELALL(fa, ((2 * s) & 3) == wr);
      if constexpr (BND == 0) hook9(std::integral_constant<int, MODE>{});
    }
    fb.wait(); SB();
    const unsigned X0 = ax[0] + nslot, W0 = aw[0] + nslot;
    MMG(fb, 0); SB();
    if constexpr (BND == 1) { if (SSM) WAIT_V(6); else WAIT_V(7); LBAR(); }
    if constexpr (BND == 2) { WAIT_V(0); LBAR(); }
    GRP(fb, fa, 0, 1, 0, W0, X0, if constexpr (DX) dma(slot, sox, sow, 0));
    GRP(fb, fa, 0, 2, 4, W0, X0, if constexpr (DX) dma(slot, sox, sow, 1));
    GRP(fb, fa, 0, 3, 8, W0, X0, if constexpr (DX) dma(slot, sox, sow, 2));
    if constexpr (NT > 4) {
      GRP(fb, fa, 0, 4 % NT, 12, W0, X0, if constexpr (DX) dma(slot, sox, sow, 3));
      GRP(fb, fa, 0, 5 % NT, 16, W0, X0, );
    } else { if constexpr (DX) dma(slot, sox, sow, 3); }
    CSELALL(fb, ((2 * s + 1) & 3) == wr);
    slot = nslot;
  };
  using T_ = std::true_type; using F_ = std::false_type;
  using I0 = std::integral_constant<int, 0>; using I1 = std::integral_constant<int, 1>; using I2 = std::integral_constant<int, 2>;
  stage(0, T_{}, F_{}, I1{});
  hook0();
#pragma unroll 1
  for (int s = 1; s < 6; ++s) { stage(s, T_{}, T_{}, I1{}); if (s == 2) hook7(); }
  hookA();
  stage(6, T_{}, T_{}, I1{});
  hook8();
  stage(7, F_{}, T_{}, I1{});
  stage(8, F_{}, F_{}, I2{});
  stage(9, F_{}, F_{}, I0{});
  fa.wait();
}

constexpr int LWS = 0;
#define DWF_ 5
constexpr int DWF = DWF_;
constexpr int DW = 6;
struct P2Ctx { __amdgpu_buffer_rsrc_t rw; int sw, vo; };
DI void p2_ldw(const P2Ctx& c, u32x4 (&wv)[5], int k) {
#pragma unroll
  for (int j = 0; j < 4; ++j) wv[j] = __builtin_amdgcn_raw_buffer_load_b128(c.rw, c.vo + j * 1024, c.sw + k * 5120, 16);
  wv[4] = __builtin_amdgcn_raw_buffer_load_b128(c.rw, c.vo, c.sw + k * 5120 + 4096, 16);
}
template <bool SSM>
DI unsigned phase1(const Params& p, char* smem, int mt, int cj  , int blk, int rot, P2Ctx& p2c, u32x4 (&wv)[DW][5]) {
  int tid = threadIdx.x, lane = tid & 63;
  const int w = __builtin_amdgcn_readfirstlane(tid >> 6);
  const int wr = w & 3, wc = w >> 2;
  int G = lane >> 4, i16 = lane & 15;
  auto relane = [&]() { int tz = threadIdx.x; asm volatile("" : "+v"(tz)); tid = tz; lane = tz & 63; G = lane >> 4; i16 = lane & 15; p2c.vo = lane * 16; };
  const unsigned lbase = (unsigned)(size_t)(lds_cp)smem;
  TS(0);
  const int cbi = SSM ? 22 + cj : cj;
  u32x4 sr0, sr1; unsigned etag;
  {
    const __amdgpu_buffer_rsrc_t rs = __builtin_amdgcn_make_buffer_rsrc((void*)(p.x + (size_t)(256 * mt + 8 * cbi + w) * D), 0, D * 2, RSRC3);
    const int vo = lane * 16;
    asm volatile("buffer_load_dwordx4 %0, %3, %4, 0 offen\n\tbuffer_load_dwordx4 %1, %3, %4, 0 offen offset:1024\n\tglobal_load_dword %2, %5, off sc1"
                 : "=&v"(sr0), "=&v"(sr1), "=&v"(etag) : "v"(vo), "s"(rs), "v"(p.ws + WS_EPOCH) : "memory");
  }
  unsigned pa_ = 0, pb_ = 0, pc_ = 0;
  if (SSM) {
    const int ch = 64 * cj + lane;
    asm volatile("global_load_ushort %0, %3, off\n\tglobal_load_ushort %1, %4, off\n\tglobal_load_ushort %2, %5, off"
                 : "=&v"(pa_), "=&v"(pb_), "=&v"(pc_) : "v"(p.lre + ch), "v"(p.lim + ch), "v"(p.ldt + ch) : "memory");
  }
  Ctx cx;
  cx.smem = smem; cx.rot = rot;
  cx.rx = __builtin_amdgcn_make_buffer_rsrc((void*)(p.x + (size_t)(256 * mt + 32 * w) * D), 0, 32 * D * 2, RSRC3);
#pragma unroll
  for (int i = 0; i < 4; ++i) {
    int f;
    if (SSM) f = ((lane >> 4) & 1) | ((((2 * w + (i >> 1)) & 3)) << 1);
    else f = ((i & 1) << 2) | (lane >> 4);
    cx.vx[i] = (lane >> 3) * 1280 + (((lane & 7) ^ f) << 4);
  }
  cx.xso_base = 0; cx.xdst = w * 4096;
  if (SSM) {
    const int c = w & 3;
    cx.rw = __builtin_amdgcn_make_buffer_rsrc((void*)((c & 1) ? p.bim : p.bre), 0, P * D * 2, RSRC3);
    cx.vw[0] = (lane >> 3) * 1280 + (((lane & 7) ^ (lane >> 4)) << 4); cx.vw[1] = cx.vw[0] ^ 64; cx.vw[2] = 0;
    cx.wso_base = (64 * cj + 32 * (w >> 2) + 16 * (c >> 1)) * 1280; cx.wso_step = 128; cx.wdst = w * 2048;
  } else {
    const int u = w & 1;
    cx.rw = __builtin_amdgcn_make_buffer_rsrc((void*)(u ? p.w3 : p.w1), 0, D * F * 2, RSRC3);
#pragma unroll
    for (int i = 0; i < 3; ++i) {
      const int n = 64 * i + lane, pos = (n / 10) & 15, row = (pos & 3) | ((pos & 4) << 1) | ((pos & 8) >> 1);
      cx.vw[i] = row * 3456 + (n % 10) * 16 + cj * 160;
    }
    cx.wso_base = 16 * (w >> 1) * 3456; cx.wso_step = 64 * 3456; cx.wdst = (w >> 1) * 5120 + (w & 1) * 2560;
  }
  auto kchunk = [&](int s) { int k = s + rot; return k >= 10 ? k - 10 : k; };
  auto issue_stage = [&](int s, int slot) {
    const int sox = kchunk(s) * 128, sow = cx.wso_base + kchunk(s) * cx.wso_step;
    char* b = smem + slot;
#pragma unroll
    for (int i = 0; i < 4; ++i) dma16(cx.rx, b + cx.xdst + i * 1024, cx.vx[i], sox + i * 10240);
    dma16(cx.rw, b + XSLOT + cx.wdst, cx.vw[0], sow);
    dma16(cx.rw, b + XSLOT + cx.wdst + 1024, cx.vw[1], SSM ? sow + 10240 : sow);
    if (!SSM) { if (lane < 32) dma16(cx.rw, b + XSLOT + cx.wdst + 2048, cx.vw[2], sow); }
  };
  issue_stage(0, 0);
  TS(6);
  BAR();
  const int unit = w * 256 + blk;
  const bool has_unit = (w < 4) && (unit < 940);
  char* rt = smem + LDS_RT + (w & 3) * 4096;
  if (has_unit) {
    if (unit < 540) {
      const int s = unit / 10, dq = unit % 10;
      const __amdgpu_buffer_rsrc_t r2 = __builtin_amdgcn_make_buffer_rsrc((void*)p.w2, 0, F * D * 2, RSRC3);
      const int vo = (lane >> 3) * 1280 + (lane & 7) * 16;
#pragma unroll
      for (int i = 0; i < 4; ++i) dma16(r2, rt + i * 1024, vo, (32 * s + 8 * i) * 1280 + dq * 128);
    } else {
      const int u = unit - 540, c = u / 10, v = u % 10;
      const int vo = (lane & 15) * 1280 + (lane >> 4) * 16;
#pragma unroll
      for (int i = 0; i < 4; ++i) {
        const int sp = 4 * v + i, jg = sp >> 1, part = sp & 1;
        const __amdgpu_buffer_rsrc_t rc = __builtin_amdgcn_make_buffer_rsrc((void*)(part ? p.cim : p.cre), 0, D * P * 2, RSRC3);
        dma16(rc, rt + i * 1024, vo, 16 * c * 1280 + jg * 64);
      }
    }
  }
  issue_stage(1, SLOT);
#define SWAIT(N) asm volatile("s_waitcnt vmcnt(" #N ")" : "+v"(sr0), "+v"(sr1), "+v"(etag), "+v"(pa_), "+v"(pb_), "+v"(pc_) :: "memory")
  if (SSM) { if (has_unit) SWAIT(16); else SWAIT(12); } else { if (has_unit) SWAIT(18); else SWAIT(14); }
  {
    etag += 1u;
    float v[16];
#pragma unroll
    for (int i = 0; i < 4; ++i) { v[2 * i] = __uint_as_float(sr0[i] << 16); v[2 * i + 1] = __uint_as_float(sr0[i] & 0xFFFF0000u); v[8 + 2 * i] = __uint_as_float(sr1[i] << 16); v[9 + 2 * i] = __uint_as_float(sr1[i] & 0xFFFF0000u); }
    float sum = 0.f;
#pragma unroll
    for (int i = 0; i < 16; ++i) sum += v[i];
    sum = wave_sum(sum);
    const float mean = sum * (1.0f / D);
    float sq = 0.f;
#pragma unroll
    for (int i = 0; i < 8; ++i) { const float d = v[i] - mean; sq = fmaf(d, d, sq); }
    float sq2 = 0.f;
#pragma unroll
    for (int i = 8; i < 16; ++i) { const float d = v[i] - mean; sq2 = fmaf(d, d, sq2); }
    sq += lane < 16 ? sq2 : 0.f;
    sq = wave_sum(sq);
    const float rstd = 1.0f / sqrtf(sq * (1.0f / D) + 1e-5f);
    if (lane == 0) {
      gu64* g = (gu64*)(p.ws + WS_STATS + (size_t)(256 * mt + 8 * cbi + w) * 16);
      __hip_atomic_store(g, ((unsigned long long)etag << 32) | __float_as_uint(mean), RLX_AGENT);
      __hip_atomic_store(g + 1, ((unsigned long long)etag << 32) | __float_as_uint(rstd), RLX_AGENT);
    }
    if (SSM && w == 5) {
      const float a = bf2f((u16)pa_), b = bf2f((u16)pb_);
      const float dt = rbf(expf(bf2f((u16)pc_)));
      const float er = expf(dt * a);
      float sn, cn; sincosf(dt * b, &sn, &cn);
      const float lr = er * cn, li = er * sn;
      const float nr = lr - 1.0f, ni = li, den = a * a + b * b;
      const float cr = (nr * a + ni * b) / den, ci = (ni * a - nr * b) / den;
      float pr = lr, pi = li;
#pragma unroll
      for (int i = 0; i < 4; ++i) { float t = pr * pr - pi * pi; pi = 2.f * pr * pi; pr = t; }
      const float l16r = pr, l16i = pi;
#pragma unroll
      for (int i = 0; i < 4; ++i) { float t = pr * pr - pi * pi; pi = 2.f * pr * pi; pr = t; }
      float* par = (float*)(smem + LDS_PAR) + lane * 8;
      *(f32x4*)par = f32x4{lr, li, cr, ci};
      *(f32x4*)(par + 4) = f32x4{l16r, l16i, pr, pi};
    }
    }
  if (SSM) WAIT_V(8); else WAIT_V(9);
  TS(13);
  const __amdgpu_buffer_rsrc_t rW2T = __builtin_amdgcn_make_buffer_rsrc((void*)(p.ws + WS_W2T), 0, (int)W2T_BYTES, RSRC3);
  {
    u32x4 vv[4];
    const unsigned rtl = lbase + LDS_RT + (w & 3) * 4096;
    if (has_unit) {
      if (unit < 540) {
        const int g = lane >> 4, q = (lane >> 2) & 3, pp = lane & 3;
        const unsigned a0 = rtl + (8 * g + q) * 128 + 8 * pp;
        u32x2 l0 = trrd<0>(a0), h0 = trrd<512>(a0), l1 = trrd<32>(a0), h1 = trrd<512 + 32>(a0), l2 = trrd<64>(a0), h2 = trrd<512 + 64>(a0), l3 = trrd<96>(a0), h3 = trrd<512 + 96>(a0);
        asm volatile("s_waitcnt lgkmcnt(0)" : "+v"(l0), "+v"(h0), "+v"(l1), "+v"(h1), "+v"(l2), "+v"(h2), "+v"(l3), "+v"(h3) :: "memory");
        vv[0] = cat(l0, h0); vv[1] = cat(l1, h1); vv[2] = cat(l2, h2); vv[3] = cat(l3, h3);
      } else {
        u32x4 v0 = rd128<0>(rtl + lane * 16), v1 = rd128<1024>(rtl + lane * 16), v2 = rd128<2048>(rtl + lane * 16), v3 = rd128<3072>(rtl + lane * 16);
        asm volatile("s_waitcnt lgkmcnt(0)" : "+v"(v0), "+v"(v1), "+v"(v2), "+v"(v3) :: "memory");
        vv[0] = v0; vv[1] = v1; vv[2] = v2; vv[3] = v3;
      }
    }
    BAR();
    issue_stage(2, 2 * SLOT);
    if (has_unit) {
#pragma unroll
      for (int i = 0; i < 4; ++i) {
        int c, s;
        if (unit < 540) { s = unit / 10; c = 4 * (unit % 10) + i; } else { const int u = unit - 540; c = u / 10; s = NSTEP_F + 4 * (u % 10) + i; }
        __builtin_amdgcn_raw_buffer_store_b128(vv[i], rW2T, lane * 16, (((c / 5) * NSTEP + s) * 5 + (c % 5)) * 1024, 16);
      }
    }
  }
  TS(1);
  auto hook0 = [&]() {};
  auto hook7 = [&]() { if (tid == 0) __hip_atomic_store((gu32*)(p.ws + WS_FLAGW) + blk, etag, RLX_AGENT); };
  const __amdgpu_buffer_rsrc_t rACT = __builtin_amdgcn_make_buffer_rsrc((void*)(p.ws + WS_ACT), 0, (int)ACT_BYTES, RSRC3);
  const __amdgpu_buffer_rsrc_t rFW = __builtin_amdgcn_make_buffer_rsrc((void*)(p.ws + WS_FLAGW), 0, 1024, RSRC3);
  const int mt64 = 4 * mt + wr;
  gu64* stg;
  unsigned long long sta = 0, stb = 0;
  u32x4 fw = {0u, 0u, 0u, 0u};
  auto hookA = [&]() { if (w == 0) { const int vo_ = lane * 16; asm volatile("buffer_load_dwordx4 %0, %1, %2, 0 offen sc1" : "=&v"(fw) : "v"(vo_), "s"(rFW) : "memory"); } };
  auto hook8 = [&]() {
    if (w == 0) {
      if (SSM) asm volatile("s_waitcnt vmcnt(6)" : "+v"(fw) :: "memory"); else asm volatile("s_waitcnt vmcnt(7)" : "+v"(fw) :: "memory");
      unsigned spins = 0;
      while (!__all(fw.x == etag && fw.y == etag && fw.z == etag && fw.w == etag)) {
        if (++spins > (1u << 16)) break;
        __builtin_amdgcn_s_sleep(1);
        fw = __builtin_amdgcn_raw_buffer_load_b128(rFW, lane * 16, 0, 16);
      }
      if (blk == 0 && lane == 0) __hip_atomic_store((gu32*)(p.ws + WS_EPOCH), etag, RLX_AGENT);
    }
  };
  auto hook9 = [&](auto mode_tag) {
    int tz = threadIdx.x; asm volatile("" : "+v"(tz));
    stg = (gu64*)(p.ws + WS_STATS + (size_t)(256 * mt + 64 * wr + (tz & 63)) * 16);
    sta = __hip_atomic_load(stg, RLX_AGENT); stb = __hip_atomic_load(stg + 1, RLX_AGENT);
  };
  f32x4 csa = {0.f, 0.f, 0.f, 0.f};
  float* cst = (float*)(smem + LDS_CS);
  float* stt = (float*)(smem + LDS_ST);
  auto finish_common = [&](int ntw  , int tile0  ) {
    unsigned spins = 0;
    while (!__all((unsigned)(sta >> 32) == etag && (unsigned)(stb >> 32) == etag)) {
      if (++spins > (1u << 16)) break;
      __builtin_amdgcn_s_sleep(1);
      sta = __hip_atomic_load(stg, RLX_AGENT); stb = __hip_atomic_load(stg + 1, RLX_AGENT);
    }
    const float mean = __uint_as_float((unsigned)sta), rstd = __uint_as_float((unsigned)stb);
    if (wc == 0) *(f32x2*)(stt + (64 * wr + lane) * 2) = f32x2{rstd, -mean * rstd};
    if (SSM) { if (G == 0) {
#pragma unroll
        for (int c = 0; c < 4; ++c) cst[wr * 160 + (tile0 + c) * 16 + i16] = csa[c]; } }
    else { if (i16 < ntw) *(f32x4*)(cst + wr * 160 + (tile0 + i16) * 16 + 4 * G) = csa; }
    WAIT_L0();
    BAR();
  };
  auto cs4 = [&](int idx) { return (*(const f32x4*)(cst + idx) + *(const f32x4*)(cst + 160 + idx)) + (*(const f32x4*)(cst + 320 + idx) + *(const f32x4*)(cst + 480 + idx)); };
  auto cs1 = [&](int idx) { return (cst[idx] + cst[160 + idx]) + (cst[320 + idx] + cst[480 + idx]); };

  if (!SSM) {
    auto ffn = [&](auto mode_tag) {
      constexpr int MODE = decltype(mode_tag)::value;
      constexpr int NT = Frags<MODE>::NT, P0 = MODE == 0 ? 0 : 3;
      f32x4 acc[4][NT];
#pragma unroll
      for (int T = 0; T < 4; ++T)
#pragma unroll
        for (int c = 0; c < NT; ++c) acc[T][c] = f32x4{0.f, 0.f, 0.f, 0.f};
      main_loop<MODE>(p, cx, lbase, w, lane, acc, csa, hook0, hook7, hookA, hook8, hook9);
      TS(2);
      relane();
      finish_common(NT, 2 * P0);
      p2_ldw(p2c, wv[0], 6); p2_ldw(p2c, wv[1], 7);
      SB();
      f32x2 st4[4];
#pragma unroll
      for (int T = 0; T < 4; ++T) st4[T] = *(const f32x2*)(stt + (64 * wr + 16 * T + i16) * 2);
#pragma unroll
      for (int Pq = 0; Pq < NT / 2; ++Pq) {
        const int ft = 5 * cj + P0 + Pq;
        const f32x4 cg = cs4((2 * P0 + 2 * Pq) * 16 + 4 * G), cu = cs4((2 * P0 + 2 * Pq + 1) * 16 + 4 * G);
        unsigned pk[4][2];
#pragma unroll
        for (int T = 0; T < 4; ++T) {
          float hv[4];
#pragma unroll
          for (int e = 0; e < 4; ++e) {
            const float g = fmaf(acc[T][2 * Pq][e], st4[T].x, st4[T].y * cg[e]);
            const float u = fmaf(acc[T][2 * Pq + 1][e], st4[T].x, st4[T].y * cu[e]);
            hv[e] = silu_f(g) * u;
          }
          pk[T][0] = pack2(hv[0], hv[1]); pk[T][1] = pack2(hv[2], hv[3]);
        }
        if (ft < 108) {
#pragma unroll
          for (int T2 = 0; T2 < 4; T2 += 2) {
            const auto a = __builtin_amdgcn_permlane16_swap(pk[T2][0], pk[T2 + 1][0], false, false);
            const auto b = __builtin_amdgcn_permlane16_swap(pk[T2][1], pk[T2 + 1][1], false, false);
            const u32x4 v = {a[0], b[0], a[1], b[1]};
            const int s2 = ft >> 1, gp = 2 * (ft & 1) + (G >> 1), T = T2 + (G & 1);
            __builtin_amdgcn_raw_buffer_store_b128(v, rACT, T * 1024 + (gp * 16 + i16) * 16, (mt64 * NSTEP + s2) * 4096, 16);
          }
        }
      }
    };
    if (wc == 0) ffn(std::integral_constant<int, 0>{}); else ffn(std::integral_constant<int, 1>{});
  } else {
    f32x4 acc[4][4];
#pragma unroll
    for (int T = 0; T < 4; ++T)
#pragma unroll
      for (int c = 0; c < 4; ++c) acc[T][c] = f32x4{0.f, 0.f, 0.f, 0.f};
    main_loop<2>(p, cx, lbase, w, lane, acc, csa, hook0, hook7, hookA, hook8, hook9);
    TS(2);
    relane();
    const float* par = (const float*)(smem + LDS_PAR);
    float lr[2], li[2], cr[2], ci[2];
#pragma unroll
    for (int j = 0; j < 2; ++j) {
      const f32x4 pv = *(const f32x4*)(par + (32 * wc + 16 * j + i16) * 8);
      lr[j] = pv.x; li[j] = pv.y; cr[j] = pv.z; ci[j] = pv.w;
    }
    f32x4 pv = *(const f32x4*)(par + lane * 8 + 4);
    asm volatile("" : "+v"(pv), "+v"(lr[0]), "+v"(li[0]), "+v"(cr[0]), "+v"(ci[0]), "+v"(lr[1]), "+v"(li[1]), "+v"(cr[1]), "+v"(ci[1]));
    finish_common(4, 4 * wc);
    {
      float csl[4];
#pragma unroll
      for (int c = 0; c < 4; ++c) csl[c] = cs1((4 * wc + c) * 16 + i16);
#pragma unroll
      for (int T = 0; T < 4; ++T)
#pragma unroll
        for (int e = 0; e < 4; e += 2) {
          const f32x4 sv = *(const f32x4*)(stt + (64 * wr + 16 * G + 4 * T + e) * 2);
#pragma unroll
          for (int c = 0; c < 4; ++c) {
            acc[T][c][e] = fmaf(acc[T][c][e], sv.x, sv.y * csl[c]);
            acc[T][c][e + 1] = fmaf(acc[T][c][e + 1], sv.z, sv.w * csl[c]);
          }
        }
    }
    f32x2* et = (f32x2*)(smem + LDS_ET);
    const int seg = 4 * wr + G;
#pragma unroll
    for (int j = 0; j < 2; ++j) {
      float hr = 0.f, hi = 0.f;
#pragma unroll
      for (int i = 0; i < 16; ++i) {
        const float nr = fmaf(lr[j], hr, fmaf(-li[j], hi, acc[i >> 2][2 * j][i & 3]));
        const float ni = fmaf(lr[j], hi, fmaf(li[j], hr, acc[i >> 2][2 * j + 1][i & 3]));
        hr = nr; hi = ni;
      }
      et[seg * 64 + 32 * wc + 16 * j + i16] = f32x2{hr, hi};
    }
    WAIT_L0(); BAR();
    TS(3);
    if (w == 0) {
      const unsigned tag = etag;
      float xr_ = 0.f, xi_ = 0.f;
      f32x2 ev[16];
#pragma unroll
      for (int s = 0; s < 16; ++s) ev[s] = et[s * 64 + lane];
#pragma unroll
      for (int s = 0; s < 16; ++s) {
        const float nr = fmaf(pv.x, xr_, fmaf(-pv.y, xi_, ev[s].x));
        const float ni = fmaf(pv.x, xi_, fmaf(pv.y, xr_, ev[s].y));
        xr_ = nr; xi_ = ni;
      }
      gu64* gran = (gu64*)(p.ws + WS_GRAN);
      if (mt < 7) {
        gu64* g = gran + ((size_t)(mt * 10 + cj) * 64 + lane) * 2;
        __hip_atomic_store(g, ((unsigned long long)tag << 32) | __float_as_uint(xr_), RLX_AGENT);
        __hip_atomic_store(g + 1, ((unsigned long long)tag << 32) | __float_as_uint(xi_), RLX_AGENT);
      }
      float Hr = 0.f, Hi = 0.f;
      if (mt > 0) {
        unsigned long long ga[7], gb[7];
        unsigned spins = 0;
        while (true) {
          bool ok = true;
#pragma unroll
          for (int m = 0; m < 7; ++m) {
            if (m < mt) {
              gu64* g = gran + ((size_t)(m * 10 + cj) * 64 + lane) * 2;
              ga[m] = __hip_atomic_load(g, RLX_AGENT); gb[m] = __hip_atomic_load(g + 1, RLX_AGENT);
            } else { ga[m] = gb[m] = (unsigned long long)tag << 32; }
          }
#pragma unroll
          for (int m = 0; m < 7; ++m) ok = ok && ((unsigned)(ga[m] >> 32) == tag) && ((unsigned)(gb[m] >> 32) == tag);
          if (__all(ok)) break;
          if (++spins > (1u << 16)) break;
          __builtin_amdgcn_s_sleep(1);
        }
#pragma unroll
        for (int m = 0; m < 7; ++m) {
          if (m < mt) {
            const float sr = __uint_as_float((unsigned)ga[m]), si = __uint_as_float((unsigned)gb[m]);
            const float nr = fmaf(pv.z, Hr, fmaf(-pv.w, Hi, sr));
            const float ni = fmaf(pv.z, Hi, fmaf(pv.w, Hr, si));
            Hr = nr; Hi = ni;
          }
        }
      }
      xr_ = Hr; xi_ = Hi;
#pragma unroll
      for (int s = 0; s < 16; ++s) {
        et[s * 64 + lane] = f32x2{xr_, xi_};
        const float nr = fmaf(pv.x, xr_, fmaf(-pv.y, xi_, ev[s].x));
        const float ni = fmaf(pv.x, xi_, fmaf(pv.y, xr_, ev[s].y));
        xr_ = nr; xi_ = ni;
      }
    }
    WAIT_L0(); BAR();
    TS(4);
    p2_ldw(p2c, wv[0], 6); p2_ldw(p2c, wv[1], 7);
    SB();
    f32x2 x0[2];
#pragma unroll
    for (int j = 0; j < 2; ++j) x0[j] = et[seg * 64 + 32 * wc + 16 * j + i16];
    unsigned pre[2][8], pim[2][8];
#pragma unroll
    for (int j = 0; j < 2; ++j) {
      float hr = x0[j].x, hi = x0[j].y;
      float ore[16], oim[16];
#pragma unroll
      for (int i = 0; i < 16; ++i) {
        const float nr = fmaf(lr[j], hr, fmaf(-li[j], hi, acc[i >> 2][2 * j][i & 3]));
        const float ni = fmaf(lr[j], hi, fmaf(li[j], hr, acc[i >> 2][2 * j + 1][i & 3]));
        hr = nr; hi = ni;
        ore[i] = cr[j] * hr - ci[j] * hi;
        oim[i] = -(cr[j] * hi + ci[j] * hr);
      }
#pragma unroll
      for (int i = 0; i < 8; ++i) { pre[j][i] = pack2(ore[2 * i], ore[2 * i + 1]); pim[j][i] = pack2(oim[2 * i], oim[2 * i + 1]); }
    }
    {
      char* tp = smem + LDS_TP + w * 4608;
      const int q = (lane >> 2) & 3, pp = lane & 3;
      const unsigned tb = lbase + LDS_TP + w * 4608 + (8 * G + q) * 144 + 8 * pp;
      const int jg = 2 * cj + wc;
#pragma unroll
      for (int part = 0; part < 2; ++part) {
#pragma unroll
        for (int j = 0; j < 2; ++j) {
          char* t0 = tp + (16 * j + i16) * 144 + 32 * G;
          const unsigned* src = part ? pim[j] : pre[j];
          *(u32x4*)t0 = u32x4{src[0], src[1], src[2], src[3]};
          *(u32x4*)(t0 + 16) = u32x4{src[4], src[5], src[6], src[7]};
        }
        WAIT_L0();
        u32x2 lo[4], hi[4];
        lo[0] = trrd<0>(tb); hi[0] = trrd<576>(tb); lo[1] = trrd<32>(tb); hi[1] = trrd<576 + 32>(tb); lo[2] = trrd<64>(tb); hi[2] = trrd<576 + 64>(tb); lo[3] = trrd<96>(tb); hi[3] = trrd<576 + 96>(tb);
        asm volatile("s_waitcnt lgkmcnt(0)" : "+v"(lo[0]), "+v"(hi[0]), "+v"(lo[1]), "+v"(hi[1]), "+v"(lo[2]), "+v"(hi[2]), "+v"(lo[3]), "+v"(hi[3]) :: "memory");
        const int s2 = NSTEP_F + 2 * jg + part;
#pragma unroll
        for (int T = 0; T < 4; ++T)
          __builtin_amdgcn_raw_buffer_store_b128(cat(lo[T], hi[T]), rACT, T * 1024 + lane * 16, (mt64 * NSTEP + s2) * 4096, 16);
      }
    }
  }
  TS(5);
  asm volatile("" ::: "memory"); SB();
  p2_ldw(p2c, wv[2], 8); p2_ldw(p2c, wv[3], 3);
  SB();
  WAIT_V(10);
  BAR();
  if (tid == 0) __hip_atomic_store((gu32*)(p.ws + WS_FLAGA + mt * 128) + cbi, etag, RLX_AGENT);
  asm volatile("" ::: "memory"); SB();
  p2_ldw(p2c, wv[4], 4); p2_ldw(p2c, wv[5], 5);
  SB();
  {
    char* lw = smem + LDS_W2 + w * 15360 + lane * 16;
#pragma unroll
    for (int k = 0; k < 3; ++k)
#pragma unroll
      for (int c = 0; c < 5; ++c) *(u32x4*)(lw + k * 5120 + c * 1024) = wv[k][c];
  }
  SB();
  p2_ldw(p2c, wv[0], 0); p2_ldw(p2c, wv[1], 1); p2_ldw(p2c, wv[2], 2);
  SB();
  return etag;
}

DI void p2_run(const Params& p, const char* lw  , __amdgpu_buffer_rsrc_t ra, const P2Ctx& p2c, u32x4 (&wv)[DW][5],
               int sa, int N, int w, int lane, int mt2, int shA, int shW, unsigned etag, f32x4 (&acc)[4][5]) {
  constexpr int DA = 2, NMIN = 10, NMAX = 14;
  u32x4 a[DA][4];
  const int vo = p2c.vo;
  auto lda = [&](int k, int slot) {
#pragma unroll
    for (int T = 0; T < 4; ++T) a[slot][T] = __builtin_amdgcn_raw_buffer_load_b128(ra, vo + T * 1024, sa + k * 4096, 16);
  };
  {
    const int fi = w < 4 ? (lane < 22 ? lane : 0) : 22 + (lane < 10 ? lane : 0);
    gu32* fl = (gu32*)(p.ws + WS_FLAGA + mt2 * 128) + fi;
    unsigned spins = 0;
    while (!__all(__hip_atomic_load(fl, RLX_AGENT) == etag)) {
      if (++spins > (1u << 16)) break;
      __builtin_amdgcn_s_sleep(1);
    }
  }
  __builtin_amdgcn_fence(__ATOMIC_ACQUIRE, "wavefront");
  TS(9);
#pragma unroll
  for (int s = 0; s < DA - 1; ++s) lda(s, s);
#pragma unroll
  for (int T = 0; T < 4; ++T)
#pragma unroll
    for (int c = 0; c < 5; ++c) asm volatile("" : "+v"(acc[T][c]));
  asm volatile("s_nop 7" ::: "memory");
  SB();
#pragma unroll
  for (int s = 0; s < NMAX; ++s) {
    if (s < NMIN || s < N) {
      if (s + DA - 1 < NMIN || s + DA - 1 < N) lda(s + DA - 1, (s + DA - 1) % DA);
      SB();
#pragma unroll
      for (int T = 0; T < 4; ++T)
#pragma unroll
        for (int c = 0; c < 5; ++c) mfma16_acc(acc[T][c], wv[s % DW][c], a[s % DA][T]);
      SB();
      if (s < 3) {
#pragma unroll
        for (int c = 0; c < 5; ++c) wv[s][c] = *(const u32x4*)(lw + s * 5120 + c * 1024);
      } else if (s + DW < NMAX) { if (s + DW < NMIN || s + DW < N) p2_ldw(p2c, wv[s % DW], s + DW); }
      SB();
    }
  }
  asm volatile("s_nop 15\n\ts_nop 7" ::: "memory");
}

DI void phase2(const Params& p, char* smem, unsigned etag, const P2Ctx& p2c, u32x4 (&wv)[DW][5], int mt64, int nt, int s0) {
  const int tid = threadIdx.x, lane = tid & 63, w = __builtin_amdgcn_readfirstlane(tid >> 6);
  TS(8);
  f32x4 acc[4][5];
#pragma unroll
  for (int T = 0; T < 4; ++T)
#pragma unroll
    for (int c = 0; c < 5; ++c) acc[T][c] = f32x4{0.f, 0.f, 0.f, 0.f};
  const __amdgpu_buffer_rsrc_t ra = __builtin_amdgcn_make_buffer_rsrc((void*)(p.ws + WS_ACT), 0, (int)ACT_BYTES, RSRC3);
  const int sa = (mt64 * NSTEP + s0) * 4096;
  const int shA = nt & 3, shW = mt64 & 7;
  const int N = w < 2 ? 14 : (w < 4 ? 13 : 10);
  const char* lw = smem + LDS_W2 + w * 15360 + lane * 16;
  p2_run(p, lw, ra, p2c, wv, sa, N, w, lane, mt64 >> 2, shA, shW, etag, acc);
  auto part_addr = [&](int wv_, int t) { return t < 15 ? LDS_W2 + wv_ * (15360) + t * 1024 : wv_ * 5120 + (t - 15) * 1024; };
  {
    const int g = lane >> 4, r = lane & 15;
    const int pos = g * 16 + ((r + 4 * g) & 15);
#pragma unroll
    for (int T = 0; T < 4; ++T)
#pragma unroll
      for (int c = 0; c < 5; ++c) *(f32x4*)(smem + part_addr(w, T * 5 + c) + pos * 16) = acc[T][c];
  }
  f32x4 res[3]; u32x2 xv[3], dv[3]; unsigned long long ga[3], gb[3];
#pragma unroll
  for (int it = 0; it < 3; ++it) {
    const int item = tid + 512 * it;
    if (item < 1280) {
      const int tok = item / 20, dg = item % 20;
      const int gt = mt64 * 64 + tok, d0 = nt * 80 + dg * 4;
      gu64* sg = (gu64*)(p.ws + WS_STATS + (size_t)gt * 16);
      ga[it] = __hip_atomic_load(sg, RLX_AGENT); gb[it] = __hip_atomic_load(sg + 1, RLX_AGENT);
      xv[it] = *(const u32x2*)(p.x + (size_t)gt * D + d0);
      dv[it] = *(const u32x2*)(p.dsk + d0);
    }
  }
  __syncthreads();
  TS(10);
#pragma unroll
  for (int it = 0; it < 3; ++it) {
    const int item = tid + 512 * it;
    if (item < 1280) {
      const int tok = item / 20, dg = item % 20, c = dg >> 2, g = dg & 3, T = tok >> 4, rr = tok & 15;
      const int t = T * 5 + c, off = (g * 16 + ((rr + 4 * g) & 15)) * 16;
      const int gt = mt64 * 64 + tok, d0 = nt * 80 + dg * 4;
      const int ta = t < 15 ? LDS_W2 + t * 1024 : (t - 15) * 1024, st = t < 15 ? 15360 : 5120;
      const char* base = smem + ta + off;
      f32x4 o = *(const f32x4*)(base);
#pragma unroll
      for (int k = 1; k < 4; ++k) o += *(const f32x4*)(base + k * st);
      f32x4 y = *(const f32x4*)(base + 4 * st);
#pragma unroll
      for (int k = 5; k < 8; ++k) y += *(const f32x4*)(base + k * st);
      const float mean = __uint_as_float((unsigned)ga[it]), rstd = __uint_as_float((unsigned)gb[it]);
      const float xs[4] = {__uint_as_float(xv[it].x << 16), __uint_as_float(xv[it].x & 0xFFFF0000u), __uint_as_float(xv[it].y << 16), __uint_as_float(xv[it].y & 0xFFFF0000u)};
      const float ds[4] = {__uint_as_float(dv[it].x << 16), __uint_as_float(dv[it].x & 0xFFFF0000u), __uint_as_float(dv[it].y << 16), __uint_as_float(dv[it].y & 0xFFFF0000u)};
#pragma unroll
      for (int e = 0; e < 4; ++e) {
        const float xn = (xs[e] - mean) * rstd;
        const float yy = fmaf(2.0f, y[e], xn * ds[e]);
        res[it][e] = fmaf(yy, silu_f(o[e]), xs[e]);
      }
      *(f32x4*)(p.out + (size_t)gt * D + d0) = res[it];
    }
  }
  TS(11);
}

__global__ __launch_bounds__(512, 2) void k_fused(Params p) {
  extern __shared__ __attribute__((aligned(16))) char smem[];
  const int b = blockIdx.x, xcd = b & 7, i = b >> 3;
  const int ml = i & 3, cl = i >> 2;
  const int mt = (xcd & 1) * 4 + ml, cb = (xcd >> 1) * 8 + cl;
  const int rot = 0;
  const int w = __builtin_amdgcn_readfirstlane(threadIdx.x >> 6), lane = threadIdx.x & 63;
  const int mt64 = (xcd >> 1) * 8 + (i & 7), nt = (xcd & 1) * 4 + (i >> 3);
  const int s0 = w < 4 ? (w < 2 ? 14 * w : 28 + 13 * (w - 2)) : 54 + 10 * (w - 4);
  P2Ctx p2c;
  p2c.rw = __builtin_amdgcn_make_buffer_rsrc((void*)(p.ws + WS_W2T), 0, (int)W2T_BYTES, RSRC3);
  p2c.sw = (nt * NSTEP + s0) * 5120; p2c.vo = 0;
  u32x4 wv[DW][5];
  unsigned etag;
  if (cb < 22) etag = phase1<false>(p, smem, mt, cb, b, rot, p2c, wv);
  else etag = phase1<true>(p, smem, mt, cb - 22, b, rot, p2c, wv);
  phase2(p, smem, etag, p2c, wv, mt64, nt, s0);
}
}

extern "C" void kernel_launch(void* const* d_in, const int* in_sizes, int n_in, void* d_out, int out_size, void* d_ws, size_t ws_size, hipStream_t stream) {
  using namespace s5;
  Params p;
  p.x = (const u16*)d_in[0]; p.lre = (const u16*)d_in[1]; p.lim = (const u16*)d_in[2];
  p.bre = (const u16*)d_in[3]; p.bim = (const u16*)d_in[4]; p.cre = (const u16*)d_in[5]; p.cim = (const u16*)d_in[6];
  p.ldt = (const u16*)d_in[7]; p.dsk = (const u16*)d_in[8];
  p.w1 = (const u16*)d_in[10]; p.w3 = (const u16*)d_in[11]; p.w2 = (const u16*)d_in[12];
  p.out = (float*)d_out; p.ws = (char*)d_ws;
  (void)hipFuncSetAttribute((const void*)k_fused, hipFuncAttributeMaxDynamicSharedMemorySize, LDS1_BYTES);
  k_fused<<<256, 512, LDS1_BYTES, stream>>>(p);
}
```

```cpp
#include <hip/hip_runtime.h>
#include <stdint.h>
#include <type_traits>

namespace s5 {
typedef unsigned short u16;
typedef short bf16x8 __attribute__((ext_vector_type(8)));
typedef float f32x4 __attribute__((ext_vector_type(4)));
typedef float f32x2 __attribute__((ext_vector_type(2)));
typedef unsigned u32x4 __attribute__((ext_vector_type(4)));
typedef unsigned u32x2 __attribute__((ext_vector_type(2)));
typedef __bf16 bf16x2 __attribute__((ext_vector_type(2)));
typedef __attribute__((address_space(3))) void* lds_vp;
typedef __attribute__((address_space(3))) char* lds_cp;
typedef __attribute__((address_space(1))) unsigned long long gu64;
typedef __attribute__((address_space(1))) unsigned gu32;
#define DI __device__ __forceinline__
#define RLX_AGENT __ATOMIC_RELAXED, __HIP_MEMORY_SCOPE_AGENT

constexpr int L = 2048, D = 640, P = 640, F = 1728;
constexpr int NSTEP = 94, NSTEP_F = 54;
constexpr size_t WS_EPOCH = 0;
constexpr size_t WS_FLAGW = 1024;
constexpr size_t WS_FLAGA = 4096;
constexpr size_t WS_GRAN = 8192;
constexpr size_t WS_STATS = WS_GRAN + 7 * 10 * 1024 + 2048;
constexpr size_t WS_ACT = 262144;
constexpr size_t ACT_BYTES = (size_t)32 * NSTEP * 4 * 1024;
constexpr size_t WS_W2T = WS_ACT + ACT_BYTES;
constexpr size_t W2T_BYTES = (size_t)8 * NSTEP * 5 * 1024;
constexpr size_t WS_STAMP = 32u << 20;
static_assert(WS_STATS + 2048 * 16 <= WS_ACT, "ws map");
constexpr int XSLOT = 32768, WSLOT = 20480, SLOT = XSLOT + WSLOT;
constexpr int LDS_PAR = 3 * SLOT;
constexpr int LDS1_BYTES = 163840;
constexpr int LDS_CS = SLOT;
constexpr int LDS_ST = SLOT + 4096;
constexpr int LDS_ET = SLOT + 8192;
constexpr int LDS_TP = SLOT + 16384;
constexpr int LDS_W2 = 40960;
constexpr int LDS_RT = 2 * SLOT;
static_assert(LDS1_BYTES <= 163840, "lds");
constexpr int RSRC3 = 0x00020000;
constexpr unsigned SPIN_MAX = 1u << 21;

struct Params {
  const u16 *x, *lre, *lim, *bre, *bim, *cre, *cim, *ldt, *dsk, *w1, *w3, *w2;
  float* out;
  char* ws;
};

DI float bf2f(u16 v) { return __uint_as_float(((unsigned)v) << 16); }
DI float rbf(float f) { unsigned u = __float_as_uint(f); u += 0x7FFFu + ((u >> 16) & 1u); return __uint_as_float(u & 0xFFFF0000u); }
DI unsigned pack2(float a, float b) { f32x2 p = {a, b}; return __builtin_bit_cast(unsigned, __builtin_convertvector(p, bf16x2)); }
DI f32x4 mfma16(u32x4 a, u32x4 b, f32x4 c) { return __builtin_amdgcn_mfma_f32_16x16x32_bf16(__builtin_bit_cast(bf16x8, a), __builtin_bit_cast(bf16x8, b), c, 0, 0, 0); }
DI void mfma16_acc(f32x4& c, u32x4 a, u32x4 b) { asm volatile("v_mfma_f32_16x16x32_bf16 %0, %1, %2, %0" : "+v"(c) : "v"(a), "v"(b)); }
template <int OFF> DI u32x2 trrd(unsigned addr) { u32x2 r; asm volatile("ds_read_b64_tr_b16 %0, %1 offset:%2" : "=v"(r) : "v"(addr), "i"(OFF) : "memory"); return r; }
template <int OFF> DI u32x4 rd128(unsigned addr) { u32x4 r; asm volatile("ds_read_b128 %0, %1 offset:%2" : "=v"(r) : "v"(addr), "i"(OFF) : "memory"); return r; }
DI u32x4 cat(u32x2 a, u32x2 b) { return u32x4{a.x, a.y, b.x, b.y}; }
DI void dma16(__amdgpu_buffer_rsrc_t rs, char* lds, int voff, int soff) { __builtin_amdgcn_raw_ptr_buffer_load_lds(rs, (lds_vp)lds, 16, voff, soff, 0, 0); }
DI void st_sc1(u32x4 v, __amdgpu_buffer_rsrc_t rs, int off) { __builtin_amdgcn_raw_buffer_store_b128(v, rs, off, 0, 16); }
#define SB() __builtin_amdgcn_sched_barrier(0)
#define WAIT_V(n) asm volatile("s_waitcnt vmcnt(" #n ")" ::: "memory")
#define WAIT_L0() asm volatile("s_waitcnt lgkmcnt(0)" ::: "memory")
#define BAR() __builtin_amdgcn_s_barrier()
#define LBAR() __builtin_amdgcn_s_barrier()
#define TS(i) do {} while (0)
DI float wave_sum(float v) {
  v += __builtin_bit_cast(float, __builtin_amdgcn_update_dpp(0, __builtin_bit_cast(int, v), 0x111, 0xf, 0xf, true));
  v += __builtin_bit_cast(float, __builtin_amdgcn_update_dpp(0, __builtin_bit_cast(int, v), 0x112, 0xf, 0xf, true));
  v += __builtin_bit_cast(float, __builtin_amdgcn_update_dpp(0, __builtin_bit_cast(int, v), 0x114, 0xf, 0xf, true));
  v += __builtin_bit_cast(float, __builtin_amdgcn_update_dpp(0, __builtin_bit_cast(int, v), 0x118, 0xf, 0xf, true));
  const int iv = __builtin_bit_cast(int, v);
  const float a = __builtin_bit_cast(float, __builtin_amdgcn_readlane(iv, 15)), b = __builtin_bit_cast(float, __builtin_amdgcn_readlane(iv, 31));
  const float c = __builtin_bit_cast(float, __builtin_amdgcn_readlane(iv, 47)), d = __builtin_bit_cast(float, __builtin_amdgcn_readlane(iv, 63));
  return (a + b) + (c + d);
}
DI float silu_f(float g) { return g * __builtin_amdgcn_rcpf(1.0f + __builtin_amdgcn_exp2f(g * -1.44269504088896f)); }

template <int MODE> struct Frags {
  static constexpr int NT = MODE == 0 ? 6 : 4;
  u32x4 x[4];
  u32x2 lo[MODE == 2 ? 1 : NT], hi[MODE == 2 ? 1 : NT];
  u32x4 v[MODE == 2 ? NT : 1];
  template <int KK, int C> DI void rdw(unsigned aw) {
    if constexpr (MODE == 2) v[C] = rd128<C * 2048>(aw);
    else { constexpr int O = KK * 10240 + (C & 1) * 2560 + ((MODE == 0 ? 0 : 3) + (C >> 1)) * 32; lo[C] = trrd<O>(aw); hi[C] = trrd<O + 1280>(aw); }
  }
  template <int KK, int C> DI void rdw_lo(unsigned aw) {
    if constexpr (MODE == 2) v[C] = rd128<C * 2048>(aw);
    else { constexpr int O = KK * 10240 + (C & 1) * 2560 + ((MODE == 0 ? 0 : 3) + (C >> 1)) * 32; lo[C] = trrd<O>(aw); }
  }
  template <int KK, int C> DI void rdw_hi(unsigned aw) {
    if constexpr (MODE != 2) { constexpr int O = KK * 10240 + (C & 1) * 2560 + ((MODE == 0 ? 0 : 3) + (C >> 1)) * 32; hi[C] = trrd<O + 1280>(aw); }
  }
  template <int T> DI void rdx(unsigned ax) { x[T] = rd128<(MODE == 2 ? 512 : 2048) * T>(ax); }
  DI u32x4 w(int c) const { if constexpr (MODE == 2) return v[c]; else return cat(lo[c], hi[c]); }
  DI void wait() {
    if constexpr (MODE == 0) asm volatile("s_waitcnt lgkmcnt(0)" : "+v"(x[0]), "+v"(x[1]), "+v"(x[2]), "+v"(x[3]), "+v"(lo[0]), "+v"(hi[0]), "+v"(lo[1]), "+v"(hi[1]), "+v"(lo[2]), "+v"(hi[2]),
                                          "+v"(lo[3 % NT]), "+v"(hi[3 % NT]), "+v"(lo[4 % NT]), "+v"(hi[4 % NT]), "+v"(lo[5 % NT]), "+v"(hi[5 % NT]) :: "memory");
    else if constexpr (MODE == 1) asm volatile("s_waitcnt lgkmcnt(0)" : "+v"(x[0]), "+v"(x[1]), "+v"(x[2]), "+v"(x[3]), "+v"(lo[0]), "+v"(hi[0]), "+v"(lo[1]), "+v"(hi[1]), "+v"(lo[2]), "+v"(hi[2]), "+v"(lo[3]), "+v"(hi[3]) :: "memory");
    else asm volatile("s_waitcnt lgkmcnt(0)" : "+v"(x[0]), "+v"(x[1]), "+v"(x[2]), "+v"(x[3]), "+v"(v[0]), "+v"(v[1 % NT]), "+v"(v[2 % NT]), "+v"(v[3 % NT]) :: "memory");
  }
};

struct Ctx {
  __amdgpu_buffer_rsrc_t rx, rw;
  int vx[4];
  int vw[3];
  int xso_base, wso_base, wso_step;
  int rot;
  char* smem;
  int xdst, wdst;
};

template <int MODE, class H0, class H7, class HA, class H8, class H9>
DI void main_loop(const Params& p, const Ctx& cx, unsigned lbase, int w, int lane, f32x4 (&acc)[4][Frags<MODE>::NT], f32x4& csa, H0 hook0, H7 hook7, HA hookA, H8 hook8, H9 hook9) {
  constexpr bool SSM = MODE == 2;
  constexpr int NT = Frags<MODE>::NT;
  const int wr = w & 3, wc = w >> 2;
  const int G = lane >> 4, i16 = lane & 15;
  unsigned ax[2], aw[2];
  if (SSM) {
    const int row = 64 * wr + 16 * (i16 >> 2) + (i16 & 3), f = ((i16 & 3) >> 1) | ((i16 >> 2) << 1);
#pragma unroll
    for (int kk = 0; kk < 2; ++kk) ax[kk] = lbase + row * 128 + (((4 * kk + G) ^ f) << 4);
    const int f2 = (i16 >> 1) & 7;
#pragma unroll
    for (int kk = 0; kk < 2; ++kk) aw[kk] = lbase + XSLOT + (64 * wc + i16) * 128 + (((4 * kk + G) ^ f2) << 4);
  } else {
    const int row = 64 * wr + i16, f = (i16 >> 1) & 7;
#pragma unroll
    for (int kk = 0; kk < 2; ++kk) ax[kk] = lbase + row * 128 + (((4 * kk + G) ^ f) << 4);
    const int q = (lane >> 2) & 3, pp = lane & 3;
    aw[0] = aw[1] = lbase + XSLOT + (G >> 1) * 5120 + (4 * (G & 1) + q) * 160 + pp * 8;
  }
  auto kchunk = [&](int s) { int k = s + cx.rot; return k >= 10 ? k - 10 : k; };
  auto dma = [&](int slot, int sox, int sow, int i) {
    char* b = cx.smem + slot;
    if (i < 4) dma16(cx.rx, b + cx.xdst + i * 1024, cx.vx[i], sox + i * 10240);
    else if (SSM) { if (i < 6) dma16(cx.rw, b + XSLOT + cx.wdst + (i - 4) * 1024, cx.vw[i - 4], sow + (i - 4) * 10240); }
    else if (i < 6) dma16(cx.rw, b + XSLOT + cx.wdst + (i - 4) * 1024, cx.vw[i - 4], sow);
    else { if (lane < 32) dma16(cx.rw, b + XSLOT + cx.wdst + 2048, cx.vw[2], sow); }
  };
  Frags<MODE> fa, fb;
  fa.template rdx<0>(ax[0]); fa.template rdx<1>(ax[0]); fa.template rdx<2>(ax[0]); fa.template rdx<3>(ax[0]);
  fa.template rdw<0, 0>(aw[0]); fa.template rdw<0, 1>(aw[0]); fa.template rdw<0, 2>(aw[0]); fa.template rdw<0, 3>(aw[0]);
  if constexpr (NT > 4) { fa.template rdw<0, 4 % NT>(aw[0]); fa.template rdw<0, 5 % NT>(aw[0]); }

#define MM1(F_, C, T) acc[T][C] = SSM ? mfma16(F_.x[T], F_.w(C), acc[T][C]) : mfma16(F_.w(C), F_.x[T], acc[T][C])
#define MMG(F_, C) do { MM1(F_, C, 0); MM1(F_, C, 1); MM1(F_, C, 2); MM1(F_, C, 3); } while (0)
#define NREADS (SSM ? 8 : 4 + 2 * NT)
#define RD(N_, KK, I, WA, XA) do { \
    if constexpr ((I) < 4) N_.template rdx<(I) & 3>(XA); \
    else if constexpr (SSM) { if constexpr ((I) < 8) N_.template rdw_lo<KK, ((I) - 4) & 3>(WA); } \
    else if constexpr ((I) < 4 + 2 * NT) { if constexpr (((I) & 1) == 0) N_.template rdw_lo<KK, (((I) - 4) >> 1) % NT>(WA); else N_.template rdw_hi<KK, (((I) - 4) >> 1) % NT>(WA); } \
  } while (0)
#define GRP(F_, N_, KK, C, R0, WA, XA, EXTRA) do { \
    MM1(F_, C, 0); RD(N_, KK, (R0), WA, XA); SB(); \
    MM1(F_, C, 1); RD(N_, KK, (R0) + 1, WA, XA); SB(); \
    MM1(F_, C, 2); RD(N_, KK, (R0) + 2, WA, XA); SB(); \
    MM1(F_, C, 3); RD(N_, KK, (R0) + 3, WA, XA); EXTRA; SB(); } while (0)
#define CSEL1(F_, C) do { const unsigned v_ = (io_ == (C)) ? 0x3F803F80u : 0u; const u32x4 s_ = {v_, v_, v_, v_}; csa = SSM ? mfma16(s_, F_.w(C), csa) : mfma16(F_.w(C), s_, csa); } while (0)
#define CSELALL(F_, M) do { if (M) { int io_ = i16; asm volatile("" : "+v"(io_)); CSEL1(F_, 0); CSEL1(F_, 1); CSEL1(F_, 2); CSEL1(F_, 3); if constexpr (NT > 4) { CSEL1(F_, 4 % NT); CSEL1(F_, 5 % NT); } } SB(); } while (0)
  int slot = 0;
  auto stage = [&](const int s, auto dx_t, auto dw_t, auto bnd_t) __attribute__((always_inline)) {
    constexpr bool DX = decltype(dx_t)::value, DW = decltype(dw_t)::value;
    constexpr int BND = decltype(bnd_t)::value;
    const int nslot = slot == 2 * SLOT ? 0 : slot + SLOT;
    int sox = 0, sow = 0, pows = 0, pslot = 0;
    if constexpr (DX) { sox = cx.xso_base + kchunk(s + 3) * 128; asm volatile("" : "+s"(sox)); }
    if constexpr (DW) { pows = cx.wso_base + kchunk(s + 2) * cx.wso_step; pslot = nslot == 2 * SLOT ? 0 : nslot + SLOT; asm volatile("" : "+s"(pows), "+s"(pslot)); }
    fa.wait(); SB();
    {
      const unsigned X1 = ax[1] + slot, W1 = aw[1] + slot;
      GRP(fa, fb, 1, 0, 0, W1, X1, if constexpr (DW) dma(pslot, 0, pows, 4));
      GRP(fa, fb, 1, 1, 4, W1, X1, if constexpr (DW) dma(pslot, 0, pows, 5));
      GRP(fa, fb, 1, 2, 8, W1, X1, if constexpr (!SSM && DW) dma(pslot, 0, pows, 6));
      GRP(fa, fb, 1, 3, 12, W1, X1, );
      if constexpr (NT > 4) {
        GRP(fa, fb, 1, 4 % NT, 16, W1, X1, );
        GRP(fa, fb, 1, 5 % NT, 20, W1, X1, );
      }
      CSELALL(fa, ((2 * s) & 3) == wr);
      if constexpr (BND == 0) hook9(std::integral_constant<int, MODE>{});
    }
    fb.wait(); SB();
    const unsigned X0 = ax[0] + nslot, W0 = aw[0] + nslot;
    MMG(fb, 0); SB();
    if constexpr (BND == 1) { if (SSM) WAIT_V(6); else WAIT_V(7); LBAR(); }
    if constexpr (BND == 2) { WAIT_V(0); LBAR(); }
    if constexpr (BND == 3) {
      if (w < 4) { if (SSM) WAIT_V(14); else WAIT_V(16); } else WAIT_V(0);
      LBAR();
    }
    GRP(fb, fa, 0, 1, 0, W0, X0, if constexpr (DX) dma(slot, sox, sow, 0));
    GRP(fb, fa, 0, 2, 4, W0, X0, if constexpr (DX) dma(slot, sox, sow, 1));
    GRP(fb, fa, 0, 3, 8, W0, X0, if constexpr (DX) dma(slot, sox, sow, 2));
    if constexpr (NT > 4) {
      GRP(fb, fa, 0, 4 % NT, 12, W0, X0, if constexpr (DX) dma(slot, sox, sow, 3));
      GRP(fb, fa, 0, 5 % NT, 16, W0, X0, );
    } else { if constexpr (DX) dma(slot, sox, sow, 3); }
    CSELALL(fb, ((2 * s + 1) & 3) == wr);
    slot = nslot;
  };
  using T_ = std::true_type; using F_ = std::false_type;
  using I0 = std::integral_constant<int, 0>; using I1 = std::integral_constant<int, 1>; using I2 = std::integral_constant<int, 2>;
  stage(0, T_{}, F_{}, std::integral_constant<int, 3>{});
  hook7();
#pragma unroll 1
  for (int s = 1; s < 6; ++s) stage(s, T_{}, T_{}, I1{});
  hookA();
  stage(6, T_{}, T_{}, I1{});
  hook8();
  stage(7, F_{}, T_{}, I1{});
  stage(8, F_{}, F_{}, I2{});
  stage(9, F_{}, F_{}, I0{});
  fa.wait();
}

constexpr int LWS = 0;
#define DWF_ 5
constexpr int DWF = DWF_;
constexpr int DW = 6;
struct P2Ctx { __amdgpu_buffer_rsrc_t rw; int sw, vo; };
DI void p2_ldw(const P2Ctx& c, u32x4 (&wv)[5], int k) {
#pragma unroll
  for (int j = 0; j < 5; ++j) wv[j] = __builtin_amdgcn_raw_buffer_load_b128(c.rw, c.vo, c.sw + k * 5120 + j * 1024, 16);
}
template <bool SSM>
DI unsigned phase1(const Params& p, char* smem, int mt, int cj  , int blk, int rot, P2Ctx& p2c, u32x4 (&wv)[DW][5]) {
  int tid = threadIdx.x, lane = tid & 63;
  const int w = __builtin_amdgcn_readfirstlane(tid >> 6);
  const int wr = w & 3, wc = w >> 2;
  int G = lane >> 4, i16 = lane & 15;
  auto relane = [&]() { int tz = threadIdx.x; asm volatile("" : "+v"(tz)); tid = tz; lane = tz & 63; G = lane >> 4; i16 = lane & 15; p2c.vo = lane * 16; };
  const unsigned lbase = (unsigned)(size_t)(lds_cp)smem;
  TS(0);
  const int cbi = SSM ? 22 + cj : cj;
  unsigned etag;
  Ctx cx;
  cx.smem = smem; cx.rot = rot;
  cx.rx = __builtin_amdgcn_make_buffer_rsrc((void*)(p.x + (size_t)(256 * mt) * D), 0, 256 * D * 2, RSRC3);
#pragma unroll
  for (int i = 0; i < 4; ++i) {
    int f;
    if (SSM) f = ((lane >> 4) & 1) | ((((2 * w + (i >> 1)) & 3)) << 1);
    else f = ((i & 1) << 2) | (lane >> 4);
    cx.vx[i] = (lane >> 3) * 1280 + (((lane & 7) ^ f) << 4);
  }
  cx.xso_base = w * 32 * 1280; cx.xdst = w * 4096;
  auto wbase = [&](int v) { return SSM ? (64 * cj + 32 * (v >> 2) + 16 * ((v & 3) >> 1)) * 1280 : 16 * (v >> 1) * 3456; };
  auto wdst = [&](int v) { return SSM ? v * 2048 : (v >> 1) * 5120 + (v & 1) * 2560; };
  if (SSM) {
    cx.rw = __builtin_amdgcn_make_buffer_rsrc((void*)((w & 1) ? p.bim : p.bre), 0, P * D * 2, RSRC3);
    cx.vw[0] = (lane >> 3) * 1280 + (((lane & 7) ^ (lane >> 4)) << 4); cx.vw[1] = cx.vw[0] ^ 64; cx.vw[2] = 0;
    cx.wso_step = 128;
  } else {
    cx.rw = __builtin_amdgcn_make_buffer_rsrc((void*)((w & 1) ? p.w3 : p.w1), 0, D * F * 2, RSRC3);
#pragma unroll
    for (int i = 0; i < 3; ++i) {
      const int n = 64 * i + lane, pos = (n / 10) & 15, row = (pos & 3) | ((pos & 4) << 1) | ((pos & 8) >> 1);
      cx.vw[i] = row * 3456 + (n % 10) * 16 + cj * 160;
    }
    cx.wso_step = 64 * 3456;
  }
  cx.wso_base = wbase(w); cx.wdst = wdst(w);
  auto kchunk = [&](int s) { int k = s + rot; return k >= 10 ? k - 10 : k; };
  auto issue_for = [&](int v, int s, int slot) {
    const int sox = v * 32 * 1280 + kchunk(s) * 128, sow = wbase(v) + kchunk(s) * cx.wso_step;
    char* b = smem + slot;
#pragma unroll
    for (int i = 0; i < 4; ++i) dma16(cx.rx, b + v * 4096 + i * 1024, cx.vx[i], sox + i * 10240);
    dma16(cx.rw, b + XSLOT + wdst(v), cx.vw[0], sow);
    dma16(cx.rw, b + XSLOT + wdst(v) + 1024, cx.vw[1], SSM ? sow + 10240 : sow);
    if (!SSM) { if (lane < 32) dma16(cx.rw, b + XSLOT + wdst(v) + 2048, cx.vw[2], sow); }
  };
  const __amdgpu_buffer_rsrc_t rW2T = __builtin_amdgcn_make_buffer_rsrc((void*)(p.ws + WS_W2T), 0, (int)W2T_BYTES, RSRC3);
  auto stats_job = [&](const u32x4& r0, const u32x4& r1) {
    float v[16];
#pragma unroll
    for (int i = 0; i < 4; ++i) { v[2 * i] = __uint_as_float(r0[i] << 16); v[2 * i + 1] = __uint_as_float(r0[i] & 0xFFFF0000u); v[8 + 2 * i] = __uint_as_float(r1[i] << 16); v[9 + 2 * i] = __uint_as_float(r1[i] & 0xFFFF0000u); }
    float sum = 0.f;
#pragma unroll
    for (int i = 0; i < 16; ++i) sum += v[i];
    sum = wave_sum(sum);
    const float mean = sum * (1.0f / D);
    float sq = 0.f;
#pragma unroll
    for (int i = 0; i < 8; ++i) { const float d = v[i] - mean; sq = fmaf(d, d, sq); }
    float sq2 = 0.f;
#pragma unroll
    for (int i = 8; i < 16; ++i) { const float d = v[i] - mean; sq2 = fmaf(d, d, sq2); }
    sq += lane < 16 ? sq2 : 0.f;
    sq = wave_sum(sq);
    const float rstd = 1.0f / sqrtf(sq * (1.0f / D) + 1e-5f);
    if (lane == 0) {
      gu64* g = (gu64*)(p.ws + WS_STATS + (size_t)(256 * mt + 8 * cbi + w) * 16);
      __hip_atomic_store(g, ((unsigned long long)etag << 32) | __float_as_uint(mean), RLX_AGENT);
      __hip_atomic_store(g + 1, ((unsigned long long)etag << 32) | __float_as_uint(rstd), RLX_AGENT);
    }
  };
  const __amdgpu_buffer_rsrc_t rrow = __builtin_amdgcn_make_buffer_rsrc((void*)(p.x + (size_t)(256 * mt + 8 * cbi + w) * D), 0, D * 2, RSRC3);
  if (w < 4) {
    unsigned e_, pa_ = 0, pb_ = 0, pc_ = 0; u32x4 r0, r1;
    { const int vo = lane * 16;
      asm volatile("global_load_dword %2, %5, off sc1\n\tbuffer_load_dwordx4 %0, %3, %4, 0 offen\n\tbuffer_load_dwordx4 %1, %3, %4, 0 offen offset:1024"
                   : "=&v"(r0), "=&v"(r1), "=&v"(e_) : "v"(vo), "s"(rrow), "v"(p.ws + WS_EPOCH) : "memory"); }
    if constexpr (SSM) {
      const int ch = 64 * cj + lane;
      asm volatile("global_load_ushort %0, %3, off\n\tglobal_load_ushort %1, %4, off\n\tglobal_load_ushort %2, %5, off"
                   : "=&v"(pa_), "=&v"(pb_), "=&v"(pc_) : "v"(p.lre + ch), "v"(p.lim + ch), "v"(p.ldt + ch) : "memory");
    }
    issue_for(w, 0, 0); issue_for(w + 4, 0, 0);
    TS(6);
    issue_for(w, 1, SLOT); issue_for(w + 4, 1, SLOT);
    if constexpr (SSM) asm volatile("s_waitcnt vmcnt(24)" : "+v"(e_), "+v"(r0), "+v"(r1), "+v"(pa_), "+v"(pb_), "+v"(pc_) :: "memory"); else asm volatile("s_waitcnt vmcnt(28)" : "+v"(e_), "+v"(r0), "+v"(r1) :: "memory");
    etag = e_ + 1u;
    stats_job(r0, r1);
    if (SSM && w == 3) {
      const float a = bf2f((u16)pa_), b = bf2f((u16)pb_);
      const float dt = rbf(expf(bf2f((u16)pc_)));
      const float er = expf(dt * a);
      float sn, cn; sincosf(dt * b, &sn, &cn);
      const float lr = er * cn, li = er * sn;
      const float nr = lr - 1.0f, ni = li, den = a * a + b * b;
      const float cr = (nr * a + ni * b) / den, ci = (ni * a - nr * b) / den;
      float pr = lr, pi = li;
#pragma unroll
      for (int i = 0; i < 4; ++i) { float t = pr * pr - pi * pi; pi = 2.f * pr * pi; pr = t; }
      const float l16r = pr, l16i = pi;
#pragma unroll
      for (int i = 0; i < 4; ++i) { float t = pr * pr - pi * pi; pi = 2.f * pr * pi; pr = t; }
      float* par = (float*)(smem + LDS_PAR) + lane * 8;
      *(f32x4*)par = f32x4{lr, li, cr, ci};
      *(f32x4*)(par + 4) = f32x4{l16r, l16i, pr, pi};
    }
    if (SSM) WAIT_V(14); else WAIT_V(16);
    TS(13);
    WAIT_L0();
    BAR();
    issue_for(w, 2, 2 * SLOT); issue_for(w + 4, 2, 2 * SLOT);
  } else {
    const int hv = w - 4;
    unsigned e_; u32x4 r0, r1;
    { const int vo = lane * 16;
      asm volatile("global_load_dword %2, %5, off sc1\n\tbuffer_load_dwordx4 %0, %3, %4, 0 offen\n\tbuffer_load_dwordx4 %1, %3, %4, 0 offen offset:1024"
                   : "=&v"(r0), "=&v"(r1), "=&v"(e_) : "v"(vo), "s"(rrow), "v"(p.ws + WS_EPOCH) : "memory"); }
    const int unit = hv * 256 + blk;
    const bool has_unit = unit < 940, ffn_unit = unit < 540;
    char* rt = smem + LDS_RT + hv * 4096;
    int c0, s0u;
    if (ffn_unit) { s0u = unit / 10; c0 = 4 * (unit % 10); } else { const int u = unit - 540; c0 = u / 10; s0u = NSTEP_F + 4 * (u % 10); }
    if (has_unit) {
      if (ffn_unit) {
        const __amdgpu_buffer_rsrc_t r2 = __builtin_amdgcn_make_buffer_rsrc((void*)p.w2, 0, F * D * 2, RSRC3);
        const int vo = (lane >> 3) * 1280 + (lane & 7) * 16;
#pragma unroll
        for (int i = 0; i < 4; ++i) dma16(r2, rt + i * 1024, vo, (32 * s0u + 8 * i) * 1280 + (c0 >> 2) * 128);
      } else {
        const int vo = (lane & 15) * 1280 + (lane >> 4) * 16;
#pragma unroll
        for (int i = 0; i < 4; ++i) {
          const int sp = s0u - NSTEP_F + i, jg = sp >> 1, part = sp & 1;
          const __amdgpu_buffer_rsrc_t rc = __builtin_amdgcn_make_buffer_rsrc((void*)(part ? p.cim : p.cre), 0, D * P * 2, RSRC3);
          dma16(rc, rt + i * 1024, vo, 16 * c0 * 1280 + jg * 64);
        }
      }
    }
    asm volatile("s_waitcnt vmcnt(0)" : "+v"(e_), "+v"(r0), "+v"(r1) :: "memory");
    etag = e_ + 1u;
    u32x4 vv[4];
    {
      const unsigned rtl = lbase + LDS_RT + hv * 4096;
      if (ffn_unit) {
        const int g = lane >> 4, q = (lane >> 2) & 3, pp = lane & 3;
        const unsigned a0 = rtl + (8 * g + q) * 128 + 8 * pp;
        u32x2 l0 = trrd<0>(a0), h0 = trrd<512>(a0), l1 = trrd<32>(a0), h1 = trrd<512 + 32>(a0), l2 = trrd<64>(a0), h2 = trrd<512 + 64>(a0), l3 = trrd<96>(a0), h3 = trrd<512 + 96>(a0);
        asm volatile("s_waitcnt lgkmcnt(0)" : "+v"(l0), "+v"(h0), "+v"(l1), "+v"(h1), "+v"(l2), "+v"(h2), "+v"(l3), "+v"(h3) :: "memory");
        vv[0] = cat(l0, h0); vv[1] = cat(l1, h1); vv[2] = cat(l2, h2); vv[3] = cat(l3, h3);
      } else {
        u32x4 v0 = rd128<0>(rtl + lane * 16), v1 = rd128<1024>(rtl + lane * 16), v2 = rd128<2048>(rtl + lane * 16), v3 = rd128<3072>(rtl + lane * 16);
        asm volatile("s_waitcnt lgkmcnt(0)" : "+v"(v0), "+v"(v1), "+v"(v2), "+v"(v3) :: "memory");
        vv[0] = v0; vv[1] = v1; vv[2] = v2; vv[3] = v3;
      }
    }
    BAR();
    if (has_unit) {
#pragma unroll
      for (int i = 0; i < 4; ++i) {
        const int c = ffn_unit ? c0 + i : c0, s = ffn_unit ? s0u : s0u + i;
        st_sc1(vv[i], rW2T, lane * 16 + (((c / 5) * NSTEP + s) * 5 + (c % 5)) * 1024);
      }
    }
    stats_job(r0, r1);
  }
  TS(1);
  auto hook0 = [&]() {};
  auto hook7 = [&]() { if (tid == 0) __hip_atomic_store((gu32*)(p.ws + WS_FLAGW) + blk, etag, RLX_AGENT); };
  const __amdgpu_buffer_rsrc_t rACT = __builtin_amdgcn_make_buffer_rsrc((void*)(p.ws + WS_ACT), 0, (int)ACT_BYTES, RSRC3);
  const __amdgpu_buffer_rsrc_t rFW = __builtin_amdgcn_make_buffer_rsrc((void*)(p.ws + WS_FLAGW), 0, 1024, RSRC3);
  const int mt64 = 4 * mt + wr;
  gu64* stg;
  unsigned long long sta = 0, stb = 0;
  u32x4 fw;
  auto hookA = [&]() { const int vo_ = lane * 16; asm volatile("buffer_load_dwordx4 %0, %1, %2, 0 offen sc1" : "=&v"(fw) : "v"(vo_), "s"(rFW) : "memory"); };
  auto hook8 = [&]() {
    if constexpr (SSM) asm volatile("s_waitcnt vmcnt(6)" : "+v"(fw) :: "memory"); else asm volatile("s_waitcnt vmcnt(7)" : "+v"(fw) :: "memory");
    if (w == 0) {
      unsigned spins = 0;
      while (!__all(fw.x == etag && fw.y == etag && fw.z == etag && fw.w == etag)) {
        if (++spins > SPIN_MAX) break;
        __builtin_amdgcn_s_sleep(1);
        fw = __builtin_amdgcn_raw_buffer_load_b128(rFW, lane * 16, 0, 16);
      }
      if (blk == 0 && lane == 0) __hip_atomic_store((gu32*)(p.ws + WS_EPOCH), etag, RLX_AGENT);
    }
  };
  auto hook9 = [&](auto mode_tag) {
    int tz = threadIdx.x; asm volatile("" : "+v"(tz));
    stg = (gu64*)(p.ws + WS_STATS + (size_t)(256 * mt + 64 * wr + (tz & 63)) * 16);
    sta = __hip_atomic_load(stg, RLX_AGENT); stb = __hip_atomic_load(stg + 1, RLX_AGENT);
  };
  f32x4 csa = {0.f, 0.f, 0.f, 0.f};
  float* cst = (float*)(smem + LDS_CS);
  float* stt = (float*)(smem + LDS_ST);
  auto finish_common = [&](int ntw  , int tile0  ) {
    unsigned spins = 0;
    while (!__all((unsigned)(sta >> 32) == etag && (unsigned)(stb >> 32) == etag)) {
      if (++spins > SPIN_MAX) break;
      __builtin_amdgcn_s_sleep(1);
      sta = __hip_atomic_load(stg, RLX_AGENT); stb = __hip_atomic_load(stg + 1, RLX_AGENT);
    }
    const float mean = __uint_as_float((unsigned)sta), rstd = __uint_as_float((unsigned)stb);
    if (wc == 0) *(f32x2*)(stt + (64 * wr + lane) * 2) = f32x2{rstd, -mean * rstd};
    if (SSM) { if (G == 0) {
#pragma unroll
        for (int c = 0; c < 4; ++c) cst[wr * 160 + (tile0 + c) * 16 + i16] = csa[c]; } }
    else { if (i16 < ntw) *(f32x4*)(cst + wr * 160 + (tile0 + i16) * 16 + 4 * G) = csa; }
    WAIT_L0();
    BAR();
  };
  auto cs4 = [&](int idx) { return (*(const f32x4*)(cst + idx) + *(const f32x4*)(cst + 160 + idx)) + (*(const f32x4*)(cst + 320 + idx) + *(const f32x4*)(cst + 480 + idx)); };
  auto cs1 = [&](int idx) { return (cst[idx] + cst[160 + idx]) + (cst[320 + idx] + cst[480 + idx]); };

  if (!SSM) {
    auto ffn = [&](auto mode_tag) {
      constexpr int MODE = decltype(mode_tag)::value;
      constexpr int NT = Frags<MODE>::NT, P0 = MODE == 0 ? 0 : 3;
      f32x4 acc[4][NT];
#pragma unroll
      for (int T = 0; T < 4; ++T)
#pragma unroll
        for (int c = 0; c < NT; ++c) acc[T][c] = f32x4{0.f, 0.f, 0.f, 0.f};
      main_loop<MODE>(p, cx, lbase, w, lane, acc, csa, hook0, hook7, hookA, hook8, hook9);
      TS(2);
      relane();
      finish_common(NT, 2 * P0);
      p2_ldw(p2c, wv[0], 6); p2_ldw(p2c, wv[1], 7); p2_ldw(p2c, wv[2], 8); p2_ldw(p2c, wv[3], 3);
      SB();
      f32x2 st4[4];
#pragma unroll
      for (int T = 0; T < 4; ++T) st4[T] = *(const f32x2*)(stt + (64 * wr + 16 * T + i16) * 2);
#pragma unroll
      for (int Pq = 0; Pq < NT / 2; ++Pq) {
        const int ft = 5 * cj + P0 + Pq;
        const f32x4 cg = cs4((2 * P0 + 2 * Pq) * 16 + 4 * G), cu = cs4((2 * P0 + 2 * Pq + 1) * 16 + 4 * G);
        unsigned pk[4][2];
#pragma unroll
        for (int T = 0; T < 4; ++T) {
          float hv[4];
#pragma unroll
          for (int e = 0; e < 4; ++e) {
            const float g = fmaf(acc[T][2 * Pq][e], st4[T].x, st4[T].y * cg[e]);
            const float u = fmaf(acc[T][2 * Pq + 1][e], st4[T].x, st4[T].y * cu[e]);
            hv[e] = silu_f(g) * u;
          }
          pk[T][0] = pack2(hv[0], hv[1]); pk[T][1] = pack2(hv[2], hv[3]);
        }
        if (ft < 108) {
#pragma unroll
          for (int T2 = 0; T2 < 4; T2 += 2) {
            const auto a = __builtin_amdgcn_permlane16_swap(pk[T2][0], pk[T2 + 1][0], false, false);
            const auto b = __builtin_amdgcn_permlane16_swap(pk[T2][1], pk[T2 + 1][1], false, false);
            const u32x4 v = {a[0], b[0], a[1], b[1]};
            const int s2 = ft >> 1, gp = 2 * (ft & 1) + (G >> 1), T = T2 + (G & 1);
            st_sc1(v, rACT, T * 1024 + (gp * 16 + i16) * 16 + (mt64 * NSTEP + s2) * 4096);
          }
        }
      }
    };
    if (wc == 0) ffn(std::integral_constant<int, 0>{}); else ffn(std::integral_constant<int, 1>{});
  } else {
    f32x4 acc[4][4];
#pragma unroll
    for (int T = 0; T < 4; ++T)
#pragma unroll
      for (int c = 0; c < 4; ++c) acc[T][c] = f32x4{0.f, 0.f, 0.f, 0.f};
    main_loop<2>(p, cx, lbase, w, lane, acc, csa, hook0, hook7, hookA, hook8, hook9);
    TS(2);
    relane();
    const float* par = (const float*)(smem + LDS_PAR);
    float lr[2], li[2], cr[2], ci[2];
#pragma unroll
    for (int j = 0; j < 2; ++j) {
      const f32x4 pv = *(const f32x4*)(par + (32 * wc + 16 * j + i16) * 8);
      lr[j] = pv.x; li[j] = pv.y; cr[j] = pv.z; ci[j] = pv.w;
    }
    f32x4 pv = *(const f32x4*)(par + lane * 8 + 4);
    asm volatile("" : "+v"(pv), "+v"(lr[0]), "+v"(li[0]), "+v"(cr[0]), "+v"(ci[0]), "+v"(lr[1]), "+v"(li[1]), "+v"(cr[1]), "+v"(ci[1]));
    finish_common(4, 4 * wc);
    {
      float csl[4];
#pragma unroll
      for (int c = 0; c < 4; ++c) csl[c] = cs1((4 * wc + c) * 16 + i16);
#pragma unroll
      for (int T = 0; T < 4; ++T)
#pragma unroll
        for (int e = 0; e < 4; e += 2) {
          const f32x4 sv = *(const f32x4*)(stt + (64 * wr + 16 * G + 4 * T + e) * 2);
#pragma unroll
          for (int c = 0; c < 4; ++c) {
            acc[T][c][e] = fmaf(acc[T][c][e], sv.x, sv.y * csl[c]);
            acc[T][c][e + 1] = fmaf(acc[T][c][e + 1], sv.z, sv.w * csl[c]);
          }
        }
    }
    f32x2* et = (f32x2*)(smem + LDS_ET);
    const int seg = 4 * wr + G;
#pragma unroll
    for (int j = 0; j < 2; ++j) {
      float hr = 0.f, hi = 0.f;
#pragma unroll
      for (int i = 0; i < 16; ++i) {
        const float nr = fmaf(lr[j], hr, fmaf(-li[j], hi, acc[i >> 2][2 * j][i & 3]));
        const float ni = fmaf(lr[j], hi, fmaf(li[j], hr, acc[i >> 2][2 * j + 1][i & 3]));
        hr = nr; hi = ni;
      }
      et[seg * 64 + 32 * wc + 16 * j + i16] = f32x2{hr, hi};
    }
    WAIT_L0(); BAR();
    TS(3);
    if (w == 0) {
      const unsigned tag = etag;
      float xr_ = 0.f, xi_ = 0.f;
      f32x2 ev[16];
#pragma unroll
      for (int s = 0; s < 16; ++s) ev[s] = et[s * 64 + lane];
#pragma unroll
      for (int s = 0; s < 16; ++s) {
        const float nr = fmaf(pv.x, xr_, fmaf(-pv.y, xi_, ev[s].x));
        const float ni = fmaf(pv.x, xi_, fmaf(pv.y, xr_, ev[s].y));
        xr_ = nr; xi_ = ni;
      }
      gu64* gran = (gu64*)(p.ws + WS_GRAN);
      if (mt < 7) {
        gu64* g = gran + ((size_t)(mt * 10 + cj) * 64 + lane) * 2;
        __hip_atomic_store(g, ((unsigned long long)tag << 32) | __float_as_uint(xr_), RLX_AGENT);
        __hip_atomic_store(g + 1, ((unsigned long long)tag << 32) | __float_as_uint(xi_), RLX_AGENT);
      }
      float Hr = 0.f, Hi = 0.f;
      if (mt > 0) {
        unsigned long long ga[7], gb[7];
        unsigned spins = 0;
        while (true) {
          bool ok = true;
#pragma unroll
          for (int m = 0; m < 7; ++m) {
            if (m < mt) {
              gu64* g = gran + ((size_t)(m * 10 + cj) * 64 + lane) * 2;
              ga[m] = __hip_atomic_load(g, RLX_AGENT); gb[m] = __hip_atomic_load(g + 1, RLX_AGENT);
            } else { ga[m] = gb[m] = (unsigned long long)tag << 32; }
          }
#pragma unroll
          for (int m = 0; m < 7; ++m) ok = ok && ((unsigned)(ga[m] >> 32) == tag) && ((unsigned)(gb[m] >> 32) == tag);
          if (__all(ok)) break;
          if (++spins > SPIN_MAX) break;
          __builtin_amdgcn_s_sleep(1);
        }
#pragma unroll
        for (int m = 0; m < 7; ++m) {
          if (m < mt) {
            const float sr = __uint_as_float((unsigned)ga[m]), si = __uint_as_float((unsigned)gb[m]);
            const float nr = fmaf(pv.z, Hr, fmaf(-pv.w, Hi, sr));
            const float ni = fmaf(pv.z, Hi, fmaf(pv.w, Hr, si));
            Hr = nr; Hi = ni;
          }
        }
      }
      xr_ = Hr; xi_ = Hi;
#pragma unroll
      for (int s = 0; s < 16; ++s) {
        et[s * 64 + lane] = f32x2{xr_, xi_};
        const float nr = fmaf(pv.x, xr_, fmaf(-pv.y, xi_, ev[s].x));
        const float ni = fmaf(pv.x, xi_, fmaf(pv.y, xr_, ev[s].y));
        xr_ = nr; xi_ = ni;
      }
    }
    WAIT_L0(); BAR();
    TS(4);
    p2_ldw(p2c, wv[0], 6); p2_ldw(p2c, wv[1], 7);
    SB();
    f32x2 x0[2];
#pragma unroll
    for (int j = 0; j < 2; ++j) x0[j] = et[seg * 64 + 32 * wc + 16 * j + i16];
    unsigned pre[2][8], pim[2][8];
#pragma unroll
    for (int j = 0; j < 2; ++j) {
      float hr = x0[j].x, hi = x0[j].y;
      float ore[16], oim[16];
#pragma unroll
      for (int i = 0; i < 16; ++i) {
        const float nr = fmaf(lr[j], hr, fmaf(-li[j], hi, acc[i >> 2][2 * j][i & 3]));
        const float ni = fmaf(lr[j], hi, fmaf(li[j], hr, acc[i >> 2][2 * j + 1][i & 3]));
        hr = nr; hi = ni;
        ore[i] = cr[j] * hr - ci[j] * hi;
        oim[i] = -(cr[j] * hi + ci[j] * hr);
      }
#pragma unroll
      for (int i = 0; i < 8; ++i) { pre[j][i] = pack2(ore[2 * i], ore[2 * i + 1]); pim[j][i] = pack2(oim[2 * i], oim[2 * i + 1]); }
    }
    {
      char* tp = smem + LDS_TP + w * 4608;
      const int q = (lane >> 2) & 3, pp = lane & 3;
      const unsigned tb = lbase + LDS_TP + w * 4608 + (8 * G + q) * 144 + 8 * pp;
      const int jg = 2 * cj + wc;
#pragma unroll
      for (int part = 0; part < 2; ++part) {
#pragma unroll
        for (int j = 0; j < 2; ++j) {
          char* t0 = tp + (16 * j + i16) * 144 + 32 * G;
          const unsigned* src = part ? pim[j] : pre[j];
          *(u32x4*)t0 = u32x4{src[0], src[1], src[2], src[3]};
          *(u32x4*)(t0 + 16) = u32x4{src[4], src[5], src[6], src[7]};
        }
        WAIT_L0();
        u32x2 lo[4], hi[4];
        lo[0] = trrd<0>(tb); hi[0] = trrd<576>(tb); lo[1] = trrd<32>(tb); hi[1] = trrd<576 + 32>(tb); lo[2] = trrd<64>(tb); hi[2] = trrd<576 + 64>(tb); lo[3] = trrd<96>(tb); hi[3] = trrd<576 + 96>(tb);
        asm volatile("s_waitcnt lgkmcnt(0)" : "+v"(lo[0]), "+v"(hi[0]), "+v"(lo[1]), "+v"(hi[1]), "+v"(lo[2]), "+v"(hi[2]), "+v"(lo[3]), "+v"(hi[3]) :: "memory");
        const int s2 = NSTEP_F + 2 * jg + part;
#pragma unroll
        for (int T = 0; T < 4; ++T)
          st_sc1(cat(lo[T], hi[T]), rACT, T * 1024 + lane * 16 + (mt64 * NSTEP + s2) * 4096);
      }
    }
  }
  TS(5);
  WAIT_V(0);
  BAR();
  if (tid == 0) __hip_atomic_store((gu32*)(p.ws + WS_FLAGA + mt * 128) + cbi, etag, RLX_AGENT);
  asm volatile("" ::: "memory"); SB();
  if (SSM) { p2_ldw(p2c, wv[2], 8); p2_ldw(p2c, wv[3], 3); }
  p2_ldw(p2c, wv[4], 4); p2_ldw(p2c, wv[5], 5);
  SB();
  {
    char* lw = smem + LDS_W2 + w * 15360 + lane * 16;
#pragma unroll
    for (int k = 0; k < 3; ++k)
#pragma unroll
      for (int c = 0; c < 5; ++c) *(u32x4*)(lw + k * 5120 + c * 1024) = wv[k][c];
  }
  SB();
  p2_ldw(p2c, wv[0], 0); p2_ldw(p2c, wv[1], 1); p2_ldw(p2c, wv[2], 2);
  SB();
  return etag;
}

DI void p2_run(const Params& p, const char* lw  , __amdgpu_buffer_rsrc_t ra, const P2Ctx& p2c, u32x4 (&wv)[DW][5],
               int sa, int N, int w, int lane, int mt2, int shA, int shW, unsigned etag, f32x4 (&acc)[4][5]) {
  constexpr int DA = 2, NMIN = 10, NMAX = 14;
  u32x4 a[DA][4];
  const int vo = p2c.vo;
  auto lda = [&](int k, int slot) {
#pragma unroll
    for (int T = 0; T < 4; ++T) a[slot][T] = __builtin_amdgcn_raw_buffer_load_b128(ra, vo, sa + k * 4096 + T * 1024, 16);
  };
  {
    const int fi = w < 4 ? (lane < 22 ? lane : 0) : 22 + (lane < 10 ? lane : 0);
    gu32* fl = (gu32*)(p.ws + WS_FLAGA + mt2 * 128) + fi;
    unsigned spins = 0;
    while (!__all(__hip_atomic_load(fl, RLX_AGENT) == etag)) {
      if (++spins > SPIN_MAX) break;
      __builtin_amdgcn_s_sleep(1);
    }
  }
  __builtin_amdgcn_fence(__ATOMIC_ACQUIRE, "wavefront");
  TS(9);
#pragma unroll
  for (int s = 0; s < DA - 1; ++s) lda(s, s);
#pragma unroll
  for (int T = 0; T < 4; ++T)
#pragma unroll
    for (int c = 0; c < 5; ++c) asm volatile("" : "+v"(acc[T][c]));
  asm volatile("s_nop 7" ::: "memory");
  SB();
#pragma unroll
  for (int s = 0; s < NMAX; ++s) {
    if (s < NMIN || s < N) {
      if (s + DA - 1 < NMIN || s + DA - 1 < N) lda(s + DA - 1, (s + DA - 1) % DA);
      SB();
#pragma unroll
      for (int T = 0; T < 4; ++T)
#pragma unroll
        for (int c = 0; c < 5; ++c) mfma16_acc(acc[T][c], wv[s % DW][c], a[s % DA][T]);
      SB();
      if (s < 3) {
#pragma unroll
        for (int c = 0; c < 5; ++c) wv[s][c] = *(const u32x4*)(lw + s * 5120 + c * 1024);
      } else if (s + DW < NMAX) { if (s + DW < NMIN || s + DW < N) p2_ldw(p2c, wv[s % DW], s + DW); }
      SB();
    }
  }
  asm volatile("s_nop 15\n\ts_nop 7" ::: "memory");
}

DI void phase2(const Params& p, char* smem, unsigned etag, const P2Ctx& p2c, u32x4 (&wv)[DW][5], int mt64, int nt, int s0) {
  const int tid = threadIdx.x, lane = tid & 63, w = __builtin_amdgcn_readfirstlane(tid >> 6);
  TS(8);
  f32x4 acc[4][5];
#pragma unroll
  for (int T = 0; T < 4; ++T)
#pragma unroll
    for (int c = 0; c < 5; ++c) acc[T][c] = f32x4{0.f, 0.f, 0.f, 0.f};
  const __amdgpu_buffer_rsrc_t ra = __builtin_amdgcn_make_buffer_rsrc((void*)(p.ws + WS_ACT), 0, (int)ACT_BYTES, RSRC3);
  const int sa = (mt64 * NSTEP + s0) * 4096;
  const int shA = nt & 3, shW = mt64 & 7;
  const int N = w < 2 ? 14 : (w < 4 ? 13 : 10);
  const char* lw = smem + LDS_W2 + w * 15360 + lane * 16;
  p2_run(p, lw, ra, p2c, wv, sa, N, w, lane, mt64 >> 2, shA, shW, etag, acc);
  auto part_addr = [&](int wv_, int t) { return t < 15 ? LDS_W2 + wv_ * (15360) + t * 1024 : wv_ * 5120 + (t - 15) * 1024; };
  {
    const int g = lane >> 4, r = lane & 15;
    const int pos = g * 16 + ((r + 4 * g) & 15);
#pragma unroll
    for (int T = 0; T < 4; ++T)
#pragma unroll
      for (int c = 0; c < 5; ++c) *(f32x4*)(smem + part_addr(w, T * 5 + c) + pos * 16) = acc[T][c];
  }
  f32x4 res[3]; u32x2 xv[3], dv[3]; unsigned long long ga[3], gb[3];
#pragma unroll
  for (int it = 0; it < 3; ++it) {
    const int item = tid + 512 * it;
    if (item < 1280) {
      const int tok = item / 20, dg = item % 20;
      const int gt = mt64 * 64 + tok, d0 = nt * 80 + dg * 4;
      gu64* sg = (gu64*)(p.ws + WS_STATS + (size_t)gt * 16);
      ga[it] = __hip_atomic_load(sg, RLX_AGENT); gb[it] = __hip_atomic_load(sg + 1, RLX_AGENT);
      xv[it] = *(const u32x2*)(p.x + (size_t)gt * D + d0);
      dv[it] = *(const u32x2*)(p.dsk + d0);
    }
  }
  __syncthreads();
  TS(10);
#pragma unroll
  for (int it = 0; it < 3; ++it) {
    const int item = tid + 512 * it;
    if (item < 1280) {
      const int tok = item / 20, dg = item % 20, c = dg >> 2, g = dg & 3, T = tok >> 4, rr = tok & 15;
      const int t = T * 5 + c, off = (g * 16 + ((rr + 4 * g) & 15)) * 16;
      const int gt = mt64 * 64 + tok, d0 = nt * 80 + dg * 4;
      const int ta = t < 15 ? LDS_W2 + t * 1024 : (t - 15) * 1024, st = t < 15 ? 15360 : 5120;
      const char* base = smem + ta + off;
      f32x4 o = *(const f32x4*)(base);
#pragma unroll
      for (int k = 1; k < 4; ++k) o += *(const f32x4*)(base + k * st);
      f32x4 y = *(const f32x4*)(base + 4 * st);
#pragma unroll
      for (int k = 5; k < 8; ++k) y += *(const f32x4*)(base + k * st);
      const float mean = __uint_as_float((unsigned)ga[it]), rstd = __uint_as_float((unsigned)gb[it]);
      const float xs[4] = {__uint_as_float(xv[it].x << 16), __uint_as_float(xv[it].x & 0xFFFF0000u), __uint_as_float(xv[it].y << 16), __uint_as_float(xv[it].y & 0xFFFF0000u)};
      const float ds[4] = {__uint_as_float(dv[it].x << 16), __uint_as_float(dv[it].x & 0xFFFF0000u), __uint_as_float(dv[it].y << 16), __uint_as_float(dv[it].y & 0xFFFF0000u)};
#pragma unroll
      for (int e = 0; e < 4; ++e) {
        const float xn = (xs[e] - mean) * rstd;
        const float yy = fmaf(2.0f, y[e], xn * ds[e]);
        res[it][e] = fmaf(yy, silu_f(o[e]), xs[e]);
      }
      *(f32x4*)(p.out + (size_t)gt * D + d0) = res[it];
    }
  }
  TS(11);
}

__global__ __launch_bounds__(512, 2) void k_fused(Params p) {
  extern __shared__ __attribute__((aligned(16))) char smem[];
  const int b = blockIdx.x, xcd = b & 7, i = b >> 3;
  const int ml = i & 3, cl = i >> 2;
  const int mt = (xcd & 1) * 4 + ml, cb = (xcd >> 1) * 8 + cl;
  const int rot = 0;
  const int w = __builtin_amdgcn_readfirstlane(threadIdx.x >> 6), lane = threadIdx.x & 63;
  const int mt64 = (xcd >> 1) * 8 + (i & 7), nt = (xcd & 1) * 4 + (i >> 3);
  const int s0 = w < 4 ? (w < 2 ? 14 * w : 28 + 13 * (w - 2)) : 54 + 10 * (w - 4);
  P2Ctx p2c;
  p2c.rw = __builtin_amdgcn_make_buffer_rsrc((void*)(p.ws + WS_W2T), 0, (int)W2T_BYTES, RSRC3);
  p2c.sw = (nt * NSTEP + s0) * 5120; p2c.vo = 0;
  u32x4 wv[DW][5];
  unsigned etag;
  if (cb < 22) etag = phase1<false>(p, smem, mt, cb, b, rot, p2c, wv);
  else etag = phase1<true>(p, smem, mt, cb - 22, b, rot, p2c, wv);
  phase2(p, smem, etag, p2c, wv, mt64, nt, s0);
}
}

extern "C" void kernel_launch(void* const* d_in, const int* in_sizes, int n_in, void* d_out, int out_size, void* d_ws, size_t ws_size, hipStream_t stream) {
  using namespace s5;
  Params p;
  p.x = (const u16*)d_in[0]; p.lre = (const u16*)d_in[1]; p.lim = (const u16*)d_in[2];
  p.bre = (const u16*)d_in[3]; p.bim = (const u16*)d_in[4]; p.cre = (const u16*)d_in[5]; p.cim = (const u16*)d_in[6];
  p.ldt = (const u16*)d_in[7]; p.dsk = (const u16*)d_in[8];
  p.w1 = (const u16*)d_in[10]; p.w3 = (const u16*)d_in[11]; p.w2 = (const u16*)d_in[12];
  p.out = (float*)d_out; p.ws = (char*)d_ws;
  (void)hipFuncSetAttribute((const void*)k_fused, hipFuncAttributeMaxDynamicSharedMemorySize, LDS1_BYTES);
  k_fused<<<256, 512, LDS1_BYTES, stream>>>(p);
}
```

```cpp
#include <hip/hip_runtime.h>
#include <stdint.h>
#include <type_traits>

namespace s5 {
typedef unsigned short u16;
typedef short bf16x8 __attribute__((ext_vector_type(8)));
typedef float f32x4 __attribute__((ext_vector_type(4)));
typedef float f32x2 __attribute__((ext_vector_type(2)));
typedef unsigned u32x4 __attribute__((ext_vector_type(4)));
typedef unsigned u32x2 __attribute__((ext_vector_type(2)));
typedef __bf16 bf16x2 __attribute__((ext_vector_type(2)));
typedef __attribute__((address_space(3))) void* lds_vp;
typedef __attribute__((address_space(3))) char* lds_cp;
typedef __attribute__((address_space(1))) unsigned long long gu64;
typedef __attribute__((address_space(1))) unsigned gu32;
#define DI __device__ __forceinline__
#define RLX_AGENT __ATOMIC_RELAXED, __HIP_MEMORY_SCOPE_AGENT

constexpr int L = 2048, D = 640, P = 640, F = 1728;
constexpr int NSTEP = 94, NSTEP_F = 54;
constexpr size_t WS_EPOCH = 0;
constexpr size_t WS_FLAGW = 1024;
constexpr size_t WS_FLAGA = 4096;
constexpr size_t WS_GRAN = 8192;
constexpr size_t WS_STATS = WS_GRAN + 7 * 10 * 1024 + 2048;
constexpr size_t WS_ACT = 262144;
constexpr size_t ACT_BYTES = (size_t)32 * NSTEP * 4 * 1024;
constexpr size_t WS_W2T = WS_ACT + ACT_BYTES;
constexpr size_t W2T_BYTES = (size_t)8 * NSTEP * 5 * 1024;
constexpr size_t WS_STAMP = 32u << 20;
static_assert(WS_STATS + 2048 * 16 <= WS_ACT, "ws map");
constexpr int XSLOT = 32768, WSLOT = 20480, SLOT = XSLOT + WSLOT;
constexpr int LDS_PAR = 3 * SLOT;
constexpr int LDS1_BYTES = 163840;
constexpr int LDS_CS = SLOT;
constexpr int LDS_ST = SLOT + 4096;
constexpr int LDS_ET = SLOT + 8192;
constexpr int LDS_TP = SLOT + 16384;
constexpr int LDS_W2 = 40960;
constexpr int LDS_RT = 2 * SLOT;
static_assert(LDS1_BYTES <= 163840, "lds");
constexpr int RSRC3 = 0x00020000;
constexpr unsigned SPIN_MAX = 1u << 21;

struct Params {
  const u16 *x, *lre, *lim, *bre, *bim, *cre, *cim, *ldt, *dsk, *w1, *w3, *w2;
  float* out;
  char* ws;
};

DI float bf2f(u16 v) { return __uint_as_float(((unsigned)v) << 16); }
DI float rbf(float f) { unsigned u = __float_as_uint(f); u += 0x7FFFu + ((u >> 16) & 1u); return __uint_as_float(u & 0xFFFF0000u); }
DI unsigned pack2(float a, float b) { f32x2 p = {a, b}; return __builtin_bit_cast(unsigned, __builtin_convertvector(p, bf16x2)); }
DI f32x4 mfma16(u32x4 a, u32x4 b, f32x4 c) { return __builtin_amdgcn_mfma_f32_16x16x32_bf16(__builtin_bit_cast(bf16x8, a), __builtin_bit_cast(bf16x8, b), c, 0, 0, 0); }
DI void mfma16_acc(f32x4& c, u32x4 a, u32x4 b) { asm volatile("v_mfma_f32_16x16x32_bf16 %0, %1, %2, %0" : "+v"(c) : "v"(a), "v"(b)); }
template <int OFF> DI u32x2 trrd(unsigned addr) { u32x2 r; asm volatile("ds_read_b64_tr_b16 %0, %1 offset:%2" : "=v"(r) : "v"(addr), "i"(OFF) : "memory"); return r; }
template <int OFF> DI u32x4 rd128(unsigned addr) { u32x4 r; asm volatile("ds_read_b128 %0, %1 offset:%2" : "=v"(r) : "v"(addr), "i"(OFF) : "memory"); return r; }
DI u32x4 cat(u32x2 a, u32x2 b) { return u32x4{a.x, a.y, b.x, b.y}; }
DI void dma16(__amdgpu_buffer_rsrc_t rs, char* lds, int voff, int soff) { __builtin_amdgcn_raw_ptr_buffer_load_lds(rs, (lds_vp)lds, 16, voff, soff, 0, 0); }
DI void st_sc1(u32x4 v, __amdgpu_buffer_rsrc_t rs, int off) { __builtin_amdgcn_raw_buffer_store_b128(v, rs, off, 0, 16); }
#define SB() __builtin_amdgcn_sched_barrier(0)
#define WAIT_V(n) asm volatile("s_waitcnt vmcnt(" #n ")" ::: "memory")
#define WAIT_L0() asm volatile("s_waitcnt lgkmcnt(0)" ::: "memory")
#define BAR() __builtin_amdgcn_s_barrier()
#define LBAR() __builtin_amdgcn_s_barrier()
#define TS(i) do {} while (0)
DI float wave_sum(float v) {
  v += __builtin_bit_cast(float, __builtin_amdgcn_update_dpp(0, __builtin_bit_cast(int, v), 0x111, 0xf, 0xf, true));
  v += __builtin_bit_cast(float, __builtin_amdgcn_update_dpp(0, __builtin_bit_cast(int, v), 0x112, 0xf, 0xf, true));
  v += __builtin_bit_cast(float, __builtin_amdgcn_update_dpp(0, __builtin_bit_cast(int, v), 0x114, 0xf, 0xf, true));
  v += __builtin_bit_cast(float, __builtin_amdgcn_update_dpp(0, __builtin_bit_cast(int, v), 0x118, 0xf, 0xf, true));
  const int iv = __builtin_bit_cast(int, v);
  const float a = __builtin_bit_cast(float, __builtin_amdgcn_readlane(iv, 15)), b = __builtin_bit_cast(float, __builtin_amdgcn_readlane(iv, 31));
  const float c = __builtin_bit_cast(float, __builtin_amdgcn_readlane(iv, 47)), d = __builtin_bit_cast(float, __builtin_amdgcn_readlane(iv, 63));
  return (a + b) + (c + d);
}
DI float silu_f(float g) { return g * __builtin_amdgcn_rcpf(1.0f + __builtin_amdgcn_exp2f(g * -1.44269504088896f)); }

template <int MODE> struct Frags {
  static constexpr int NT = MODE == 0 ? 6 : 4;
  u32x4 x[4];
  u32x2 lo[MODE == 2 ? 1 : NT], hi[MODE == 2 ? 1 : NT];
  u32x4 v[MODE == 2 ? NT : 1];
  template <int KK, int C> DI void rdw(unsigned aw) {
    if constexpr (MODE == 2) v[C] = rd128<C * 2048>(aw);
    else { constexpr int O = KK * 10240 + (C & 1) * 2560 + ((MODE == 0 ? 0 : 3) + (C >> 1)) * 32; lo[C] = trrd<O>(aw); hi[C] = trrd<O + 1280>(aw); }
  }
  template <int KK, int C> DI void rdw_lo(unsigned aw) {
    if constexpr (MODE == 2) v[C] = rd128<C * 2048>(aw);
    else { constexpr int O = KK * 10240 + (C & 1) * 2560 + ((MODE == 0 ? 0 : 3) + (C >> 1)) * 32; lo[C] = trrd<O>(aw); }
  }
  template <int KK, int C> DI void rdw_hi(unsigned aw) {
    if constexpr (MODE != 2) { constexpr int O = KK * 10240 + (C & 1) * 2560 + ((MODE == 0 ? 0 : 3) + (C >> 1)) * 32; hi[C] = trrd<O + 1280>(aw); }
  }
  template <int T> DI void rdx(unsigned ax) { x[T] = rd128<(MODE == 2 ? 512 : 2048) * T>(ax); }
  DI u32x4 w(int c) const { if constexpr (MODE == 2) return v[c]; else return cat(lo[c], hi[c]); }
  DI void wait() {
    if constexpr (MODE == 0) asm volatile("s_waitcnt lgkmcnt(0)" : "+v"(x[0]), "+v"(x[1]), "+v"(x[2]), "+v"(x[3]), "+v"(lo[0]), "+v"(hi[0]), "+v"(lo[1]), "+v"(hi[1]), "+v"(lo[2]), "+v"(hi[2]),
                                          "+v"(lo[3 % NT]), "+v"(hi[3 % NT]), "+v"(lo[4 % NT]), "+v"(hi[4 % NT]), "+v"(lo[5 % NT]), "+v"(hi[5 % NT]) :: "memory");
    else if constexpr (MODE == 1) asm volatile("s_waitcnt lgkmcnt(0)" : "+v"(x[0]), "+v"(x[1]), "+v"(x[2]), "+v"(x[3]), "+v"(lo[0]), "+v"(hi[0]), "+v"(lo[1]), "+v"(hi[1]), "+v"(lo[2]), "+v"(hi[2]), "+v"(lo[3]), "+v"(hi[3]) :: "memory");
    else asm volatile("s_waitcnt lgkmcnt(0)" : "+v"(x[0]), "+v"(x[1]), "+v"(x[2]), "+v"(x[3]), "+v"(v[0]), "+v"(v[1 % NT]), "+v"(v[2 % NT]), "+v"(v[3 % NT]) :: "memory");
  }
};

struct Ctx {
  __amdgpu_buffer_rsrc_t rx, rw;
  int vx[4];
  int vw[3];
  int xso_base, wso_base, wso_step;
  int rot;
  char* smem;
  int xdst, wdst;
};

template <int MODE, class H0, class H7, class HA, class H8, class H9>
DI void main_loop(const Params& p, const Ctx& cx, unsigned lbase, int w, int lane, f32x4 (&acc)[4][Frags<MODE>::NT], f32x4& csa, H0 hook0, H7 hook7, HA hookA, H8 hook8, H9 hook9) {
  constexpr bool SSM = MODE == 2;
  constexpr int NT = Frags<MODE>::NT;
  const int wr = w & 3, wc = w >> 2;
  const int G = lane >> 4, i16 = lane & 15;
  unsigned ax[2], aw[2];
  if (SSM) {
    const int row = 64 * wr + 16 * (i16 >> 2) + (i16 & 3), f = ((i16 & 3) >> 1) | ((i16 >> 2) << 1);
#pragma unroll
    for (int kk = 0; kk < 2; ++kk) ax[kk] = lbase + row * 128 + (((4 * kk + G) ^ f) << 4);
    const int f2 = (i16 >> 1) & 7;
#pragma unroll
    for (int kk = 0; kk < 2; ++kk) aw[kk] = lbase + XSLOT + (64 * wc + i16) * 128 + (((4 * kk + G) ^ f2) << 4);
  } else {
    const int row = 64 * wr + i16, f = (i16 >> 1) & 7;
#pragma unroll
    for (int kk = 0; kk < 2; ++kk) ax[kk] = lbase + row * 128 + (((4 * kk + G) ^ f) << 4);
    const int q = (lane >> 2) & 3, pp = lane & 3;
    aw[0] = aw[1] = lbase + XSLOT + (G >> 1) * 5120 + (4 * (G & 1) + q) * 160 + pp * 8;
  }
  auto kchunk = [&](int s) { int k = s + cx.rot; return k >= 10 ? k - 10 : k; };
  auto dma = [&](int slot, int sox, int sow, int i) {
    char* b = cx.smem + slot;
    if (i < 4) dma16(cx.rx, b + cx.xdst + i * 1024, cx.vx[i], sox + i * 10240);
    else if (SSM) { if (i < 6) dma16(cx.rw, b + XSLOT + cx.wdst + (i - 4) * 1024, cx.vw[i - 4], sow + (i - 4) * 10240); }
    else if (i < 6) dma16(cx.rw, b + XSLOT + cx.wdst + (i - 4) * 1024, cx.vw[i - 4], sow);
    else { if (lane < 32) dma16(cx.rw, b + XSLOT + cx.wdst + 2048, cx.vw[2], sow); }
  };
  Frags<MODE> fa, fb;
  fa.template rdx<0>(ax[0]); fa.template rdx<1>(ax[0]); fa.template rdx<2>(ax[0]); fa.template rdx<3>(ax[0]);
  fa.template rdw<0, 0>(aw[0]); fa.template rdw<0, 1>(aw[0]); fa.template rdw<0, 2>(aw[0]); fa.template rdw<0, 3>(aw[0]);
  if constexpr (NT > 4) { fa.template rdw<0, 4 % NT>(aw[0]); fa.template rdw<0, 5 % NT>(aw[0]); }

#define MM1(F_, C, T) acc[T][C] = SSM ? mfma16(F_.x[T], F_.w(C), acc[T][C]) : mfma16(F_.w(C), F_.x[T], acc[T][C])
#define MMG(F_, C) do { MM1(F_, C, 0); MM1(F_, C, 1); MM1(F_, C, 2); MM1(F_, C, 3); } while (0)
#define NREADS (SSM ? 8 : 4 + 2 * NT)
#define RD(N_, KK, I, WA, XA) do { \
    if constexpr ((I) < 4) N_.template rdx<(I) & 3>(XA); \
    else if constexpr (SSM) { if constexpr ((I) < 8) N_.template rdw_lo<KK, ((I) - 4) & 3>(WA); } \
    else if constexpr ((I) < 4 + 2 * NT) { if constexpr (((I) & 1) == 0) N_.template rdw_lo<KK, (((I) - 4) >> 1) % NT>(WA); else N_.template rdw_hi<KK, (((I) - 4) >> 1) % NT>(WA); } \
  } while (0)
#define GRP(F_, N_, KK, C, R0, WA, XA, EXTRA) do { \
    MM1(F_, C, 0); RD(N_, KK, (R0), WA, XA); SB(); \
    MM1(F_, C, 1); RD(N_, KK, (R0) + 1, WA, XA); SB(); \
    MM1(F_, C, 2); RD(N_, KK, (R0) + 2, WA, XA); SB(); \
    MM1(F_, C, 3); RD(N_, KK, (R0) + 3, WA, XA); EXTRA; SB(); } while (0)
#define CSEL1(F_, C) do { const unsigned v_ = (io_ == (C)) ? 0x3F803F80u : 0u; const u32x4 s_ = {v_, v_, v_, v_}; csa = SSM ? mfma16(s_, F_.w(C), csa) : mfma16(F_.w(C), s_, csa); } while (0)
#define CSELALL(F_, M) do { if (M) { int io_ = i16; asm volatile("" : "+v"(io_)); CSEL1(F_, 0); CSEL1(F_, 1); CSEL1(F_, 2); CSEL1(F_, 3); if constexpr (NT > 4) { CSEL1(F_, 4 % NT); CSEL1(F_, 5 % NT); } } SB(); } while (0)
  int slot = 0;
  auto stage = [&](const int s, auto dx_t, auto dw_t, auto bnd_t) __attribute__((always_inline)) {
    constexpr bool DX = decltype(dx_t)::value, DW = decltype(dw_t)::value;
    constexpr int BND = decltype(bnd_t)::value;
    const int nslot = slot == 2 * SLOT ? 0 : slot + SLOT;
    int sox = 0, sow = 0, pows = 0, pslot = 0;
    if constexpr (DX) { sox = cx.xso_base + kchunk(s + 3) * 128; asm volatile("" : "+s"(sox)); }
    if constexpr (DW) { pows = cx.wso_base + kchunk(s + 2) * cx.wso_step; pslot = nslot == 2 * SLOT ? 0 : nslot + SLOT; asm volatile("" : "+s"(pows), "+s"(pslot)); }
    fa.wait(); SB();
    {
      const unsigned X1 = ax[1] + slot, W1 = aw[1] + slot;
      GRP(fa, fb, 1, 0, 0, W1, X1, if constexpr (DW) dma(pslot, 0, pows, 4));
      GRP(fa, fb, 1, 1, 4, W1, X1, if constexpr (DW) dma(pslot, 0, pows, 5));
      GRP(fa, fb, 1, 2, 8, W1, X1, if constexpr (!SSM && DW) dma(pslot, 0, pows, 6));
      GRP(fa, fb, 1, 3, 12, W1, X1, );
      if constexpr (NT > 4) {
        GRP(fa, fb, 1, 4 % NT, 16, W1, X1, );
        GRP(fa, fb, 1, 5 % NT, 20, W1, X1, );
      }
      CSELALL(fa, ((2 * s) & 3) == wr);
      if constexpr (BND == 0) hook9(std::integral_constant<int, MODE>{});
    }
    fb.wait(); SB();
    const unsigned X0 = ax[0] + nslot, W0 = aw[0] + nslot;
    MMG(fb, 0); SB();
    if constexpr (BND == 1) { if (SSM) WAIT_V(6); else WAIT_V(7); LBAR(); }
    if constexpr (BND == 2) { WAIT_V(0); LBAR(); }
    if constexpr (BND == 3) {
      if (w < 4) { if (SSM) WAIT_V(14); else WAIT_V(16); } else WAIT_V(0);
      LBAR();
    }
    GRP(fb, fa, 0, 1, 0, W0, X0, if constexpr (DX) dma(slot, sox, sow, 0));
    GRP(fb, fa, 0, 2, 4, W0, X0, if constexpr (DX) dma(slot, sox, sow, 1));
    GRP(fb, fa, 0, 3, 8, W0, X0, if constexpr (DX) dma(slot, sox, sow, 2));
    if constexpr (NT > 4) {
      GRP(fb, fa, 0, 4 % NT, 12, W0, X0, if constexpr (DX) dma(slot, sox, sow, 3));
      GRP(fb, fa, 0, 5 % NT, 16, W0, X0, );
    } else { if constexpr (DX) dma(slot, sox, sow, 3); }
    CSELALL(fb, ((2 * s + 1) & 3) == wr);
    slot = nslot;
  };
  using T_ = std::true_type; using F_ = std::false_type;
  using I0 = std::integral_constant<int, 0>; using I1 = std::integral_constant<int, 1>; using I2 = std::integral_constant<int, 2>;
  stage(0, T_{}, F_{}, std::integral_constant<int, 3>{});
  hook7();
#pragma unroll 1
  for (int s = 1; s < 6; ++s) stage(s, T_{}, T_{}, I1{});
  hookA();
  stage(6, T_{}, T_{}, I1{});
  hook8();
  stage(7, F_{}, T_{}, I1{});
  stage(8, F_{}, F_{}, I2{});
  stage(9, F_{}, F_{}, I0{});
  fa.wait();
}

constexpr int LWS = 0;
#define DWF_ 5
constexpr int DWF = DWF_;
constexpr int DW = 6;
struct P2Ctx { __amdgpu_buffer_rsrc_t rw; int sw, vo; };
DI void p2_ldw(const P2Ctx& c, u32x4 (&wv)[5], int k) {
#pragma unroll
  for (int j = 0; j < 5; ++j) wv[j] = __builtin_amdgcn_raw_buffer_load_b128(c.rw, c.vo, c.sw + k * 5120 + j * 1024, 16);
}
template <bool SSM>
DI unsigned phase1(const Params& p, char* smem, int mt, int cj  , int blk, int rot, P2Ctx& p2c, u32x4 (&wv)[DW][5]) {
  int tid = threadIdx.x, lane = tid & 63;
  const int w = __builtin_amdgcn_readfirstlane(tid >> 6);
  const int wr = w & 3, wc = w >> 2;
  int G = lane >> 4, i16 = lane & 15;
  auto relane = [&]() { int tz = threadIdx.x; asm volatile("" : "+v"(tz)); tid = tz; lane = tz & 63; G = lane >> 4; i16 = lane & 15; p2c.vo = lane * 16; };
  const unsigned lbase = (unsigned)(size_t)(lds_cp)smem;
  TS(0);
  const int cbi = SSM ? 22 + cj : cj;
  unsigned etag;
  Ctx cx;
  cx.smem = smem; cx.rot = rot;
  cx.rx = __builtin_amdgcn_make_buffer_rsrc((void*)(p.x + (size_t)(256 * mt) * D), 0, 256 * D * 2, RSRC3);
#pragma unroll
  for (int i = 0; i < 4; ++i) {
    int f;
    if (SSM) f = ((lane >> 4) & 1) | ((((2 * w + (i >> 1)) & 3)) << 1);
    else f = ((i & 1) << 2) | (lane >> 4);
    cx.vx[i] = (lane >> 3) * 1280 + (((lane & 7) ^ f) << 4);
  }
  cx.xso_base = w * 32 * 1280; cx.xdst = w * 4096;
  auto wbase = [&](int v) { return SSM ? (64 * cj + 32 * (v >> 2) + 16 * ((v & 3) >> 1)) * 1280 : 16 * (v >> 1) * 3456; };
  auto wdst = [&](int v) { return SSM ? v * 2048 : (v >> 1) * 5120 + (v & 1) * 2560; };
  if (SSM) {
    cx.rw = __builtin_amdgcn_make_buffer_rsrc((void*)((w & 1) ? p.bim : p.bre), 0, P * D * 2, RSRC3);
    cx.vw[0] = (lane >> 3) * 1280 + (((lane & 7) ^ (lane >> 4)) << 4); cx.vw[1] = cx.vw[0] ^ 64; cx.vw[2] = 0;
    cx.wso_step = 128;
  } else {
    cx.rw = __builtin_amdgcn_make_buffer_rsrc((void*)((w & 1) ? p.w3 : p.w1), 0, D * F * 2, RSRC3);
#pragma unroll
    for (int i = 0; i < 3; ++i) {
      const int n = 64 * i + lane, pos = (n / 10) & 15, row = (pos & 3) | ((pos & 4) << 1) | ((pos & 8) >> 1);
      cx.vw[i] = row * 3456 + (n % 10) * 16 + cj * 160;
    }
    cx.wso_step = 64 * 3456;
  }
  cx.wso_base = wbase(w); cx.wdst = wdst(w);
  auto kchunk = [&](int s) { int k = s + rot; return k >= 10 ? k - 10 : k; };
  auto issue_for = [&](int v, int s, int slot) {
    const int sox = v * 32 * 1280 + kchunk(s) * 128, sow = wbase(v) + kchunk(s) * cx.wso_step;
    char* b = smem + slot;
#pragma unroll
    for (int i = 0; i < 4; ++i) dma16(cx.rx, b + v * 4096 + i * 1024, cx.vx[i], sox + i * 10240);
    dma16(cx.rw, b + XSLOT + wdst(v), cx.vw[0], sow);
    dma16(cx.rw, b + XSLOT + wdst(v) + 1024, cx.vw[1], SSM ? sow + 10240 : sow);
    if (!SSM) { if (lane < 32) dma16(cx.rw, b + XSLOT + wdst(v) + 2048, cx.vw[2], sow); }
  };
  const __amdgpu_buffer_rsrc_t rW2T = __builtin_amdgcn_make_buffer_rsrc((void*)(p.ws + WS_W2T), 0, (int)W2T_BYTES, RSRC3);
  auto stats_job = [&](const u32x4& r0, const u32x4& r1) {
    float v[16];
#pragma unroll
    for (int i = 0; i < 4; ++i) { v[2 * i] = __uint_as_float(r0[i] << 16); v[2 * i + 1] = __uint_as_float(r0[i] & 0xFFFF0000u); v[8 + 2 * i] = __uint_as_float(r1[i] << 16); v[9 + 2 * i] = __uint_as_float(r1[i] & 0xFFFF0000u); }
    float sum = 0.f;
#pragma unroll
    for (int i = 0; i < 16; ++i) sum += v[i];
    sum = wave_sum(sum);
    const float mean = sum * (1.0f / D);
    float sq = 0.f;
#pragma unroll
    for (int i = 0; i < 8; ++i) { const float d = v[i] - mean; sq = fmaf(d, d, sq); }
    float sq2 = 0.f;
#pragma unroll
    for (int i = 8; i < 16; ++i) { const float d = v[i] - mean; sq2 = fmaf(d, d, sq2); }
    sq += lane < 16 ? sq2 : 0.f;
    sq = wave_sum(sq);
    const float rstd = 1.0f / sqrtf(sq * (1.0f / D) + 1e-5f);
    if (lane == 0) {
      gu64* g = (gu64*)(p.ws + WS_STATS + (size_t)(256 * mt + 8 * cbi + w) * 16);
      __hip_atomic_store(g, ((unsigned long long)etag << 32) | __float_as_uint(mean), RLX_AGENT);
      __hip_atomic_store(g + 1, ((unsigned long long)etag << 32) | __float_as_uint(rstd), RLX_AGENT);
    }
  };
  const __amdgpu_buffer_rsrc_t rrow = __builtin_amdgcn_make_buffer_rsrc((void*)(p.x + (size_t)(256 * mt + 8 * cbi + w) * D), 0, D * 2, RSRC3);
  if (w < 4) {
    unsigned e_, pa_ = 0, pb_ = 0, pc_ = 0; u32x4 r0, r1;
    { const int vo = lane * 16;
      asm volatile("global_load_dword %2, %5, off sc1\n\tbuffer_load_dwordx4 %0, %3, %4, 0 offen\n\tbuffer_load_dwordx4 %1, %3, %4, 0 offen offset:1024"
                   : "=&v"(r0), "=&v"(r1), "=&v"(e_) : "v"(vo), "s"(rrow), "v"(p.ws + WS_EPOCH) : "memory"); }
    if constexpr (SSM) {
      const int ch = 64 * cj + lane;
      asm volatile("global_load_ushort %0, %3, off\n\tglobal_load_ushort %1, %4, off\n\tglobal_load_ushort %2, %5, off"
                   : "=&v"(pa_), "=&v"(pb_), "=&v"(pc_) : "v"(p.lre + ch), "v"(p.lim + ch), "v"(p.ldt + ch) : "memory");
    }
    issue_for(w, 0, 0); issue_for(w + 4, 0, 0);
    TS(6);
    issue_for(w, 1, SLOT); issue_for(w + 4, 1, SLOT);
    if constexpr (SSM) asm volatile("s_waitcnt vmcnt(24)" : "+v"(e_), "+v"(r0), "+v"(r1), "+v"(pa_), "+v"(pb_), "+v"(pc_) :: "memory"); else asm volatile("s_waitcnt vmcnt(28)" : "+v"(e_), "+v"(r0), "+v"(r1) :: "memory");
    etag = e_ + 1u;
    stats_job(r0, r1);
    if (SSM && w == 3) {
      const float a = bf2f((u16)pa_), b = bf2f((u16)pb_);
      const float dt = rbf(expf(bf2f((u16)pc_)));
      const float er = expf(dt * a);
      float sn, cn; sincosf(dt * b, &sn, &cn);
      const float lr = er * cn, li = er * sn;
      const float nr = lr - 1.0f, ni = li, den = a * a + b * b;
      const float cr = (nr * a + ni * b) / den, ci = (ni * a - nr * b) / den;
      float pr = lr, pi = li;
#pragma unroll
      for (int i = 0; i < 4; ++i) { float t = pr * pr - pi * pi; pi = 2.f * pr * pi; pr = t; }
      const float l16r = pr, l16i = pi;
#pragma unroll
      for (int i = 0; i < 4; ++i) { float t = pr * pr - pi * pi; pi = 2.f * pr * pi; pr = t; }
      float* par = (float*)(smem + LDS_PAR) + lane * 8;
      *(f32x4*)par = f32x4{lr, li, cr, ci};
      *(f32x4*)(par + 4) = f32x4{l16r, l16i, pr, pi};
    }
    if (SSM) WAIT_V(14); else WAIT_V(16);
    TS(13);
    WAIT_L0();
    BAR();
    issue_for(w, 2, 2 * SLOT); issue_for(w + 4, 2, 2 * SLOT);
  } else {
    const int hv = w - 4;
    unsigned e_; u32x4 r0, r1;
    { const int vo = lane * 16;
      asm volatile("global_load_dword %2, %5, off sc1\n\tbuffer_load_dwordx4 %0, %3, %4, 0 offen\n\tbuffer_load_dwordx4 %1, %3, %4, 0 offen offset:1024"
                   : "=&v"(r0), "=&v"(r1), "=&v"(e_) : "v"(vo), "s"(rrow), "v"(p.ws + WS_EPOCH) : "memory"); }
    const int unit = hv * 256 + blk;
    const bool has_unit = unit < 940, ffn_unit = unit < 540;
    char* rt = smem + LDS_RT + hv * 4096;
    int c0, s0u;
    if (ffn_unit) { s0u = unit / 10; c0 = 4 * (unit % 10); } else { const int u = unit - 540; c0 = u / 10; s0u = NSTEP_F + 4 * (u % 10); }
    if (has_unit) {
      if (ffn_unit) {
        const __amdgpu_buffer_rsrc_t r2 = __builtin_amdgcn_make_buffer_rsrc((void*)p.w2, 0, F * D * 2, RSRC3);
        const int vo = (lane >> 3) * 1280 + (lane & 7) * 16;
#pragma unroll
        for (int i = 0; i < 4; ++i) dma16(r2, rt + i * 1024, vo, (32 * s0u + 8 * i) * 1280 + (c0 >> 2) * 128);
      } else {
        const int vo = (lane & 15) * 1280 + (lane >> 4) * 16;
#pragma unroll
        for (int i = 0; i < 4; ++i) {
          const int sp = s0u - NSTEP_F + i, jg = sp >> 1, part = sp & 1;
          const __amdgpu_buffer_rsrc_t rc = __builtin_amdgcn_make_buffer_rsrc((void*)(part ? p.cim : p.cre), 0, D * P * 2, RSRC3);
          dma16(rc, rt + i * 1024, vo, 16 * c0 * 1280 + jg * 64);
        }
      }
    }
    asm volatile("s_waitcnt vmcnt(0)" : "+v"(e_), "+v"(r0), "+v"(r1) :: "memory");
    etag = e_ + 1u;
    u32x4 vv[4];
    {
      const unsigned rtl = lbase + LDS_RT + hv * 4096;
      if (ffn_unit) {
        const int g = lane >> 4, q = (lane >> 2) & 3, pp = lane & 3;
        const unsigned a0 = rtl + (8 * g + q) * 128 + 8 * pp;
        u32x2 l0 = trrd<0>(a0), h0 = trrd<512>(a0), l1 = trrd<32>(a0), h1 = trrd<512 + 32>(a0), l2 = trrd<64>(a0), h2 = trrd<512 + 64>(a0), l3 = trrd<96>(a0), h3 = trrd<512 + 96>(a0);
        asm volatile("s_waitcnt lgkmcnt(0)" : "+v"(l0), "+v"(h0), "+v"(l1), "+v"(h1), "+v"(l2), "+v"(h2), "+v"(l3), "+v"(h3) :: "memory");
        vv[0] = cat(l0, h0); vv[1] = cat(l1, h1); vv[2] = cat(l2, h2); vv[3] = cat(l3, h3);
      } else {
        u32x4 v0 = rd128<0>(rtl + lane * 16), v1 = rd128<1024>(rtl + lane * 16), v2 = rd128<2048>(rtl + lane * 16), v3 = rd128<3072>(rtl + lane * 16);
        asm volatile("s_waitcnt lgkmcnt(0)" : "+v"(v0), "+v"(v1), "+v"(v2), "+v"(v3) :: "memory");
        vv[0] = v0; vv[1] = v1; vv[2] = v2; vv[3] = v3;
      }
    }
    BAR();
    if (has_unit) {
#pragma unroll
      for (int i = 0; i < 4; ++i) {
        const int c = ffn_unit ? c0 + i : c0, s = ffn_unit ? s0u : s0u + i;
        st_sc1(vv[i], rW2T, lane * 16 + (((c / 5) * NSTEP + s) * 5 + (c % 5)) * 1024);
      }
    }
    stats_job(r0, r1);
  }
  TS(1);
  auto hook0 = [&]() {};
  auto hook7 = [&]() { if (tid == 0) __hip_atomic_store((gu32*)(p.ws + WS_FLAGW) + blk, etag, RLX_AGENT); };
  const __amdgpu_buffer_rsrc_t rACT = __builtin_amdgcn_make_buffer_rsrc((void*)(p.ws + WS_ACT), 0, (int)ACT_BYTES, RSRC3);
  const __amdgpu_buffer_rsrc_t rFW = __builtin_amdgcn_make_buffer_rsrc((void*)(p.ws + WS_FLAGW), 0, 1024, RSRC3);
  const int mt64 = 4 * mt + wr;
  gu64* stg;
  unsigned long long sta = 0, stb = 0;
  u32x4 fw;
  auto hookA = [&]() { const int vo_ = lane * 16; asm volatile("buffer_load_dwordx4 %0, %1, %2, 0 offen sc1" : "=&v"(fw) : "v"(vo_), "s"(rFW) : "memory"); };
  auto hook8 = [&]() {
    if constexpr (SSM) asm volatile("s_waitcnt vmcnt(6)" : "+v"(fw) :: "memory"); else asm volatile("s_waitcnt vmcnt(7)" : "+v"(fw) :: "memory");
    if (w == 0) {
      unsigned spins = 0;
      while (!__all(fw.x == etag && fw.y == etag && fw.z == etag && fw.w == etag)) {
        if (++spins > SPIN_MAX) break;
        __builtin_amdgcn_s_sleep(1);
        fw = __builtin_amdgcn_raw_buffer_load_b128(rFW, lane * 16, 0, 16);
      }
      if (blk == 0 && lane == 0) __hip_atomic_store((gu32*)(p.ws + WS_EPOCH), etag, RLX_AGENT);
    }
  };
  auto hook9 = [&](auto mode_tag) {
    int tz = threadIdx.x; asm volatile("" : "+v"(tz));
    stg = (gu64*)(p.ws + WS_STATS + (size_t)(256 * mt + 64 * wr + (tz & 63)) * 16);
    sta = __hip_atomic_load(stg, RLX_AGENT); stb = __hip_atomic_load(stg + 1, RLX_AGENT);
  };
  f32x4 csa = {0.f, 0.f, 0.f, 0.f};
  float* cst = (float*)(smem + LDS_CS);
  float* stt = (float*)(smem + LDS_ST);
  auto finish_common = [&](int ntw  , int tile0  ) {
    unsigned spins = 0;
    while (!__all((unsigned)(sta >> 32) == etag && (unsigned)(stb >> 32) == etag)) {
      if (++spins > SPIN_MAX) break;
      __builtin_amdgcn_s_sleep(1);
      sta = __hip_atomic_load(stg, RLX_AGENT); stb = __hip_atomic_load(stg + 1, RLX_AGENT);
    }
    const float mean = __uint_as_float((unsigned)sta), rstd = __uint_as_float((unsigned)stb);
    if (wc == 0) *(f32x2*)(stt + (64 * wr + lane) * 2) = f32x2{rstd, -mean * rstd};
    if (SSM) { if (G == 0) {
#pragma unroll
        for (int c = 0; c < 4; ++c) cst[wr * 160 + (tile0 + c) * 16 + i16] = csa[c]; } }
    else { if (i16 < ntw) *(f32x4*)(cst + wr * 160 + (tile0 + i16) * 16 + 4 * G) = csa; }
    WAIT_L0();
    BAR();
  };
  auto cs4 = [&](int idx) { return (*(const f32x4*)(cst + idx) + *(const f32x4*)(cst + 160 + idx)) + (*(const f32x4*)(cst + 320 + idx) + *(const f32x4*)(cst + 480 + idx)); };
  auto cs1 = [&](int idx) { return (cst[idx] + cst[160 + idx]) + (cst[320 + idx] + cst[480 + idx]); };

  if (!SSM) {
    auto ffn = [&](auto mode_tag) {
      constexpr int MODE = decltype(mode_tag)::value;
      constexpr int NT = Frags<MODE>::NT, P0 = MODE == 0 ? 0 : 3;
      f32x4 acc[4][NT];
#pragma unroll
      for (int T = 0; T < 4; ++T)
#pragma unroll
        for (int c = 0; c < NT; ++c) acc[T][c] = f32x4{0.f, 0.f, 0.f, 0.f};
      main_loop<MODE>(p, cx, lbase, w, lane, acc, csa, hook0, hook7, hookA, hook8, hook9);
      TS(2);
      relane();
      finish_common(NT, 2 * P0);
      constexpr int NB = 4 * (NT / 2);
      auto ldw_part = [&](int b) {
#pragma unroll
        for (int j = 0; j < 20; ++j)
          if (j * NB / 20 == b) wv[j / 5][j % 5] = __builtin_amdgcn_raw_buffer_load_b128(p2c.rw, p2c.vo, p2c.sw + (j / 5 == 3 ? 3 : 6 + j / 5) * 5120 + (j % 5) * 1024, 16);
      };
      f32x2 st4[4];
#pragma unroll
      for (int T = 0; T < 4; ++T) st4[T] = *(const f32x2*)(stt + (64 * wr + 16 * T + i16) * 2);
#pragma unroll
      for (int Pq = 0; Pq < NT / 2; ++Pq) {
        const int ft = 5 * cj + P0 + Pq;
        const f32x4 cg = cs4((2 * P0 + 2 * Pq) * 16 + 4 * G), cu = cs4((2 * P0 + 2 * Pq + 1) * 16 + 4 * G);
        unsigned pk[4][2];
#pragma unroll
        for (int T = 0; T < 4; ++T) {
          float hv[4];
#pragma unroll
          for (int e = 0; e < 4; ++e) {
            const float g = fmaf(acc[T][2 * Pq][e], st4[T].x, st4[T].y * cg[e]);
            const float u = fmaf(acc[T][2 * Pq + 1][e], st4[T].x, st4[T].y * cu[e]);
            hv[e] = silu_f(g) * u;
          }
          pk[T][0] = pack2(hv[0], hv[1]); pk[T][1] = pack2(hv[2], hv[3]);
          ldw_part(4 * Pq + T);
          SB();
        }
        if (ft < 108) {
#pragma unroll
          for (int T2 = 0; T2 < 4; T2 += 2) {
            const auto a = __builtin_amdgcn_permlane16_swap(pk[T2][0], pk[T2 + 1][0], false, false);
            const auto b = __builtin_amdgcn_permlane16_swap(pk[T2][1], pk[T2 + 1][1], false, false);
            const u32x4 v = {a[0], b[0], a[1], b[1]};
            const int s2 = ft >> 1, gp = 2 * (ft & 1) + (G >> 1), T = T2 + (G & 1);
            st_sc1(v, rACT, T * 1024 + (gp * 16 + i16) * 16 + (mt64 * NSTEP + s2) * 4096);
          }
        }
      }
    };
    if (wc == 0) ffn(std::integral_constant<int, 0>{}); else ffn(std::integral_constant<int, 1>{});
  } else {
    f32x4 acc[4][4];
#pragma unroll
    for (int T = 0; T < 4; ++T)
#pragma unroll
      for (int c = 0; c < 4; ++c) acc[T][c] = f32x4{0.f, 0.f, 0.f, 0.f};
    main_loop<2>(p, cx, lbase, w, lane, acc, csa, hook0, hook7, hookA, hook8, hook9);
    TS(2);
    relane();
    const float* par = (const float*)(smem + LDS_PAR);
    float lr[2], li[2], cr[2], ci[2];
#pragma unroll
    for (int j = 0; j < 2; ++j) {
      const f32x4 pv = *(const f32x4*)(par + (32 * wc + 16 * j + i16) * 8);
      lr[j] = pv.x; li[j] = pv.y; cr[j] = pv.z; ci[j] = pv.w;
    }
    f32x4 pv = *(const f32x4*)(par + lane * 8 + 4);
    asm volatile("" : "+v"(pv), "+v"(lr[0]), "+v"(li[0]), "+v"(cr[0]), "+v"(ci[0]), "+v"(lr[1]), "+v"(li[1]), "+v"(cr[1]), "+v"(ci[1]));
    finish_common(4, 4 * wc);
    {
      float csl[4];
#pragma unroll
      for (int c = 0; c < 4; ++c) csl[c] = cs1((4 * wc + c) * 16 + i16);
#pragma unroll
      for (int T = 0; T < 4; ++T)
#pragma unroll
        for (int e = 0; e < 4; e += 2) {
          const f32x4 sv = *(const f32x4*)(stt + (64 * wr + 16 * G + 4 * T + e) * 2);
#pragma unroll
          for (int c = 0; c < 4; ++c) {
            acc[T][c][e] = fmaf(acc[T][c][e], sv.x, sv.y * csl[c]);
            acc[T][c][e + 1] = fmaf(acc[T][c][e + 1], sv.z, sv.w * csl[c]);
          }
        }
    }
    f32x2* et = (f32x2*)(smem + LDS_ET);
    const int seg = 4 * wr + G;
#pragma unroll
    for (int j = 0; j < 2; ++j) {
      float hr = 0.f, hi = 0.f;
#pragma unroll
      for (int i = 0; i < 16; ++i) {
        const float nr = fmaf(lr[j], hr, fmaf(-li[j], hi, acc[i >> 2][2 * j][i & 3]));
        const float ni = fmaf(lr[j], hi, fmaf(li[j], hr, acc[i >> 2][2 * j + 1][i & 3]));
        hr = nr; hi = ni;
      }
      et[seg * 64 + 32 * wc + 16 * j + i16] = f32x2{hr, hi};
    }
    WAIT_L0(); BAR();
    TS(3);
    if (w == 0) {
      const unsigned tag = etag;
      float xr_ = 0.f, xi_ = 0.f;
      f32x2 ev[16];
#pragma unroll
      for (int s = 0; s < 16; ++s) ev[s] = et[s * 64 + lane];
#pragma unroll
      for (int s = 0; s < 16; ++s) {
        const float nr = fmaf(pv.x, xr_, fmaf(-pv.y, xi_, ev[s].x));
        const float ni = fmaf(pv.x, xi_, fmaf(pv.y, xr_, ev[s].y));
        xr_ = nr; xi_ = ni;
      }
      gu64* gran = (gu64*)(p.ws + WS_GRAN);
      if (mt < 7) {
        gu64* g = gran + ((size_t)(mt * 10 + cj) * 64 + lane) * 2;
        __hip_atomic_store(g, ((unsigned long long)tag << 32) | __float_as_uint(xr_), RLX_AGENT);
        __hip_atomic_store(g + 1, ((unsigned long long)tag << 32) | __float_as_uint(xi_), RLX_AGENT);
      }
      float Hr = 0.f, Hi = 0.f;
      if (mt > 0) {
        unsigned long long ga[7], gb[7];
        unsigned spins = 0;
        while (true) {
          bool ok = true;
#pragma unroll
          for (int m = 0; m < 7; ++m) {
            if (m < mt) {
              gu64* g = gran + ((size_t)(m * 10 + cj) * 64 + lane) * 2;
              ga[m] = __hip_atomic_load(g, RLX_AGENT); gb[m] = __hip_atomic_load(g + 1, RLX_AGENT);
            } else { ga[m] = gb[m] = (unsigned long long)tag << 32; }
          }
#pragma unroll
          for (int m = 0; m < 7; ++m) ok = ok && ((unsigned)(ga[m] >> 32) == tag) && ((unsigned)(gb[m] >> 32) == tag);
          if (__all(ok)) break;
          if (++spins > SPIN_MAX) break;
          __builtin_amdgcn_s_sleep(1);
        }
#pragma unroll
        for (int m = 0; m < 7; ++m) {
          if (m < mt) {
            const float sr = __uint_as_float((unsigned)ga[m]), si = __uint_as_float((unsigned)gb[m]);
            const float nr = fmaf(pv.z, Hr, fmaf(-pv.w, Hi, sr));
            const float ni = fmaf(pv.z, Hi, fmaf(pv.w, Hr, si));
            Hr = nr; Hi = ni;
          }
        }
      }
      xr_ = Hr; xi_ = Hi;
#pragma unroll
      for (int s = 0; s < 16; ++s) {
        et[s * 64 + lane] = f32x2{xr_, xi_};
        const float nr = fmaf(pv.x, xr_, fmaf(-pv.y, xi_, ev[s].x));
        const float ni = fmaf(pv.x, xi_, fmaf(pv.y, xr_, ev[s].y));
        xr_ = nr; xi_ = ni;
      }
    }
    WAIT_L0(); BAR();
    TS(4);
    p2_ldw(p2c, wv[0], 6); p2_ldw(p2c, wv[1], 7);
    SB();
    f32x2 x0[2];
#pragma unroll
    for (int j = 0; j < 2; ++j) x0[j] = et[seg * 64 + 32 * wc + 16 * j + i16];
    unsigned pre[2][8], pim[2][8];
#pragma unroll
    for (int j = 0; j < 2; ++j) {
      float hr = x0[j].x, hi = x0[j].y;
      float ore[16], oim[16];
#pragma unroll
      for (int i = 0; i < 16; ++i) {
        const float nr = fmaf(lr[j], hr, fmaf(-li[j], hi, acc[i >> 2][2 * j][i & 3]));
        const float ni = fmaf(lr[j], hi, fmaf(li[j], hr, acc[i >> 2][2 * j + 1][i & 3]));
        hr = nr; hi = ni;
        ore[i] = cr[j] * hr - ci[j] * hi;
        oim[i] = -(cr[j] * hi + ci[j] * hr);
      }
#pragma unroll
      for (int i = 0; i < 8; ++i) { pre[j][i] = pack2(ore[2 * i], ore[2 * i + 1]); pim[j][i] = pack2(oim[2 * i], oim[2 * i + 1]); }
    }
    {
      char* tp = smem + LDS_TP + w * 4608;
      const int q = (lane >> 2) & 3, pp = lane & 3;
      const unsigned tb = lbase + LDS_TP + w * 4608 + (8 * G + q) * 144 + 8 * pp;
      const int jg = 2 * cj + wc;
#pragma unroll
      for (int part = 0; part < 2; ++part) {
#pragma unroll
        for (int j = 0; j < 2; ++j) {
          char* t0 = tp + (16 * j + i16) * 144 + 32 * G;
          const unsigned* src = part ? pim[j] : pre[j];
          *(u32x4*)t0 = u32x4{src[0], src[1], src[2], src[3]};
          *(u32x4*)(t0 + 16) = u32x4{src[4], src[5], src[6], src[7]};
        }
        WAIT_L0();
        u32x2 lo[4], hi[4];
        lo[0] = trrd<0>(tb); hi[0] = trrd<576>(tb); lo[1] = trrd<32>(tb); hi[1] = trrd<576 + 32>(tb); lo[2] = trrd<64>(tb); hi[2] = trrd<576 + 64>(tb); lo[3] = trrd<96>(tb); hi[3] = trrd<576 + 96>(tb);
        asm volatile("s_waitcnt lgkmcnt(0)" : "+v"(lo[0]), "+v"(hi[0]), "+v"(lo[1]), "+v"(hi[1]), "+v"(lo[2]), "+v"(hi[2]), "+v"(lo[3]), "+v"(hi[3]) :: "memory");
        const int s2 = NSTEP_F + 2 * jg + part;
#pragma unroll
        for (int T = 0; T < 4; ++T)
          st_sc1(cat(lo[T], hi[T]), rACT, T * 1024 + lane * 16 + (mt64 * NSTEP + s2) * 4096);
      }
    }
  }
  TS(5);
  WAIT_V(0);
  BAR();
  if (tid == 0) __hip_atomic_store((gu32*)(p.ws + WS_FLAGA + mt * 128) + cbi, etag, RLX_AGENT);
  asm volatile("" ::: "memory"); SB();
  if (SSM) { p2_ldw(p2c, wv[2], 8); p2_ldw(p2c, wv[3], 3); }
  p2_ldw(p2c, wv[4], 4); p2_ldw(p2c, wv[5], 5);
  SB();
  {
    char* lw = smem + LDS_W2 + w * 15360 + lane * 16;
#pragma unroll
    for (int k = 0; k < 3; ++k)
#pragma unroll
      for (int c = 0; c < 5; ++c) *(u32x4*)(lw + k * 5120 + c * 1024) = wv[k][c];
  }
  SB();
  p2_ldw(p2c, wv[0], 0); p2_ldw(p2c, wv[1], 1); p2_ldw(p2c, wv[2], 2);
  SB();
  return etag;
}

DI void p2_run(const Params& p, const char* lw  , __amdgpu_buffer_rsrc_t ra, const P2Ctx& p2c, u32x4 (&wv)[DW][5],
               int sa, int N, int w, int lane, int mt2, int shA, int shW, unsigned etag, f32x4 (&acc)[4][5]) {
  constexpr int DA = 2, NMIN = 10, NMAX = 14;
  u32x4 a[DA][4];
  const int vo = p2c.vo;
  auto lda = [&](int k, int slot) {
#pragma unroll
    for (int T = 0; T < 4; ++T) a[slot][T] = __builtin_amdgcn_raw_buffer_load_b128(ra, vo, sa + k * 4096 + T * 1024, 16);
  };
  {
    const int fi = w < 4 ? (lane < 22 ? lane : 0) : 22 + (lane < 10 ? lane : 0);
    gu32* fl = (gu32*)(p.ws + WS_FLAGA + mt2 * 128) + fi;
    unsigned spins = 0;
    while (!__all(__hip_atomic_load(fl, RLX_AGENT) == etag)) {
      if (++spins > SPIN_MAX) break;
      __builtin_amdgcn_s_sleep(1);
    }
  }
  __builtin_amdgcn_fence(__ATOMIC_ACQUIRE, "wavefront");
  TS(9);
#pragma unroll
  for (int s = 0; s < DA - 1; ++s) lda(s, s);
#pragma unroll
  for (int T = 0; T < 4; ++T)
#pragma unroll
    for (int c = 0; c < 5; ++c) asm volatile("" : "+v"(acc[T][c]));
  asm volatile("s_nop 7" ::: "memory");
  SB();
#pragma unroll
  for (int s = 0; s < NMAX; ++s) {
    if (s < NMIN || s < N) {
      if (s + DA - 1 < NMIN || s + DA - 1 < N) lda(s + DA - 1, (s + DA - 1) % DA);
      SB();
#pragma unroll
      for (int T = 0; T < 4; ++T)
#pragma unroll
        for (int c = 0; c < 5; ++c) mfma16_acc(acc[T][c], wv[s % DW][c], a[s % DA][T]);
      SB();
      if (s < 3) {
#pragma unroll
        for (int c = 0; c < 5; ++c) wv[s][c] = *(const u32x4*)(lw + s * 5120 + c * 1024);
      } else if (s + DW < NMAX) { if (s + DW < NMIN || s + DW < N) p2_ldw(p2c, wv[s % DW], s + DW); }
      SB();
    }
  }
  asm volatile("s_nop 15\n\ts_nop 7" ::: "memory");
}

DI void phase2(const Params& p, char* smem, unsigned etag, const P2Ctx& p2c, u32x4 (&wv)[DW][5], int mt64, int nt, int s0) {
  const int tid = threadIdx.x, lane = tid & 63, w = __builtin_amdgcn_readfirstlane(tid >> 6);
  TS(8);
  f32x4 acc[4][5];
#pragma unroll
  for (int T = 0; T < 4; ++T)
#pragma unroll
    for (int c = 0; c < 5; ++c) acc[T][c] = f32x4{0.f, 0.f, 0.f, 0.f};
  const __amdgpu_buffer_rsrc_t ra = __builtin_amdgcn_make_buffer_rsrc((void*)(p.ws + WS_ACT), 0, (int)ACT_BYTES, RSRC3);
  const int sa = (mt64 * NSTEP + s0) * 4096;
  const int shA = nt & 3, shW = mt64 & 7;
  const int N = w < 2 ? 14 : (w < 4 ? 13 : 10);
  const char* lw = smem + LDS_W2 + w * 15360 + lane * 16;
  p2_run(p, lw, ra, p2c, wv, sa, N, w, lane, mt64 >> 2, shA, shW, etag, acc);
  auto part_addr = [&](int wv_, int t) { return t < 15 ? LDS_W2 + wv_ * (15360) + t * 1024 : wv_ * 5120 + (t - 15) * 1024; };
  {
    const int g = lane >> 4, r = lane & 15;
    const int pos = g * 16 + ((r + 4 * g) & 15);
#pragma unroll
    for (int T = 0; T < 4; ++T)
#pragma unroll
      for (int c = 0; c < 5; ++c) *(f32x4*)(smem + part_addr(w, T * 5 + c) + pos * 16) = acc[T][c];
  }
  f32x4 res[3]; u32x2 xv[3], dv[3]; unsigned long long ga[3], gb[3];
#pragma unroll
  for (int it = 0; it < 3; ++it) {
    const int item = tid + 512 * it;
    if (item < 1280) {
      const int tok = item / 20, dg = item % 20;
      const int gt = mt64 * 64 + tok, d0 = nt * 80 + dg * 4;
      gu64* sg = (gu64*)(p.ws + WS_STATS + (size_t)gt * 16);
      ga[it] = __hip_atomic_load(sg, RLX_AGENT); gb[it] = __hip_atomic_load(sg + 1, RLX_AGENT);
      xv[it] = *(const u32x2*)(p.x + (size_t)gt * D + d0);
      dv[it] = *(const u32x2*)(p.dsk + d0);
    }
  }
  __syncthreads();
  TS(10);
#pragma unroll
  for (int it = 0; it < 3; ++it) {
    const int item = tid + 512 * it;
    if (item < 1280) {
      const int tok = item / 20, dg = item % 20, c = dg >> 2, g = dg & 3, T = tok >> 4, rr = tok & 15;
      const int t = T * 5 + c, off = (g * 16 + ((rr + 4 * g) & 15)) * 16;
      const int gt = mt64 * 64 + tok, d0 = nt * 80 + dg * 4;
      const int ta = t < 15 ? LDS_W2 + t * 1024 : (t - 15) * 1024, st = t < 15 ? 15360 : 5120;
      const char* base = smem + ta + off;
      f32x4 o = *(const f32x4*)(base);
#pragma unroll
      for (int k = 1; k < 4; ++k) o += *(const f32x4*)(base + k * st);
      f32x4 y = *(const f32x4*)(base + 4 * st);
#pragma unroll
      for (int k = 5; k < 8; ++k) y += *(const f32x4*)(base + k * st);
      const float mean = __uint_as_float((unsigned)ga[it]), rstd = __uint_as_float((unsigned)gb[it]);
      const float xs[4] = {__uint_as_float(xv[it].x << 16), __uint_as_float(xv[it].x & 0xFFFF0000u), __uint_as_float(xv[it].y << 16), __uint_as_float(xv[it].y & 0xFFFF0000u)};
      const float ds[4] = {__uint_as_float(dv[it].x << 16), __uint_as_float(dv[it].x & 0xFFFF0000u), __uint_as_float(dv[it].y << 16), __uint_as_float(dv[it].y & 0xFFFF0000u)};
#pragma unroll
      for (int e = 0; e < 4; ++e) {
        const float xn = (xs[e] - mean) * rstd;
        const float yy = fmaf(2.0f, y[e], xn * ds[e]);
        res[it][e] = fmaf(yy, silu_f(o[e]), xs[e]);
      }
      *(f32x4*)(p.out + (size_t)gt * D + d0) = res[it];
    }
  }
  TS(11);
}

__global__ __launch_bounds__(512, 2) void k_fused(Params p) {
  extern __shared__ __attribute__((aligned(16))) char smem[];
  const int b = blockIdx.x, xcd = b & 7, i = b >> 3;
  const int ml = i & 3, cl = i >> 2;
  const int mt = (xcd & 1) * 4 + ml, cb = (xcd >> 1) * 8 + cl;
  const int rot = 0;
  const int w = __builtin_amdgcn_readfirstlane(threadIdx.x >> 6), lane = threadIdx.x & 63;
  const int mt64 = (xcd >> 1) * 8 + (i & 7), nt = (xcd & 1) * 4 + (i >> 3);
  const int s0 = w < 4 ? (w < 2 ? 14 * w : 28 + 13 * (w - 2)) : 54 + 10 * (w - 4);
  P2Ctx p2c;
  p2c.rw = __builtin_amdgcn_make_buffer_rsrc((void*)(p.ws + WS_W2T), 0, (int)W2T_BYTES, RSRC3);
  p2c.sw = (nt * NSTEP + s0) * 5120; p2c.vo = 0;
  u32x4 wv[DW][5];
  unsigned etag;
  if (cb < 22) etag = phase1<false>(p, smem, mt, cb, b, rot, p2c, wv);
  else etag = phase1<true>(p, smem, mt, cb - 22, b, rot, p2c, wv);
  phase2(p, smem, etag, p2c, wv, mt64, nt, s0);
}
}

extern "C" void kernel_launch(void* const* d_in, const int* in_sizes, int n_in, void* d_out, int out_size, void* d_ws, size_t ws_size, hipStream_t stream) {
  using namespace s5;
  Params p;
  p.x = (const u16*)d_in[0]; p.lre = (const u16*)d_in[1]; p.lim = (const u16*)d_in[2];
  p.bre = (const u16*)d_in[3]; p.bim = (const u16*)d_in[4]; p.cre = (const u16*)d_in[5]; p.cim = (const u16*)d_in[6];
  p.ldt = (const u16*)d_in[7]; p.dsk = (const u16*)d_in[8];
  p.w1 = (const u16*)d_in[10]; p.w3 = (const u16*)d_in[11]; p.w2 = (const u16*)d_in[12];
  p.out = (float*)d_out; p.ws = (char*)d_ws;
  (void)hipFuncSetAttribute((const void*)k_fused, hipFuncAttributeMaxDynamicSharedMemorySize, LDS1_BYTES);
  k_fused<<<256, 512, LDS1_BYTES, stream>>>(p);
}
```

```cpp
#include <hip/hip_runtime.h>
#include <stdint.h>
#include <type_traits>

namespace s5 {
typedef unsigned short u16;
typedef short bf16x8 __attribute__((ext_vector_type(8)));
typedef float f32x4 __attribute__((ext_vector_type(4)));
typedef float f32x2 __attribute__((ext_vector_type(2)));
typedef unsigned u32x4 __attribute__((ext_vector_type(4)));
typedef unsigned u32x2 __attribute__((ext_vector_type(2)));
typedef __bf16 bf16x2 __attribute__((ext_vector_type(2)));
typedef __attribute__((address_space(3))) void* lds_vp;
typedef __attribute__((address_space(3))) char* lds_cp;
typedef __attribute__((address_space(1))) unsigned long long gu64;
typedef __attribute__((address_space(1))) unsigned gu32;
#define DI __device__ __forceinline__
#define RLX_AGENT __ATOMIC_RELAXED, __HIP_MEMORY_SCOPE_AGENT

constexpr int L = 2048, D = 640, P = 640, F = 1728;
constexpr int NSTEP = 94, NSTEP_F = 54;
constexpr size_t WS_EPOCH = 0;
constexpr size_t WS_FLAGW = 1024;
constexpr size_t WS_FLAGA = 4096;
constexpr size_t WS_GRAN = 8192;
constexpr size_t WS_STATS = WS_GRAN + 7 * 10 * 1024 + 2048;
constexpr size_t WS_ACT = 262144;
constexpr int NSTR = NSTEP + 1;
constexpr size_t ACT_BYTES = (size_t)32 * NSTR * 4 * 1024;
constexpr size_t WS_W2T = WS_ACT + ACT_BYTES;
constexpr size_t W2T_BYTES = (size_t)8 * NSTR * 5 * 1024;
constexpr size_t WS_STAMP = 32u << 20;
static_assert(WS_STATS + 2048 * 16 <= WS_ACT, "ws map");
constexpr int XSLOT = 32768, WSLOT = 20480, SLOT = XSLOT + WSLOT;
constexpr int LDS_PAR = 3 * SLOT;
constexpr int LDS1_BYTES = 163840;
constexpr int LDS_CS = SLOT;
constexpr int LDS_ST = SLOT + 4096;
constexpr int LDS_ET = SLOT + 8192;
constexpr int LDS_TP = SLOT + 16384;
constexpr int LDS_W2 = 40960;
constexpr int LDS_RT = 2 * SLOT;
static_assert(LDS1_BYTES <= 163840, "lds");
constexpr int RSRC3 = 0x00020000;
constexpr unsigned SPIN_MAX = 1u << 21;

struct Params {
  const u16 *x, *lre, *lim, *bre, *bim, *cre, *cim, *ldt, *dsk, *w1, *w3, *w2;
  float* out;
  char* ws;
};

DI float bf2f(u16 v) { return __uint_as_float(((unsigned)v) << 16); }
DI float rbf(float f) { unsigned u = __float_as_uint(f); u += 0x7FFFu + ((u >> 16) & 1u); return __uint_as_float(u & 0xFFFF0000u); }
DI unsigned pack2(float a, float b) { f32x2 p = {a, b}; return __builtin_bit_cast(unsigned, __builtin_convertvector(p, bf16x2)); }
DI f32x4 mfma16(u32x4 a, u32x4 b, f32x4 c) { return __builtin_amdgcn_mfma_f32_16x16x32_bf16(__builtin_bit_cast(bf16x8, a), __builtin_bit_cast(bf16x8, b), c, 0, 0, 0); }
DI void mfma16_acc(f32x4& c, u32x4 a, u32x4 b) { asm volatile("v_mfma_f32_16x16x32_bf16 %0, %1, %2, %0" : "+v"(c) : "v"(a), "v"(b)); }
template <int OFF> DI u32x2 trrd(unsigned addr) { u32x2 r; asm volatile("ds_read_b64_tr_b16 %0, %1 offset:%2" : "=v"(r) : "v"(addr), "i"(OFF) : "memory"); return r; }
template <int OFF> DI u32x4 rd128(unsigned addr) { u32x4 r; asm volatile("ds_read_b128 %0, %1 offset:%2" : "=v"(r) : "v"(addr), "i"(OFF) : "memory"); return r; }
DI u32x4 cat(u32x2 a, u32x2 b) { return u32x4{a.x, a.y, b.x, b.y}; }
DI void dma16(__amdgpu_buffer_rsrc_t rs, char* lds, int voff, int soff) { __builtin_amdgcn_raw_ptr_buffer_load_lds(rs, (lds_vp)lds, 16, voff, soff, 0, 0); }
DI void st_sc1(u32x4 v, __amdgpu_buffer_rsrc_t rs, int off) { __builtin_amdgcn_raw_buffer_store_b128(v, rs, off, 0, 16); }
#define SB() __builtin_amdgcn_sched_barrier(0)
#define WAIT_V(n) asm volatile("s_waitcnt vmcnt(" #n ")" ::: "memory")
#define WAIT_L0() asm volatile("s_waitcnt lgkmcnt(0)" ::: "memory")
#define BAR() __builtin_amdgcn_s_barrier()
#define LBAR() __builtin_amdgcn_s_barrier()
#define TS(i) do {} while (0)
DI float wave_sum(float v) {
  v += __builtin_bit_cast(float, __builtin_amdgcn_update_dpp(0, __builtin_bit_cast(int, v), 0x111, 0xf, 0xf, true));
  v += __builtin_bit_cast(float, __builtin_amdgcn_update_dpp(0, __builtin_bit_cast(int, v), 0x112, 0xf, 0xf, true));
  v += __builtin_bit_cast(float, __builtin_amdgcn_update_dpp(0, __builtin_bit_cast(int, v), 0x114, 0xf, 0xf, true));
  v += __builtin_bit_cast(float, __builtin_amdgcn_update_dpp(0, __builtin_bit_cast(int, v), 0x118, 0xf, 0xf, true));
  const int iv = __builtin_bit_cast(int, v);
  const float a = __builtin_bit_cast(float, __builtin_amdgcn_readlane(iv, 15)), b = __builtin_bit_cast(float, __builtin_amdgcn_readlane(iv, 31));
  const float c = __builtin_bit_cast(float, __builtin_amdgcn_readlane(iv, 47)), d = __builtin_bit_cast(float, __builtin_amdgcn_readlane(iv, 63));
  return (a + b) + (c + d);
}
DI float silu_f(float g) { return g * __builtin_amdgcn_rcpf(1.0f + __builtin_amdgcn_exp2f(g * -1.44269504088896f)); }

template <int MODE> struct Frags {
  static constexpr int NT = MODE == 0 ? 6 : 4;
  u32x4 x[4];
  u32x2 lo[MODE == 2 ? 1 : NT], hi[MODE == 2 ? 1 : NT];
  u32x4 v[MODE == 2 ? NT : 1];
  template <int KK, int C> DI void rdw(unsigned aw) {
    if constexpr (MODE == 2) v[C] = rd128<C * 2048>(aw);
    else { constexpr int O = KK * 10240 + (C & 1) * 2560 + ((MODE == 0 ? 0 : 3) + (C >> 1)) * 32; lo[C] = trrd<O>(aw); hi[C] = trrd<O + 1280>(aw); }
  }
  template <int KK, int C> DI void rdw_lo(unsigned aw) {
    if constexpr (MODE == 2) v[C] = rd128<C * 2048>(aw);
    else { constexpr int O = KK * 10240 + (C & 1) * 2560 + ((MODE == 0 ? 0 : 3) + (C >> 1)) * 32; lo[C] = trrd<O>(aw); }
  }
  template <int KK, int C> DI void rdw_hi(unsigned aw) {
    if constexpr (MODE != 2) { constexpr int O = KK * 10240 + (C & 1) * 2560 + ((MODE == 0 ? 0 : 3) + (C >> 1)) * 32; hi[C] = trrd<O + 1280>(aw); }
  }
  template <int T> DI void rdx(unsigned ax) { x[T] = rd128<(MODE == 2 ? 512 : 2048) * T>(ax); }
  DI u32x4 w(int c) const { if constexpr (MODE == 2) return v[c]; else return cat(lo[c], hi[c]); }
  DI void wait() {
    if constexpr (MODE == 0) asm volatile("s_waitcnt lgkmcnt(0)" : "+v"(x[0]), "+v"(x[1]), "+v"(x[2]), "+v"(x[3]), "+v"(lo[0]), "+v"(hi[0]), "+v"(lo[1]), "+v"(hi[1]), "+v"(lo[2]), "+v"(hi[2]),
                                          "+v"(lo[3 % NT]), "+v"(hi[3 % NT]), "+v"(lo[4 % NT]), "+v"(hi[4 % NT]), "+v"(lo[5 % NT]), "+v"(hi[5 % NT]) :: "memory");
    else if constexpr (MODE == 1) asm volatile("s_waitcnt lgkmcnt(0)" : "+v"(x[0]), "+v"(x[1]), "+v"(x[2]), "+v"(x[3]), "+v"(lo[0]), "+v"(hi[0]), "+v"(lo[1]), "+v"(hi[1]), "+v"(lo[2]), "+v"(hi[2]), "+v"(lo[3]), "+v"(hi[3]) :: "memory");
    else asm volatile("s_waitcnt lgkmcnt(0)" : "+v"(x[0]), "+v"(x[1]), "+v"(x[2]), "+v"(x[3]), "+v"(v[0]), "+v"(v[1 % NT]), "+v"(v[2 % NT]), "+v"(v[3 % NT]) :: "memory");
  }
};

struct Ctx {
  __amdgpu_buffer_rsrc_t rx, rw;
  int vx[4];
  int vw[3];
  int xso_base, wso_base, wso_step;
  int rot;
  char* smem;
  int xdst, wdst;
};

template <int MODE, class H0, class H7, class HA, class H8, class H9>
DI void main_loop(const Params& p, const Ctx& cx, unsigned lbase, int w, int lane, f32x4 (&acc)[4][Frags<MODE>::NT], f32x4& csa, H0 hook0, H7 hook7, HA hookA, H8 hook8, H9 hook9) {
  constexpr bool SSM = MODE == 2;
  constexpr int NT = Frags<MODE>::NT;
  const int wr = w & 3, wc = w >> 2;
  const int G = lane >> 4, i16 = lane & 15;
  unsigned ax[2], aw[2];
  if (SSM) {
    const int row = 64 * wr + 16 * (i16 >> 2) + (i16 & 3), f = ((i16 & 3) >> 1) | ((i16 >> 2) << 1);
#pragma unroll
    for (int kk = 0; kk < 2; ++kk) ax[kk] = lbase + row * 128 + (((4 * kk + G) ^ f) << 4);
    const int f2 = (i16 >> 1) & 7;
#pragma unroll
    for (int kk = 0; kk < 2; ++kk) aw[kk] = lbase + XSLOT + (64 * wc + i16) * 128 + (((4 * kk + G) ^ f2) << 4);
  } else {
    const int row = 64 * wr + i16, f = (i16 >> 1) & 7;
#pragma unroll
    for (int kk = 0; kk < 2; ++kk) ax[kk] = lbase + row * 128 + (((4 * kk + G) ^ f) << 4);
    const int q = (lane >> 2) & 3, pp = lane & 3;
    aw[0] = aw[1] = lbase + XSLOT + (G >> 1) * 5120 + (4 * (G & 1) + q) * 160 + pp * 8;
  }
  auto kchunk = [&](int s) { int k = s + cx.rot; return k >= 10 ? k - 10 : k; };
  auto dma = [&](int slot, int sox, int sow, int i) {
    char* b = cx.smem + slot;
    if (i < 4) dma16(cx.rx, b + cx.xdst + i * 1024, cx.vx[i], sox + i * 10240);
    else if (SSM) { if (i < 6) dma16(cx.rw, b + XSLOT + cx.wdst + (i - 4) * 1024, cx.vw[i - 4], sow + (i - 4) * 10240); }
    else if (i < 6) dma16(cx.rw, b + XSLOT + cx.wdst + (i - 4) * 1024, cx.vw[i - 4], sow);
    else { if (lane < 32) dma16(cx.rw, b + XSLOT + cx.wdst + 2048, cx.vw[2], sow); }
  };
  Frags<MODE> fa, fb;
  fa.template rdx<0>(ax[0]); fa.template rdx<1>(ax[0]); fa.template rdx<2>(ax[0]); fa.template rdx<3>(ax[0]);
  fa.template rdw<0, 0>(aw[0]); fa.template rdw<0, 1>(aw[0]); fa.template rdw<0, 2>(aw[0]); fa.template rdw<0, 3>(aw[0]);
  if constexpr (NT > 4) { fa.template rdw<0, 4 % NT>(aw[0]); fa.template rdw<0, 5 % NT>(aw[0]); }

#define MM1(F_, C, T) acc[T][C] = SSM ? mfma16(F_.x[T], F_.w(C), acc[T][C]) : mfma16(F_.w(C), F_.x[T], acc[T][C])
#define MMG(F_, C) do { MM1(F_, C, 0); MM1(F_, C, 1); MM1(F_, C, 2); MM1(F_, C, 3); } while (0)
#define NREADS (SSM ? 8 : 4 + 2 * NT)
#define RD(N_, KK, I, WA, XA) do { \
    if constexpr ((I) < 4) N_.template rdx<(I) & 3>(XA); \
    else if constexpr (SSM) { if constexpr ((I) < 8) N_.template rdw_lo<KK, ((I) - 4) & 3>(WA); } \
    else if constexpr ((I) < 4 + 2 * NT) { if constexpr (((I) & 1) == 0) N_.template rdw_lo<KK, (((I) - 4) >> 1) % NT>(WA); else N_.template rdw_hi<KK, (((I) - 4) >> 1) % NT>(WA); } \
  } while (0)
#define GRP(F_, N_, KK, C, R0, WA, XA, EXTRA) do { \
    MM1(F_, C, 0); RD(N_, KK, (R0), WA, XA); SB(); \
    MM1(F_, C, 1); RD(N_, KK, (R0) + 1, WA, XA); SB(); \
    MM1(F_, C, 2); RD(N_, KK, (R0) + 2, WA, XA); SB(); \
    MM1(F_, C, 3); RD(N_, KK, (R0) + 3, WA, XA); EXTRA; SB(); } while (0)
#define CSEL1(F_, C) do { const unsigned v_ = (io_ == (C)) ? 0x3F803F80u : 0u; const u32x4 s_ = {v_, v_, v_, v_}; csa = SSM ? mfma16(s_, F_.w(C), csa) : mfma16(F_.w(C), s_, csa); } while (0)
#define CSELALL(F_, M) do { if (M) { int io_ = i16; asm volatile("" : "+v"(io_)); CSEL1(F_, 0); CSEL1(F_, 1); CSEL1(F_, 2); CSEL1(F_, 3); if constexpr (NT > 4) { CSEL1(F_, 4 % NT); CSEL1(F_, 5 % NT); } } SB(); } while (0)
  int slot = 0;
  auto stage = [&](const int s, auto dx_t, auto dw_t, auto bnd_t) __attribute__((always_inline)) {
    constexpr bool DX = decltype(dx_t)::value, DW = decltype(dw_t)::value;
    constexpr int BND = decltype(bnd_t)::value;
    const int nslot = slot == 2 * SLOT ? 0 : slot + SLOT;
    int sox = 0, sow = 0, pows = 0, pslot = 0;
    if constexpr (DX) { sox = cx.xso_base + kchunk(s + 3) * 128; asm volatile("" : "+s"(sox)); }
    if constexpr (DW) { pows = cx.wso_base + kchunk(s + 2) * cx.wso_step; pslot = nslot == 2 * SLOT ? 0 : nslot + SLOT; asm volatile("" : "+s"(pows), "+s"(pslot)); }
    fa.wait(); SB();
    {
      const unsigned X1 = ax[1] + slot, W1 = aw[1] + slot;
      GRP(fa, fb, 1, 0, 0, W1, X1, if constexpr (DW) dma(pslot, 0, pows, 4));
      GRP(fa, fb, 1, 1, 4, W1, X1, if constexpr (DW) dma(pslot, 0, pows, 5));
      GRP(fa, fb, 1, 2, 8, W1, X1, if constexpr (!SSM && DW) dma(pslot, 0, pows, 6));
      GRP(fa, fb, 1, 3, 12, W1, X1, );
      if constexpr (NT > 4) {
        GRP(fa, fb, 1, 4 % NT, 16, W1, X1, );
        GRP(fa, fb, 1, 5 % NT, 20, W1, X1, );
      }
      CSELALL(fa, ((2 * s) & 3) == wr);
      if constexpr (BND == 0) hook9(std::integral_constant<int, MODE>{});
    }
    fb.wait(); SB();
    const unsigned X0 = ax[0] + nslot, W0 = aw[0] + nslot;
    MMG(fb, 0); SB();
    if constexpr (BND == 1) { if (SSM) WAIT_V(6); else WAIT_V(7); LBAR(); }
    if constexpr (BND == 2) { WAIT_V(0); LBAR(); }
    if constexpr (BND == 3) {
      if (w < 4) { if (SSM) WAIT_V(14); else WAIT_V(16); } else WAIT_V(0);
      LBAR();
    }
    GRP(fb, fa, 0, 1, 0, W0, X0, if constexpr (DX) dma(slot, sox, sow, 0));
    GRP(fb, fa, 0, 2, 4, W0, X0, if constexpr (DX) dma(slot, sox, sow, 1));
    GRP(fb, fa, 0, 3, 8, W0, X0, if constexpr (DX) dma(slot, sox, sow, 2));
    if constexpr (NT > 4) {
      GRP(fb, fa, 0, 4 % NT, 12, W0, X0, if constexpr (DX) dma(slot, sox, sow, 3));
      GRP(fb, fa, 0, 5 % NT, 16, W0, X0, );
    } else { if constexpr (DX) dma(slot, sox, sow, 3); }
    CSELALL(fb, ((2 * s + 1) & 3) == wr);
    slot = nslot;
  };
  using T_ = std::true_type; using F_ = std::false_type;
  using I0 = std::integral_constant<int, 0>; using I1 = std::integral_constant<int, 1>; using I2 = std::integral_constant<int, 2>;
  stage(0, T_{}, F_{}, std::integral_constant<int, 3>{});
  hook7();
#pragma unroll 1
  for (int s = 1; s < 6; ++s) stage(s, T_{}, T_{}, I1{});
  hookA();
  stage(6, T_{}, T_{}, I1{});
  hook8();
  stage(7, F_{}, T_{}, I1{});
  stage(8, F_{}, F_{}, I2{});
  stage(9, F_{}, F_{}, I0{});
  fa.wait();
}

constexpr int LWS = 0;
#define DWF_ 5
constexpr int DWF = DWF_;
constexpr int DW = 6;
struct P2Ctx { __amdgpu_buffer_rsrc_t rw; int sw, vo; };
DI void p2_ldw(const P2Ctx& c, u32x4 (&wv)[5], int k) {
#pragma unroll
  for (int j = 0; j < 5; ++j) wv[j] = __builtin_amdgcn_raw_buffer_load_b128(c.rw, c.vo, c.sw + k * 5120 + j * 1024, 16);
}
template <bool SSM>
DI unsigned phase1(const Params& p, char* smem, int mt, int cj  , int blk, int rot, P2Ctx& p2c, u32x4 (&wv)[DW][5]) {
  int tid = threadIdx.x, lane = tid & 63;
  const int w = __builtin_amdgcn_readfirstlane(tid >> 6);
  const int wr = w & 3, wc = w >> 2;
  int G = lane >> 4, i16 = lane & 15;
  auto relane = [&]() { int tz = threadIdx.x; asm volatile("" : "+v"(tz)); tid = tz; lane = tz & 63; G = lane >> 4; i16 = lane & 15; p2c.vo = lane * 16; };
  const unsigned lbase = (unsigned)(size_t)(lds_cp)smem;
  TS(0);
  const int cbi = SSM ? 22 + cj : cj;
  unsigned etag;
  if (w == 7) {
    const char* a;
    if (lane < 32) a = p.ws + WS_ACT + ((size_t)lane * NSTR + NSTEP) * 4096;
    else if (lane < 40) a = p.ws + WS_W2T + ((size_t)(lane - 32) * NSTR + NSTEP) * 5120;
    else if (lane < 50) a = (const char*)p.out + (size_t)(lane - 40) * (512u << 10);
    else a = (const char*)p.dsk;
    __builtin_amdgcn_global_load_lds((const unsigned*)a, (unsigned*)(smem + LDS_PAR + 2048), 4, 0, 0);
  }
  Ctx cx;
  cx.smem = smem; cx.rot = rot;
  cx.rx = __builtin_amdgcn_make_buffer_rsrc((void*)(p.x + (size_t)(256 * mt) * D), 0, 256 * D * 2, RSRC3);
#pragma unroll
  for (int i = 0; i < 4; ++i) {
    int f;
    if (SSM) f = ((lane >> 4) & 1) | ((((2 * w + (i >> 1)) & 3)) << 1);
    else f = ((i & 1) << 2) | (lane >> 4);
    cx.vx[i] = (lane >> 3) * 1280 + (((lane & 7) ^ f) << 4);
  }
  cx.xso_base = w * 32 * 1280; cx.xdst = w * 4096;
  auto wbase = [&](int v) { return SSM ? (64 * cj + 32 * (v >> 2) + 16 * ((v & 3) >> 1)) * 1280 : 16 * (v >> 1) * 3456; };
  auto wdst = [&](int v) { return SSM ? v * 2048 : (v >> 1) * 5120 + (v & 1) * 2560; };
  if (SSM) {
    cx.rw = __builtin_amdgcn_make_buffer_rsrc((void*)((w & 1) ? p.bim : p.bre), 0, P * D * 2, RSRC3);
    cx.vw[0] = (lane >> 3) * 1280 + (((lane & 7) ^ (lane >> 4)) << 4); cx.vw[1] = cx.vw[0] ^ 64; cx.vw[2] = 0;
    cx.wso_step = 128;
  } else {
    cx.rw = __builtin_amdgcn_make_buffer_rsrc((void*)((w & 1) ? p.w3 : p.w1), 0, D * F * 2, RSRC3);
#pragma unroll
    for (int i = 0; i < 3; ++i) {
      const int n = 64 * i + lane, pos = (n / 10) & 15, row = (pos & 3) | ((pos & 4) << 1) | ((pos & 8) >> 1);
      cx.vw[i] = row * 3456 + (n % 10) * 16 + cj * 160;
    }
    cx.wso_step = 64 * 3456;
  }
  cx.wso_base = wbase(w); cx.wdst = wdst(w);
  auto kchunk = [&](int s) { int k = s + rot; return k >= 10 ? k - 10 : k; };
  auto issue_for = [&](int v, int s, int slot) {
    const int sox = v * 32 * 1280 + kchunk(s) * 128, sow = wbase(v) + kchunk(s) * cx.wso_step;
    char* b = smem + slot;
#pragma unroll
    for (int i = 0; i < 4; ++i) dma16(cx.rx, b + v * 4096 + i * 1024, cx.vx[i], sox + i * 10240);
    dma16(cx.rw, b + XSLOT + wdst(v), cx.vw[0], sow);
    dma16(cx.rw, b + XSLOT + wdst(v) + 1024, cx.vw[1], SSM ? sow + 10240 : sow);
    if (!SSM) { if (lane < 32) dma16(cx.rw, b + XSLOT + wdst(v) + 2048, cx.vw[2], sow); }
  };
  const __amdgpu_buffer_rsrc_t rW2T = __builtin_amdgcn_make_buffer_rsrc((void*)(p.ws + WS_W2T), 0, (int)W2T_BYTES, RSRC3);
  auto stats_job = [&](const u32x4& r0, const u32x4& r1) {
    float v[16];
#pragma unroll
    for (int i = 0; i < 4; ++i) { v[2 * i] = __uint_as_float(r0[i] << 16); v[2 * i + 1] = __uint_as_float(r0[i] & 0xFFFF0000u); v[8 + 2 * i] = __uint_as_float(r1[i] << 16); v[9 + 2 * i] = __uint_as_float(r1[i] & 0xFFFF0000u); }
    float sum = 0.f;
#pragma unroll
    for (int i = 0; i < 16; ++i) sum += v[i];
    sum = wave_sum(sum);
    const float mean = sum * (1.0f / D);
    float sq = 0.f;
#pragma unroll
    for (int i = 0; i < 8; ++i) { const float d = v[i] - mean; sq = fmaf(d, d, sq); }
    float sq2 = 0.f;
#pragma unroll
    for (int i = 8; i < 16; ++i) { const float d = v[i] - mean; sq2 = fmaf(d, d, sq2); }
    sq += lane < 16 ? sq2 : 0.f;
    sq = wave_sum(sq);
    const float rstd = 1.0f / sqrtf(sq * (1.0f / D) + 1e-5f);
    if (lane == 0) {
      gu64* g = (gu64*)(p.ws + WS_STATS + (size_t)(256 * mt + 8 * cbi + w) * 16);
      __hip_atomic_store(g, ((unsigned long long)etag << 32) | __float_as_uint(mean), RLX_AGENT);
      __hip_atomic_store(g + 1, ((unsigned long long)etag << 32) | __float_as_uint(rstd), RLX_AGENT);
    }
  };
  const __amdgpu_buffer_rsrc_t rrow = __builtin_amdgcn_make_buffer_rsrc((void*)(p.x + (size_t)(256 * mt + 8 * cbi + w) * D), 0, D * 2, RSRC3);
  if (w < 4) {
    unsigned e_, pa_ = 0, pb_ = 0, pc_ = 0; u32x4 r0, r1;
    { const int vo = lane * 16;
      asm volatile("global_load_dword %2, %5, off sc1\n\tbuffer_load_dwordx4 %0, %3, %4, 0 offen\n\tbuffer_load_dwordx4 %1, %3, %4, 0 offen offset:1024"
                   : "=&v"(r0), "=&v"(r1), "=&v"(e_) : "v"(vo), "s"(rrow), "v"(p.ws + WS_EPOCH) : "memory"); }
    if constexpr (SSM) {
      const int ch = 64 * cj + lane;
      asm volatile("global_load_ushort %0, %3, off\n\tglobal_load_ushort %1, %4, off\n\tglobal_load_ushort %2, %5, off"
                   : "=&v"(pa_), "=&v"(pb_), "=&v"(pc_) : "v"(p.lre + ch), "v"(p.lim + ch), "v"(p.ldt + ch) : "memory");
    }
    issue_for(w, 0, 0); issue_for(w + 4, 0, 0);
    TS(6);
    issue_for(w, 1, SLOT); issue_for(w + 4, 1, SLOT);
    if constexpr (SSM) asm volatile("s_waitcnt vmcnt(24)" : "+v"(e_), "+v"(r0), "+v"(r1), "+v"(pa_), "+v"(pb_), "+v"(pc_) :: "memory"); else asm volatile("s_waitcnt vmcnt(28)" : "+v"(e_), "+v"(r0), "+v"(r1) :: "memory");
    etag = e_ + 1u;
    stats_job(r0, r1);
    if (SSM && w == 3) {
      const float a = bf2f((u16)pa_), b = bf2f((u16)pb_);
      const float dt = rbf(expf(bf2f((u16)pc_)));
      const float er = expf(dt * a);
      float sn, cn; sincosf(dt * b, &sn, &cn);
      const float lr = er * cn, li = er * sn;
      const float nr = lr - 1.0f, ni = li, den = a * a + b * b;
      const float cr = (nr * a + ni * b) / den, ci = (ni * a - nr * b) / den;
      float pr = lr, pi = li;
#pragma unroll
      for (int i = 0; i < 4; ++i) { float t = pr * pr - pi * pi; pi = 2.f * pr * pi; pr = t; }
      const float l16r = pr, l16i = pi;
#pragma unroll
      for (int i = 0; i < 4; ++i) { float t = pr * pr - pi * pi; pi = 2.f * pr * pi; pr = t; }
      float* par = (float*)(smem + LDS_PAR) + lane * 8;
      *(f32x4*)par = f32x4{lr, li, cr, ci};
      *(f32x4*)(par + 4) = f32x4{l16r, l16i, pr, pi};
    }
    if (SSM) WAIT_V(14); else WAIT_V(16);
    TS(13);
    WAIT_L0();
    BAR();
    issue_for(w, 2, 2 * SLOT); issue_for(w + 4, 2, 2 * SLOT);
  } else {
    const int hv = w - 4;
    unsigned e_; u32x4 r0, r1;
    { const int vo = lane * 16;
      asm volatile("global_load_dword %2, %5, off sc1\n\tbuffer_load_dwordx4 %0, %3, %4, 0 offen\n\tbuffer_load_dwordx4 %1, %3, %4, 0 offen offset:1024"
                   : "=&v"(r0), "=&v"(r1), "=&v"(e_) : "v"(vo), "s"(rrow), "v"(p.ws + WS_EPOCH) : "memory"); }
    const int unit = hv * 256 + blk;
    const bool has_unit = unit < 940, ffn_unit = unit < 540;
    char* rt = smem + LDS_RT + hv * 4096;
    int c0, s0u;
    if (ffn_unit) { s0u = unit / 10; c0 = 4 * (unit % 10); } else { const int u = unit - 540; c0 = u / 10; s0u = NSTEP_F + 4 * (u % 10); }
    if (has_unit) {
      if (ffn_unit) {
        const __amdgpu_buffer_rsrc_t r2 = __builtin_amdgcn_make_buffer_rsrc((void*)p.w2, 0, F * D * 2, RSRC3);
        const int vo = (lane >> 3) * 1280 + (lane & 7) * 16;
#pragma unroll
        for (int i = 0; i < 4; ++i) dma16(r2, rt + i * 1024, vo, (32 * s0u + 8 * i) * 1280 + (c0 >> 2) * 128);
      } else {
        const int vo = (lane & 15) * 1280 + (lane >> 4) * 16;
#pragma unroll
        for (int i = 0; i < 4; ++i) {
          const int sp = s0u - NSTEP_F + i, jg = sp >> 1, part = sp & 1;
          const __amdgpu_buffer_rsrc_t rc = __builtin_amdgcn_make_buffer_rsrc((void*)(part ? p.cim : p.cre), 0, D * P * 2, RSRC3);
          dma16(rc, rt + i * 1024, vo, 16 * c0 * 1280 + jg * 64);
        }
      }
    }
    asm volatile("s_waitcnt vmcnt(0)" : "+v"(e_), "+v"(r0), "+v"(r1) :: "memory");
    etag = e_ + 1u;
    u32x4 vv[4];
    {
      const unsigned rtl = lbase + LDS_RT + hv * 4096;
      if (ffn_unit) {
        const int g = lane >> 4, q = (lane >> 2) & 3, pp = lane & 3;
        const unsigned a0 = rtl + (8 * g + q) * 128 + 8 * pp;
        u32x2 l0 = trrd<0>(a0), h0 = trrd<512>(a0), l1 = trrd<32>(a0), h1 = trrd<512 + 32>(a0), l2 = trrd<64>(a0), h2 = trrd<512 + 64>(a0), l3 = trrd<96>(a0), h3 = trrd<512 + 96>(a0);
        asm volatile("s_waitcnt lgkmcnt(0)" : "+v"(l0), "+v"(h0), "+v"(l1), "+v"(h1), "+v"(l2), "+v"(h2), "+v"(l3), "+v"(h3) :: "memory");
        vv[0] = cat(l0, h0); vv[1] = cat(l1, h1); vv[2] = cat(l2, h2); vv[3] = cat(l3, h3);
      } else {
        u32x4 v0 = rd128<0>(rtl + lane * 16), v1 = rd128<1024>(rtl + lane * 16), v2 = rd128<2048>(rtl + lane * 16), v3 = rd128<3072>(rtl + lane * 16);
        asm volatile("s_waitcnt lgkmcnt(0)" : "+v"(v0), "+v"(v1), "+v"(v2), "+v"(v3) :: "memory");
        vv[0] = v0; vv[1] = v1; vv[2] = v2; vv[3] = v3;
      }
    }
    BAR();
    if (has_unit) {
#pragma unroll
      for (int i = 0; i < 4; ++i) {
        const int c = ffn_unit ? c0 + i : c0, s = ffn_unit ? s0u : s0u + i;
        st_sc1(vv[i], rW2T, lane * 16 + (((c / 5) * NSTR + s) * 5 + (c % 5)) * 1024);
      }
    }
    stats_job(r0, r1);
  }
  TS(1);
  auto hook0 = [&]() {};
  auto hook7 = [&]() { if (tid == 0) __hip_atomic_store((gu32*)(p.ws + WS_FLAGW) + blk, etag, RLX_AGENT); };
  const __amdgpu_buffer_rsrc_t rACT = __builtin_amdgcn_make_buffer_rsrc((void*)(p.ws + WS_ACT), 0, (int)ACT_BYTES, RSRC3);
  const __amdgpu_buffer_rsrc_t rFW = __builtin_amdgcn_make_buffer_rsrc((void*)(p.ws + WS_FLAGW), 0, 1024, RSRC3);
  const int mt64 = 4 * mt + wr;
  gu64* stg;
  unsigned long long sta = 0, stb = 0;
  u32x4 fw;
  auto hookA = [&]() { const int vo_ = lane * 16; asm volatile("buffer_load_dwordx4 %0, %1, %2, 0 offen sc1" : "=&v"(fw) : "v"(vo_), "s"(rFW) : "memory"); };
  auto hook8 = [&]() {
    if constexpr (SSM) asm volatile("s_waitcnt vmcnt(6)" : "+v"(fw) :: "memory"); else asm volatile("s_waitcnt vmcnt(7)" : "+v"(fw) :: "memory");
    if (w == 0) {
      unsigned spins = 0;
      while (!__all(fw.x == etag && fw.y == etag && fw.z == etag && fw.w == etag)) {
        if (++spins > SPIN_MAX) break;
        __builtin_amdgcn_s_sleep(1);
        fw = __builtin_amdgcn_raw_buffer_load_b128(rFW, lane * 16, 0, 16);
      }
      if (blk == 0 && lane == 0) __hip_atomic_store((gu32*)(p.ws + WS_EPOCH), etag, RLX_AGENT);
    }
  };
  auto hook9 = [&](auto mode_tag) {
    int tz = threadIdx.x; asm volatile("" : "+v"(tz));
    stg = (gu64*)(p.ws + WS_STATS + (size_t)(256 * mt + 64 * wr + (tz & 63)) * 16);
    sta = __hip_atomic_load(stg, RLX_AGENT); stb = __hip_atomic_load(stg + 1, RLX_AGENT);
  };
  f32x4 csa = {0.f, 0.f, 0.f, 0.f};
  float* cst = (float*)(smem + LDS_CS);
  float* stt = (float*)(smem + LDS_ST);
  auto finish_common = [&](int ntw  , int tile0  ) {
    unsigned spins = 0;
    while (!__all((unsigned)(sta >> 32) == etag && (unsigned)(stb >> 32) == etag)) {
      if (++spins > SPIN_MAX) break;
      __builtin_amdgcn_s_sleep(1);
      sta = __hip_atomic_load(stg, RLX_AGENT); stb = __hip_atomic_load(stg + 1, RLX_AGENT);
    }
    const float mean = __uint_as_float((unsigned)sta), rstd = __uint_as_float((unsigned)stb);
    if (wc == 0) *(f32x2*)(stt + (64 * wr + lane) * 2) = f32x2{rstd, -mean * rstd};
    if (SSM) { if (G == 0) {
#pragma unroll
        for (int c = 0; c < 4; ++c) cst[wr * 160 + (tile0 + c) * 16 + i16] = csa[c]; } }
    else { if (i16 < ntw) *(f32x4*)(cst + wr * 160 + (tile0 + i16) * 16 + 4 * G) = csa; }
    WAIT_L0();
    BAR();
  };
  auto cs4 = [&](int idx) { return (*(const f32x4*)(cst + idx) + *(const f32x4*)(cst + 160 + idx)) + (*(const f32x4*)(cst + 320 + idx) + *(const f32x4*)(cst + 480 + idx)); };
  auto cs1 = [&](int idx) { return (cst[idx] + cst[160 + idx]) + (cst[320 + idx] + cst[480 + idx]); };

  if (!SSM) {
    auto ffn = [&](auto mode_tag) {
      constexpr int MODE = decltype(mode_tag)::value;
      constexpr int NT = Frags<MODE>::NT, P0 = MODE == 0 ? 0 : 3;
      f32x4 acc[4][NT];
#pragma unroll
      for (int T = 0; T < 4; ++T)
#pragma unroll
        for (int c = 0; c < NT; ++c) acc[T][c] = f32x4{0.f, 0.f, 0.f, 0.f};
      main_loop<MODE>(p, cx, lbase, w, lane, acc, csa, hook0, hook7, hookA, hook8, hook9);
      TS(2);
      relane();
      finish_common(NT, 2 * P0);
      constexpr int NB = 4 * (NT / 2);
      auto ldw_part = [&](int b) {
#pragma unroll
        for (int j = 0; j < 20; ++j)
          if (j * NB / 20 == b) wv[j / 5][j % 5] = __builtin_amdgcn_raw_buffer_load_b128(p2c.rw, p2c.vo, p2c.sw + (j / 5 == 3 ? 3 : 6 + j / 5) * 5120 + (j % 5) * 1024, 16);
      };
      f32x2 st4[4];
#pragma unroll
      for (int T = 0; T < 4; ++T) st4[T] = *(const f32x2*)(stt + (64 * wr + 16 * T + i16) * 2);
#pragma unroll
      for (int Pq = 0; Pq < NT / 2; ++Pq) {
        const int ft = 5 * cj + P0 + Pq;
        const f32x4 cg = cs4((2 * P0 + 2 * Pq) * 16 + 4 * G), cu = cs4((2 * P0 + 2 * Pq + 1) * 16 + 4 * G);
        unsigned pk[4][2];
#pragma unroll
        for (int T = 0; T < 4; ++T) {
          float hv[4];
#pragma unroll
          for (int e = 0; e < 4; ++e) {
            const float g = fmaf(acc[T][2 * Pq][e], st4[T].x, st4[T].y * cg[e]);
            const float u = fmaf(acc[T][2 * Pq + 1][e], st4[T].x, st4[T].y * cu[e]);
            hv[e] = silu_f(g) * u;
          }
          pk[T][0] = pack2(hv[0], hv[1]); pk[T][1] = pack2(hv[2], hv[3]);
          ldw_part(4 * Pq + T);
          SB();
        }
        if (ft < 108) {
#pragma unroll
          for (int T2 = 0; T2 < 4; T2 += 2) {
            const auto a = __builtin_amdgcn_permlane16_swap(pk[T2][0], pk[T2 + 1][0], false, false);
            const auto b = __builtin_amdgcn_permlane16_swap(pk[T2][1], pk[T2 + 1][1], false, false);
            const u32x4 v = {a[0], b[0], a[1], b[1]};
            const int s2 = ft >> 1, gp = 2 * (ft & 1) + (G >> 1), T = T2 + (G & 1);
            st_sc1(v, rACT, T * 1024 + (gp * 16 + i16) * 16 + (mt64 * NSTR + s2) * 4096);
          }
        }
      }
    };
    if (wc == 0) ffn(std::integral_constant<int, 0>{}); else ffn(std::integral_constant<int, 1>{});
  } else {
    f32x4 acc[4][4];
#pragma unroll
    for (int T = 0; T < 4; ++T)
#pragma unroll
      for (int c = 0; c < 4; ++c) acc[T][c] = f32x4{0.f, 0.f, 0.f, 0.f};
    main_loop<2>(p, cx, lbase, w, lane, acc, csa, hook0, hook7, hookA, hook8, hook9);
    TS(2);
    relane();
    const float* par = (const float*)(smem + LDS_PAR);
    float lr[2], li[2], cr[2], ci[2];
#pragma unroll
    for (int j = 0; j < 2; ++j) {
      const f32x4 pv = *(const f32x4*)(par + (32 * wc + 16 * j + i16) * 8);
      lr[j] = pv.x; li[j] = pv.y; cr[j] = pv.z; ci[j] = pv.w;
    }
    f32x4 pv = *(const f32x4*)(par + lane * 8 + 4);
    asm volatile("" : "+v"(pv), "+v"(lr[0]), "+v"(li[0]), "+v"(cr[0]), "+v"(ci[0]), "+v"(lr[1]), "+v"(li[1]), "+v"(cr[1]), "+v"(ci[1]));
    finish_common(4, 4 * wc);
    {
      float csl[4];
#pragma unroll
      for (int c = 0; c < 4; ++c) csl[c] = cs1((4 * wc + c) * 16 + i16);
#pragma unroll
      for (int T = 0; T < 4; ++T)
#pragma unroll
        for (int e = 0; e < 4; e += 2) {
          const f32x4 sv = *(const f32x4*)(stt + (64 * wr + 16 * G + 4 * T + e) * 2);
#pragma unroll
          for (int c = 0; c < 4; ++c) {
            acc[T][c][e] = fmaf(acc[T][c][e], sv.x, sv.y * csl[c]);
            acc[T][c][e + 1] = fmaf(acc[T][c][e + 1], sv.z, sv.w * csl[c]);
          }
        }
    }
    f32x2* et = (f32x2*)(smem + LDS_ET);
    const int seg = 4 * wr + G;
#pragma unroll
    for (int j = 0; j < 2; ++j) {
      float hr = 0.f, hi = 0.f;
#pragma unroll
      for (int i = 0; i < 16; ++i) {
        const float nr = fmaf(lr[j], hr, fmaf(-li[j], hi, acc[i >> 2][2 * j][i & 3]));
        const float ni = fmaf(lr[j], hi, fmaf(li[j], hr, acc[i >> 2][2 * j + 1][i & 3]));
        hr = nr; hi = ni;
      }
      et[seg * 64 + 32 * wc + 16 * j + i16] = f32x2{hr, hi};
    }
    WAIT_L0(); BAR();
    TS(3);
    if (w == 0) {
      const unsigned tag = etag;
      float xr_ = 0.f, xi_ = 0.f;
      f32x2 ev[16];
#pragma unroll
      for (int s = 0; s < 16; ++s) ev[s] = et[s * 64 + lane];
#pragma unroll
      for (int s = 0; s < 16; ++s) {
        const float nr = fmaf(pv.x, xr_, fmaf(-pv.y, xi_, ev[s].x));
        const float ni = fmaf(pv.x, xi_, fmaf(pv.y, xr_, ev[s].y));
        xr_ = nr; xi_ = ni;
      }
      gu64* gran = (gu64*)(p.ws + WS_GRAN);
      if (mt < 7) {
        gu64* g = gran + ((size_t)(mt * 10 + cj) * 64 + lane) * 2;
        __hip_atomic_store(g, ((unsigned long long)tag << 32) | __float_as_uint(xr_), RLX_AGENT);
        __hip_atomic_store(g + 1, ((unsigned long long)tag << 32) | __float_as_uint(xi_), RLX_AGENT);
      }
      float Hr = 0.f, Hi = 0.f;
      if (mt > 0) {
        unsigned long long ga[7], gb[7];
        unsigned spins = 0;
        while (true) {
          bool ok = true;
#pragma unroll
          for (int m = 0; m < 7; ++m) {
            if (m < mt) {
              gu64* g = gran + ((size_t)(m * 10 + cj) * 64 + lane) * 2;
              ga[m] = __hip_atomic_load(g, RLX_AGENT); gb[m] = __hip_atomic_load(g + 1, RLX_AGENT);
            } else { ga[m] = gb[m] = (unsigned long long)tag << 32; }
          }
#pragma unroll
          for (int m = 0; m < 7; ++m) ok = ok && ((unsigned)(ga[m] >> 32) == tag) && ((unsigned)(gb[m] >> 32) == tag);
          if (__all(ok)) break;
          if (++spins > SPIN_MAX) break;
          __builtin_amdgcn_s_sleep(1);
        }
#pragma unroll
        for (int m = 0; m < 7; ++m) {
          if (m < mt) {
            const float sr = __uint_as_float((unsigned)ga[m]), si = __uint_as_float((unsigned)gb[m]);
            const float nr = fmaf(pv.z, Hr, fmaf(-pv.w, Hi, sr));
            const float ni = fmaf(pv.z, Hi, fmaf(pv.w, Hr, si));
            Hr = nr; Hi = ni;
          }
        }
      }
      xr_ = Hr; xi_ = Hi;
#pragma unroll
      for (int s = 0; s < 16; ++s) {
        et[s * 64 + lane] = f32x2{xr_, xi_};
        const float nr = fmaf(pv.x, xr_, fmaf(-pv.y, xi_, ev[s].x));
        const float ni = fmaf(pv.x, xi_, fmaf(pv.y, xr_, ev[s].y));
        xr_ = nr; xi_ = ni;
      }
    }
    WAIT_L0(); BAR();
    TS(4);
    p2_ldw(p2c, wv[0], 6); p2_ldw(p2c, wv[1], 7);
    SB();
    f32x2 x0[2];
#pragma unroll
    for (int j = 0; j < 2; ++j) x0[j] = et[seg * 64 + 32 * wc + 16 * j + i16];
    unsigned pre[2][8], pim[2][8];
#pragma unroll
    for (int j = 0; j < 2; ++j) {
      float hr = x0[j].x, hi = x0[j].y;
      float ore[16], oim[16];
#pragma unroll
      for (int i = 0; i < 16; ++i) {
        const float nr = fmaf(lr[j], hr, fmaf(-li[j], hi, acc[i >> 2][2 * j][i & 3]));
        const float ni = fmaf(lr[j], hi, fmaf(li[j], hr, acc[i >> 2][2 * j + 1][i & 3]));
        hr = nr; hi = ni;
        ore[i] = cr[j] * hr - ci[j] * hi;
        oim[i] = -(cr[j] * hi + ci[j] * hr);
      }
#pragma unroll
      for (int i = 0; i < 8; ++i) { pre[j][i] = pack2(ore[2 * i], ore[2 * i + 1]); pim[j][i] = pack2(oim[2 * i], oim[2 * i + 1]); }
    }
    {
      char* tp = smem + LDS_TP + w * 4608;
      const int q = (lane >> 2) & 3, pp = lane & 3;
      const unsigned tb = lbase + LDS_TP + w * 4608 + (8 * G + q) * 144 + 8 * pp;
      const int jg = 2 * cj + wc;
#pragma unroll
      for (int part = 0; part < 2; ++part) {
#pragma unroll
        for (int j = 0; j < 2; ++j) {
          char* t0 = tp + (16 * j + i16) * 144 + 32 * G;
          const unsigned* src = part ? pim[j] : pre[j];
          *(u32x4*)t0 = u32x4{src[0], src[1], src[2], src[3]};
          *(u32x4*)(t0 + 16) = u32x4{src[4], src[5], src[6], src[7]};
        }
        WAIT_L0();
        u32x2 lo[4], hi[4];
        lo[0] = trrd<0>(tb); hi[0] = trrd<576>(tb); lo[1] = trrd<32>(tb); hi[1] = trrd<576 + 32>(tb); lo[2] = trrd<64>(tb); hi[2] = trrd<576 + 64>(tb); lo[3] = trrd<96>(tb); hi[3] = trrd<576 + 96>(tb);
        asm volatile("s_waitcnt lgkmcnt(0)" : "+v"(lo[0]), "+v"(hi[0]), "+v"(lo[1]), "+v"(hi[1]), "+v"(lo[2]), "+v"(hi[2]), "+v"(lo[3]), "+v"(hi[3]) :: "memory");
        const int s2 = NSTEP_F + 2 * jg + part;
#pragma unroll
        for (int T = 0; T < 4; ++T)
          st_sc1(cat(lo[T], hi[T]), rACT, T * 1024 + lane * 16 + (mt64 * NSTR + s2) * 4096);
      }
    }
  }
  TS(5);
  WAIT_V(0);
  BAR();
  if (tid == 0) __hip_atomic_store((gu32*)(p.ws + WS_FLAGA + mt * 128) + cbi, etag, RLX_AGENT);
  asm volatile("" ::: "memory"); SB();
  if (SSM) { p2_ldw(p2c, wv[2], 8); p2_ldw(p2c, wv[3], 3); }
  p2_ldw(p2c, wv[4], 4); p2_ldw(p2c, wv[5], 5);
  SB();
  {
    char* lw = smem + LDS_W2 + w * 15360 + lane * 16;
#pragma unroll
    for (int k = 0; k < 3; ++k)
#pragma unroll
      for (int c = 0; c < 5; ++c) *(u32x4*)(lw + k * 5120 + c * 1024) = wv[k][c];
  }
  SB();
  p2_ldw(p2c, wv[0], 0); p2_ldw(p2c, wv[1], 1); p2_ldw(p2c, wv[2], 2);
  SB();
  return etag;
}

DI void p2_run(const Params& p, const char* lw  , __amdgpu_buffer_rsrc_t ra, const P2Ctx& p2c, u32x4 (&wv)[DW][5],
               int sa, int N, int w, int lane, int mt2, int shA, int shW, unsigned etag, f32x4 (&acc)[4][5]) {
  constexpr int DA = 2, NMIN = 10, NMAX = 14;
  u32x4 a[DA][4];
  const int vo = p2c.vo;
  auto lda = [&](int k, int slot) {
#pragma unroll
    for (int T = 0; T < 4; ++T) a[slot][T] = __builtin_amdgcn_raw_buffer_load_b128(ra, vo, sa + k * 4096 + T * 1024, 16);
  };
  {
    const int fi = w < 4 ? (lane < 22 ? lane : 0) : 22 + (lane < 10 ? lane : 0);
    gu32* fl = (gu32*)(p.ws + WS_FLAGA + mt2 * 128) + fi;
    unsigned spins = 0;
    while (!__all(__hip_atomic_load(fl, RLX_AGENT) == etag)) {
      if (++spins > SPIN_MAX) break;
      __builtin_amdgcn_s_sleep(1);
    }
  }
  __builtin_amdgcn_fence(__ATOMIC_ACQUIRE, "wavefront");
  TS(9);
#pragma unroll
  for (int s = 0; s < DA - 1; ++s) lda(s, s);
#pragma unroll
  for (int T = 0; T < 4; ++T)
#pragma unroll
    for (int c = 0; c < 5; ++c) asm volatile("" : "+v"(acc[T][c]));
  asm volatile("s_nop 7" ::: "memory");
  SB();
#pragma unroll
  for (int s = 0; s < NMAX; ++s) {
    if (s < NMIN || s < N) {
      if (s + DA - 1 < NMIN || s + DA - 1 < N) lda(s + DA - 1, (s + DA - 1) % DA);
      SB();
#pragma unroll
      for (int T = 0; T < 4; ++T)
#pragma unroll
        for (int c = 0; c < 5; ++c) mfma16_acc(acc[T][c], wv[s % DW][c], a[s % DA][T]);
      SB();
      if (s < 3) {
#pragma unroll
        for (int c = 0; c < 5; ++c) wv[s][c] = *(const u32x4*)(lw + s * 5120 + c * 1024);
      } else if (s + DW < NMAX) { if (s + DW < NMIN || s + DW < N) p2_ldw(p2c, wv[s % DW], s + DW); }
      SB();
    }
  }
  asm volatile("s_nop 15\n\ts_nop 7" ::: "memory");
}

DI void phase2(const Params& p, char* smem, unsigned etag, const P2Ctx& p2c, u32x4 (&wv)[DW][5], int mt64, int nt, int s0) {
  const int tid = threadIdx.x, lane = tid & 63, w = __builtin_amdgcn_readfirstlane(tid >> 6);
  TS(8);
  f32x4 acc[4][5];
#pragma unroll
  for (int T = 0; T < 4; ++T)
#pragma unroll
    for (int c = 0; c < 5; ++c) acc[T][c] = f32x4{0.f, 0.f, 0.f, 0.f};
  const __amdgpu_buffer_rsrc_t ra = __builtin_amdgcn_make_buffer_rsrc((void*)(p.ws + WS_ACT), 0, (int)ACT_BYTES, RSRC3);
  const int sa = (mt64 * NSTR + s0) * 4096;
  const int shA = nt & 3, shW = mt64 & 7;
  const int N = w < 2 ? 14 : (w < 4 ? 13 : 10);
  const char* lw = smem + LDS_W2 + w * 15360 + lane * 16;
  p2_run(p, lw, ra, p2c, wv, sa, N, w, lane, mt64 >> 2, shA, shW, etag, acc);
  auto part_addr = [&](int wv_, int t) { return t < 15 ? LDS_W2 + wv_ * (15360) + t * 1024 : wv_ * 5120 + (t - 15) * 1024; };
  {
    const int g = lane >> 4, r = lane & 15;
    const int pos = g * 16 + ((r + 4 * g) & 15);
#pragma unroll
    for (int T = 0; T < 4; ++T)
#pragma unroll
      for (int c = 0; c < 5; ++c) *(f32x4*)(smem + part_addr(w, T * 5 + c) + pos * 16) = acc[T][c];
  }
  f32x4 res[3]; u32x2 xv[3], dv[3]; unsigned long long ga[3], gb[3];
#pragma unroll
  for (int it = 0; it < 3; ++it) {
    const int item = tid + 512 * it;
    if (item < 1280) {
      const int tok = item / 20, dg = item % 20;
      const int gt = mt64 * 64 + tok, d0 = nt * 80 + dg * 4;
      gu64* sg = (gu64*)(p.ws + WS_STATS + (size_t)gt * 16);
      ga[it] = __hip_atomic_load(sg, RLX_AGENT); gb[it] = __hip_atomic_load(sg + 1, RLX_AGENT);
      xv[it] = *(const u32x2*)(p.x + (size_t)gt * D + d0);
      dv[it] = *(const u32x2*)(p.dsk + d0);
    }
  }
  __syncthreads();
  TS(10);
#pragma unroll
  for (int it = 0; it < 3; ++it) {
    const int item = tid + 512 * it;
    if (item < 1280) {
      const int tok = item / 20, dg = item % 20, c = dg >> 2, g = dg & 3, T = tok >> 4, rr = tok & 15;
      const int t = T * 5 + c, off = (g * 16 + ((rr + 4 * g) & 15)) * 16;
      const int gt = mt64 * 64 + tok, d0 = nt * 80 + dg * 4;
      const int ta = t < 15 ? LDS_W2 + t * 1024 : (t - 15) * 1024, st = t < 15 ? 15360 : 5120;
      const char* base = smem + ta + off;
      f32x4 o = *(const f32x4*)(base);
#pragma unroll
      for (int k = 1; k < 4; ++k) o += *(const f32x4*)(base + k * st);
      f32x4 y = *(const f32x4*)(base + 4 * st);
#pragma unroll
      for (int k = 5; k < 8; ++k) y += *(const f32x4*)(base + k * st);
      const float mean = __uint_as_float((unsigned)ga[it]), rstd = __uint_as_float((unsigned)gb[it]);
      const float xs[4] = {__uint_as_float(xv[it].x << 16), __uint_as_float(xv[it].x & 0xFFFF0000u), __uint_as_float(xv[it].y << 16), __uint_as_float(xv[it].y & 0xFFFF0000u)};
      const float ds[4] = {__uint_as_float(dv[it].x << 16), __uint_as_float(dv[it].x & 0xFFFF0000u), __uint_as_float(dv[it].y << 16), __uint_as_float(dv[it].y & 0xFFFF0000u)};
#pragma unroll
      for (int e = 0; e < 4; ++e) {
        const float xn = (xs[e] - mean) * rstd;
        const float yy = fmaf(2.0f, y[e], xn * ds[e]);
        res[it][e] = fmaf(yy, silu_f(o[e]), xs[e]);
      }
      *(f32x4*)(p.out + (size_t)gt * D + d0) = res[it];
    }
  }
  TS(11);
}

__global__ __launch_bounds__(512, 2) void k_fused(Params p) {
  extern __shared__ __attribute__((aligned(16))) char smem[];
  const int b = blockIdx.x, xcd = b & 7, i = b >> 3;
  const int ml = i & 3, cl = i >> 2;
  const int mt = (xcd & 1) * 4 + ml, cb = (xcd >> 1) * 8 + cl;
  const int rot = 0;
  const int w = __builtin_amdgcn_readfirstlane(threadIdx.x >> 6), lane = threadIdx.x & 63;
  const int mt64 = (xcd >> 1) * 8 + (i & 7), nt = (xcd & 1) * 4 + (i >> 3);
  const int s0 = w < 4 ? (w < 2 ? 14 * w : 28 + 13 * (w - 2)) : 54 + 10 * (w - 4);
  P2Ctx p2c;
  p2c.rw = __builtin_amdgcn_make_buffer_rsrc((void*)(p.ws + WS_W2T), 0, (int)W2T_BYTES, RSRC3);
  p2c.sw = (nt * NSTR + s0) * 5120; p2c.vo = 0;
  u32x4 wv[DW][5];
  unsigned etag;
  if (cb < 22) etag = phase1<false>(p, smem, mt, cb, b, rot, p2c, wv);
  else etag = phase1<true>(p, smem, mt, cb - 22, b, rot, p2c, wv);
  phase2(p, smem, etag, p2c, wv, mt64, nt, s0);
}
}

extern "C" void kernel_launch(void* const* d_in, const int* in_sizes, int n_in, void* d_out, int out_size, void* d_ws, size_t ws_size, hipStream_t stream) {
  using namespace s5;
  Params p;
  p.x = (const u16*)d_in[0]; p.lre = (const u16*)d_in[1]; p.lim = (const u16*)d_in[2];
  p.bre = (const u16*)d_in[3]; p.bim = (const u16*)d_in[4]; p.cre = (const u16*)d_in[5]; p.cim = (const u16*)d_in[6];
  p.ldt = (const u16*)d_in[7]; p.dsk = (const u16*)d_in[8];
  p.w1 = (const u16*)d_in[10]; p.w3 = (const u16*)d_in[11]; p.w2 = (const u16*)d_in[12];
  p.out = (float*)d_out; p.ws = (char*)d_ws;
  (void)hipFuncSetAttribute((const void*)k_fused, hipFuncAttributeMaxDynamicSharedMemorySize, LDS1_BYTES);
  k_fused<<<256, 512, LDS1_BYTES, stream>>>(p);
}
```

```cpp
#include <hip/hip_runtime.h>
#include <stdint.h>
#include <type_traits>

namespace s5 {
typedef unsigned short u16;
typedef short bf16x8 __attribute__((ext_vector_type(8)));
typedef float f32x4 __attribute__((ext_vector_type(4)));
typedef float f32x2 __attribute__((ext_vector_type(2)));
typedef unsigned u32x4 __attribute__((ext_vector_type(4)));
typedef unsigned u32x2 __attribute__((ext_vector_type(2)));
typedef __bf16 bf16x2 __attribute__((ext_vector_type(2)));
typedef __attribute__((address_space(3))) void* lds_vp;
typedef __attribute__((address_space(3))) char* lds_cp;
typedef __attribute__((address_space(1))) unsigned long long gu64;
typedef __attribute__((address_space(1))) unsigned gu32;
#define DI __device__ __forceinline__
#define RLX_AGENT __ATOMIC_RELAXED, __HIP_MEMORY_SCOPE_AGENT

constexpr int L = 2048, D = 640, P = 640, F = 1728;
constexpr int NSTEP = 94, NSTEP_F = 54;
constexpr size_t WS_EPOCH = 0;
constexpr size_t WS_FLAGW = 1024;
constexpr size_t WS_FLAGA = 4096;
constexpr size_t WS_GRAN = 8192;
constexpr size_t WS_STATS = WS_GRAN + 7 * 10 * 1024 + 2048;
constexpr size_t WS_ACT = 262144;
constexpr int NSTR = NSTEP + 1;
constexpr size_t ACT_BYTES = (size_t)32 * NSTR * 4 * 1024;
constexpr size_t WS_W2T = WS_ACT + ACT_BYTES;
constexpr size_t W2T_BYTES = (size_t)8 * NSTR * 5 * 1024;
constexpr size_t WS_STAMP = 32u << 20;
static_assert(WS_STATS + 2048 * 16 <= WS_ACT, "ws map");
constexpr int XSLOT = 32768, WSLOT = 20480, SLOT = XSLOT + WSLOT;
constexpr int LDS_PAR = 3 * SLOT;
constexpr int LDS1_BYTES = 163840;
constexpr int LDS_CS = SLOT;
constexpr int LDS_ST = SLOT + 4096;
constexpr int LDS_ET = SLOT + 8192;
constexpr int LDS_TP = SLOT + 16384;
constexpr int LDS_W2 = 40960;
constexpr int LDS_RT = 2 * SLOT;
static_assert(LDS1_BYTES <= 163840, "lds");
constexpr int RSRC3 = 0x00020000;
constexpr unsigned SPIN_MAX = 1u << 21;

struct Params {
  const u16 *x, *lre, *lim, *bre, *bim, *cre, *cim, *ldt, *dsk, *w1, *w3, *w2;
  float* out;
  char* ws;
};

DI float bf2f(u16 v) { return __uint_as_float(((unsigned)v) << 16); }
DI float rbf(float f) { unsigned u = __float_as_uint(f); u += 0x7FFFu + ((u >> 16) & 1u); return __uint_as_float(u & 0xFFFF0000u); }
DI unsigned pack2(float a, float b) { f32x2 p = {a, b}; return __builtin_bit_cast(unsigned, __builtin_convertvector(p, bf16x2)); }
DI f32x4 mfma16(u32x4 a, u32x4 b, f32x4 c) { return __builtin_amdgcn_mfma_f32_16x16x32_bf16(__builtin_bit_cast(bf16x8, a), __builtin_bit_cast(bf16x8, b), c, 0, 0, 0); }
DI void mfma16_acc(f32x4& c, u32x4 a, u32x4 b) { asm volatile("v_mfma_f32_16x16x32_bf16 %0, %1, %2, %0" : "+v"(c) : "v"(a), "v"(b)); }
template <int OFF> DI u32x2 trrd(unsigned addr) { u32x2 r; asm volatile("ds_read_b64_tr_b16 %0, %1 offset:%2" : "=v"(r) : "v"(addr), "i"(OFF) : "memory"); return r; }
template <int OFF> DI u32x4 rd128(unsigned addr) { u32x4 r; asm volatile("ds_read_b128 %0, %1 offset:%2" : "=v"(r) : "v"(addr), "i"(OFF) : "memory"); return r; }
DI u32x4 cat(u32x2 a, u32x2 b) { return u32x4{a.x, a.y, b.x, b.y}; }
DI void dma16(__amdgpu_buffer_rsrc_t rs, char* lds, int voff, int soff) { __builtin_amdgcn_raw_ptr_buffer_load_lds(rs, (lds_vp)lds, 16, voff, soff, 0, 0); }
DI void st_sc1(u32x4 v, __amdgpu_buffer_rsrc_t rs, int off) { __builtin_amdgcn_raw_buffer_store_b128(v, rs, off, 0, 16); }
#define SB() __builtin_amdgcn_sched_barrier(0)
#define WAIT_V(n) asm volatile("s_waitcnt vmcnt(" #n ")" ::: "memory")
#define WAIT_L0() asm volatile("s_waitcnt lgkmcnt(0)" ::: "memory")
#define BAR() __builtin_amdgcn_s_barrier()
#define LBAR() __builtin_amdgcn_s_barrier()
#define TS(i) do {} while (0)
DI float wave_sum(float v) {
  v += __builtin_bit_cast(float, __builtin_amdgcn_update_dpp(0, __builtin_bit_cast(int, v), 0x111, 0xf, 0xf, true));
  v += __builtin_bit_cast(float, __builtin_amdgcn_update_dpp(0, __builtin_bit_cast(int, v), 0x112, 0xf, 0xf, true));
  v += __builtin_bit_cast(float, __builtin_amdgcn_update_dpp(0, __builtin_bit_cast(int, v), 0x114, 0xf, 0xf, true));
  v += __builtin_bit_cast(float, __builtin_amdgcn_update_dpp(0, __builtin_bit_cast(int, v), 0x118, 0xf, 0xf, true));
  const int iv = __builtin_bit_cast(int, v);
  const float a = __builtin_bit_cast(float, __builtin_amdgcn_readlane(iv, 15)), b = __builtin_bit_cast(float, __builtin_amdgcn_readlane(iv, 31));
  const float c = __builtin_bit_cast(float, __builtin_amdgcn_readlane(iv, 47)), d = __builtin_bit_cast(float, __builtin_amdgcn_readlane(iv, 63));
  return (a + b) + (c + d);
}
DI float silu_f(float g) { return g * __builtin_amdgcn_rcpf(1.0f + __builtin_amdgcn_exp2f(g * -1.44269504088896f)); }

template <int MODE> struct Frags {
  static constexpr int NT = MODE == 0 ? 6 : 4;
  u32x4 x[4];
  u32x2 lo[MODE == 2 ? 1 : NT], hi[MODE == 2 ? 1 : NT];
  u32x4 v[MODE == 2 ? NT : 1];
  template <int KK, int C> DI void rdw(unsigned aw) {
    if constexpr (MODE == 2) v[C] = rd128<C * 2048>(aw);
    else { constexpr int O = KK * 10240 + (C & 1) * 2560 + ((MODE == 0 ? 0 : 3) + (C >> 1)) * 32; lo[C] = trrd<O>(aw); hi[C] = trrd<O + 1280>(aw); }
  }
  template <int KK, int C> DI void rdw_lo(unsigned aw) {
    if constexpr (MODE == 2) v[C] = rd128<C * 2048>(aw);
    else { constexpr int O = KK * 10240 + (C & 1) * 2560 + ((MODE == 0 ? 0 : 3) + (C >> 1)) * 32; lo[C] = trrd<O>(aw); }
  }
  template <int KK, int C> DI void rdw_hi(unsigned aw) {
    if constexpr (MODE != 2) { constexpr int O = KK * 10240 + (C & 1) * 2560 + ((MODE == 0 ? 0 : 3) + (C >> 1)) * 32; hi[C] = trrd<O + 1280>(aw); }
  }
  template <int T> DI void rdx(unsigned ax) { x[T] = rd128<(MODE == 2 ? 512 : 2048) * T>(ax); }
  DI u32x4 w(int c) const { if constexpr (MODE == 2) return v[c]; else return cat(lo[c], hi[c]); }
  DI void wait() {
    if constexpr (MODE == 0) asm volatile("s_waitcnt lgkmcnt(0)" : "+v"(x[0]), "+v"(x[1]), "+v"(x[2]), "+v"(x[3]), "+v"(lo[0]), "+v"(hi[0]), "+v"(lo[1]), "+v"(hi[1]), "+v"(lo[2]), "+v"(hi[2]),
                                          "+v"(lo[3 % NT]), "+v"(hi[3 % NT]), "+v"(lo[4 % NT]), "+v"(hi[4 % NT]), "+v"(lo[5 % NT]), "+v"(hi[5 % NT]) :: "memory");
    else if constexpr (MODE == 1) asm volatile("s_waitcnt lgkmcnt(0)" : "+v"(x[0]), "+v"(x[1]), "+v"(x[2]), "+v"(x[3]), "+v"(lo[0]), "+v"(hi[0]), "+v"(lo[1]), "+v"(hi[1]), "+v"(lo[2]), "+v"(hi[2]), "+v"(lo[3]), "+v"(hi[3]) :: "memory");
    else asm volatile("s_waitcnt lgkmcnt(0)" : "+v"(x[0]), "+v"(x[1]), "+v"(x[2]), "+v"(x[3]), "+v"(v[0]), "+v"(v[1 % NT]), "+v"(v[2 % NT]), "+v"(v[3 % NT]) :: "memory");
  }
};

struct Ctx {
  __amdgpu_buffer_rsrc_t rx, rw;
  int vx[4];
  int vw[3];
  int xso_base, wso_base, wso_step;
  int rot;
  char* smem;
  int xdst, wdst;
};

template <int MODE, class H0, class H7, class HA, class H8, class H9>
DI void main_loop(const Params& p, const Ctx& cx, unsigned lbase, int w, int lane, f32x4 (&acc)[4][Frags<MODE>::NT], f32x4& csa, H0 hook0, H7 hook7, HA hookA, H8 hook8, H9 hook9) {
  constexpr bool SSM = MODE == 2;
  constexpr int NT = Frags<MODE>::NT;
  const int wr = w & 3, wc = w >> 2;
  const int G = lane >> 4, i16 = lane & 15;
  unsigned ax[2], aw[2];
  if (SSM) {
    const int row = 64 * wr + 16 * (i16 >> 2) + (i16 & 3), f = ((i16 & 3) >> 1) | ((i16 >> 2) << 1);
#pragma unroll
    for (int kk = 0; kk < 2; ++kk) ax[kk] = lbase + row * 128 + (((4 * kk + G) ^ f) << 4);
    const int f2 = (i16 >> 1) & 7;
#pragma unroll
    for (int kk = 0; kk < 2; ++kk) aw[kk] = lbase + XSLOT + (64 * wc + i16) * 128 + (((4 * kk + G) ^ f2) << 4);
  } else {
    const int row = 64 * wr + i16, f = (i16 >> 1) & 7;
#pragma unroll
    for (int kk = 0; kk < 2; ++kk) ax[kk] = lbase + row * 128 + (((4 * kk + G) ^ f) << 4);
    const int q = (lane >> 2) & 3, pp = lane & 3;
    aw[0] = aw[1] = lbase + XSLOT + (G >> 1) * 5120 + (4 * (G & 1) + q) * 160 + pp * 8;
  }
  auto kchunk = [&](int s) { int k = s + cx.rot; return k >= 10 ? k - 10 : k; };
  auto dma = [&](int slot, int sox, int sow, int i) {
    char* b = cx.smem + slot;
    if (i < 4) dma16(cx.rx, b + cx.xdst + i * 1024, cx.vx[i], sox + i * 10240);
    else if (SSM) { if (i < 6) dma16(cx.rw, b + XSLOT + cx.wdst + (i - 4) * 1024, cx.vw[i - 4], sow + (i - 4) * 10240); }
    else if (i < 6) dma16(cx.rw, b + XSLOT + cx.wdst + (i - 4) * 1024, cx.vw[i - 4], sow);
    else { if (lane < 32) dma16(cx.rw, b + XSLOT + cx.wdst + 2048, cx.vw[2], sow); }
  };
  Frags<MODE> fa, fb;
  fa.template rdx<0>(ax[0]); fa.template rdx<1>(ax[0]); fa.template rdx<2>(ax[0]); fa.template rdx<3>(ax[0]);
  fa.template rdw<0, 0>(aw[0]); fa.template rdw<0, 1>(aw[0]); fa.template rdw<0, 2>(aw[0]); fa.template rdw<0, 3>(aw[0]);
  if constexpr (NT > 4) { fa.template rdw<0, 4 % NT>(aw[0]); fa.template rdw<0, 5 % NT>(aw[0]); }

#define MM1(F_, C, T) acc[T][C] = SSM ? mfma16(F_.x[T], F_.w(C), acc[T][C]) : mfma16(F_.w(C), F_.x[T], acc[T][C])
#define MMG(F_, C) do { MM1(F_, C, 0); MM1(F_, C, 1); MM1(F_, C, 2); MM1(F_, C, 3); } while (0)
#define NREADS (SSM ? 8 : 4 + 2 * NT)
#define RD(N_, KK, I, WA, XA) do { \
    if constexpr ((I) < 4) N_.template rdx<(I) & 3>(XA); \
    else if constexpr (SSM) { if constexpr ((I) < 8) N_.template rdw_lo<KK, ((I) - 4) & 3>(WA); } \
    else if constexpr ((I) < 4 + 2 * NT) { if constexpr (((I) & 1) == 0) N_.template rdw_lo<KK, (((I) - 4) >> 1) % NT>(WA); else N_.template rdw_hi<KK, (((I) - 4) >> 1) % NT>(WA); } \
  } while (0)
#define GRP(F_, N_, KK, C, R0, WA, XA, EXTRA) do { \
    MM1(F_, C, 0); RD(N_, KK, (R0), WA, XA); SB(); \
    MM1(F_, C, 1); RD(N_, KK, (R0) + 1, WA, XA); SB(); \
    MM1(F_, C, 2); RD(N_, KK, (R0) + 2, WA, XA); SB(); \
    MM1(F_, C, 3); RD(N_, KK, (R0) + 3, WA, XA); EXTRA; SB(); } while (0)
#define CSEL1(F_, C) do { const unsigned v_ = (io_ == (C)) ? 0x3F803F80u : 0u; const u32x4 s_ = {v_, v_, v_, v_}; csa = SSM ? mfma16(s_, F_.w(C), csa) : mfma16(F_.w(C), s_, csa); } while (0)
#define CSELALL(F_, M) do { if (M) { int io_ = i16; asm volatile("" : "+v"(io_)); CSEL1(F_, 0); CSEL1(F_, 1); CSEL1(F_, 2); CSEL1(F_, 3); if constexpr (NT > 4) { CSEL1(F_, 4 % NT); CSEL1(F_, 5 % NT); } } SB(); } while (0)
  int slot = 0;
  auto stage = [&](const int s, auto dx_t, auto dw_t, auto bnd_t) __attribute__((always_inline)) {
    constexpr bool DX = decltype(dx_t)::value, DW = decltype(dw_t)::value;
    constexpr int BND = decltype(bnd_t)::value;
    const int nslot = slot == 2 * SLOT ? 0 : slot + SLOT;
    int sox = 0, sow = 0, pows = 0, pslot = 0;
    if constexpr (DX) { sox = cx.xso_base + kchunk(s + 3) * 128; asm volatile("" : "+s"(sox)); }
    if constexpr (DW) { pows = cx.wso_base + kchunk(s + 2) * cx.wso_step; pslot = nslot == 2 * SLOT ? 0 : nslot + SLOT; asm volatile("" : "+s"(pows), "+s"(pslot)); }
    fa.wait(); SB();
    {
      const unsigned X1 = ax[1] + slot, W1 = aw[1] + slot;
      GRP(fa, fb, 1, 0, 0, W1, X1, if constexpr (DW) dma(pslot, 0, pows, 4));
      GRP(fa, fb, 1, 1, 4, W1, X1, if constexpr (DW) dma(pslot, 0, pows, 5));
      GRP(fa, fb, 1, 2, 8, W1, X1, if constexpr (!SSM && DW) dma(pslot, 0, pows, 6));
      GRP(fa, fb, 1, 3, 12, W1, X1, );
      if constexpr (NT > 4) {
        GRP(fa, fb, 1, 4 % NT, 16, W1, X1, );
        GRP(fa, fb, 1, 5 % NT, 20, W1, X1, );
      }
      CSELALL(fa, ((2 * s) & 3) == wr);
      if constexpr (BND == 0) hook9(std::integral_constant<int, MODE>{});
    }
    fb.wait(); SB();
    const unsigned X0 = ax[0] + nslot, W0 = aw[0] + nslot;
    MMG(fb, 0); SB();
    if constexpr (BND == 1) { if (SSM) WAIT_V(6); else WAIT_V(7); LBAR(); }
    if constexpr (BND == 2) { WAIT_V(0); LBAR(); }
    if constexpr (BND == 3) {
      if (w < 4) { if (SSM) WAIT_V(14); else WAIT_V(16); } else WAIT_V(0);
      LBAR();
    }
    GRP(fb, fa, 0, 1, 0, W0, X0, if constexpr (DX) dma(slot, sox, sow, 0));
    GRP(fb, fa, 0, 2, 4, W0, X0, if constexpr (DX) dma(slot, sox, sow, 1));
    GRP(fb, fa, 0, 3, 8, W0, X0, if constexpr (DX) dma(slot, sox, sow, 2));
    if constexpr (NT > 4) {
      GRP(fb, fa, 0, 4 % NT, 12, W0, X0, if constexpr (DX) dma(slot, sox, sow, 3));
      GRP(fb, fa, 0, 5 % NT, 16, W0, X0, );
    } else { if constexpr (DX) dma(slot, sox, sow, 3); }
    CSELALL(fb, ((2 * s + 1) & 3) == wr);
    slot = nslot;
  };
  using T_ = std::true_type; using F_ = std::false_type;
  using I0 = std::integral_constant<int, 0>; using I1 = std::integral_constant<int, 1>; using I2 = std::integral_constant<int, 2>;
  stage(0, T_{}, F_{}, std::integral_constant<int, 3>{});
  hook7();
#pragma unroll 1
  for (int s = 1; s < 6; ++s) stage(s, T_{}, T_{}, I1{});
  hookA();
  stage(6, T_{}, T_{}, I1{});
  hook8();
  stage(7, F_{}, T_{}, I1{});
  stage(8, F_{}, F_{}, I2{});
  stage(9, F_{}, F_{}, I0{});
  fa.wait();
}

constexpr int LWS = 0;
#define DWF_ 5
constexpr int DWF = DWF_;
constexpr int DW = 6;
struct P2Ctx { __amdgpu_buffer_rsrc_t rw; int sw, vo; };
DI void p2_ldw(const P2Ctx& c, u32x4 (&wv)[5], int k) {
#pragma unroll
  for (int j = 0; j < 5; ++j) wv[j] = __builtin_amdgcn_raw_buffer_load_b128(c.rw, c.vo, c.sw + k * 5120 + j * 1024, 16);
}
template <bool SSM>
DI unsigned phase1(const Params& p, char* smem, int mt, int cj  , int blk, int rot, P2Ctx& p2c, u32x4 (&wv)[DW][5]) {
  int tid = threadIdx.x, lane = tid & 63;
  const int w = __builtin_amdgcn_readfirstlane(tid >> 6);
  const int wr = w & 3, wc = w >> 2;
  int G = lane >> 4, i16 = lane & 15;
  auto relane = [&]() { int tz = threadIdx.x; asm volatile("" : "+v"(tz)); tid = tz; lane = tz & 63; G = lane >> 4; i16 = lane & 15; p2c.vo = lane * 16; };
  const unsigned lbase = (unsigned)(size_t)(lds_cp)smem;
  TS(0);
  const int cbi = SSM ? 22 + cj : cj;
  unsigned etag;
  if (w == 7) {
    const char* a;
    if (lane < 32) a = p.ws + WS_ACT + ((size_t)lane * NSTR + NSTEP) * 4096;
    else if (lane < 40) a = p.ws + WS_W2T + ((size_t)(lane - 32) * NSTR + NSTEP) * 5120;
    else if (lane < 50) a = (const char*)p.out + (size_t)(lane - 40) * (512u << 10);
    else a = (const char*)p.dsk;
    __builtin_amdgcn_global_load_lds((const unsigned*)a, (unsigned*)(smem + LDS_PAR + 2048), 4, 0, 0);
  }
  Ctx cx;
  cx.smem = smem; cx.rot = rot;
  cx.rx = __builtin_amdgcn_make_buffer_rsrc((void*)(p.x + (size_t)(256 * mt) * D), 0, 256 * D * 2, RSRC3);
#pragma unroll
  for (int i = 0; i < 4; ++i) {
    int f;
    if (SSM) f = ((lane >> 4) & 1) | ((((2 * w + (i >> 1)) & 3)) << 1);
    else f = ((i & 1) << 2) | (lane >> 4);
    cx.vx[i] = (lane >> 3) * 1280 + (((lane & 7) ^ f) << 4);
  }
  cx.xso_base = w * 32 * 1280; cx.xdst = w * 4096;
  auto wbase = [&](int v) { return SSM ? (64 * cj + 32 * (v >> 2) + 16 * ((v & 3) >> 1)) * 1280 : 16 * (v >> 1) * 3456; };
  auto wdst = [&](int v) { return SSM ? v * 2048 : (v >> 1) * 5120 + (v & 1) * 2560; };
  if (SSM) {
    cx.rw = __builtin_amdgcn_make_buffer_rsrc((void*)((w & 1) ? p.bim : p.bre), 0, P * D * 2, RSRC3);
    cx.vw[0] = (lane >> 3) * 1280 + (((lane & 7) ^ (lane >> 4)) << 4); cx.vw[1] = cx.vw[0] ^ 64; cx.vw[2] = 0;
    cx.wso_step = 128;
  } else {
    cx.rw = __builtin_amdgcn_make_buffer_rsrc((void*)((w & 1) ? p.w3 : p.w1), 0, D * F * 2, RSRC3);
#pragma unroll
    for (int i = 0; i < 3; ++i) {
      const int n = 64 * i + lane, pos = (n / 10) & 15, row = (pos & 3) | ((pos & 4) << 1) | ((pos & 8) >> 1);
      cx.vw[i] = row * 3456 + (n % 10) * 16 + cj * 160;
    }
    cx.wso_step = 64 * 3456;
  }
  cx.wso_base = wbase(w); cx.wdst = wdst(w);
  auto kchunk = [&](int s) { int k = s + rot; return k >= 10 ? k - 10 : k; };
  auto issue_for = [&](int v, int s, int slot) {
    const int sox = v * 32 * 1280 + kchunk(s) * 128, sow = wbase(v) + kchunk(s) * cx.wso_step;
    char* b = smem + slot;
#pragma unroll
    for (int i = 0; i < 4; ++i) dma16(cx.rx, b + v * 4096 + i * 1024, cx.vx[i], sox + i * 10240);
    dma16(cx.rw, b + XSLOT + wdst(v), cx.vw[0], sow);
    dma16(cx.rw, b + XSLOT + wdst(v) + 1024, cx.vw[1], SSM ? sow + 10240 : sow);
    if (!SSM) { if (lane < 32) dma16(cx.rw, b + XSLOT + wdst(v) + 2048, cx.vw[2], sow); }
  };
  const __amdgpu_buffer_rsrc_t rW2T = __builtin_amdgcn_make_buffer_rsrc((void*)(p.ws + WS_W2T), 0, (int)W2T_BYTES, RSRC3);
  auto stats_job = [&](const u32x4& r0, const u32x4& r1) {
    float v[16];
#pragma unroll
    for (int i = 0; i < 4; ++i) { v[2 * i] = __uint_as_float(r0[i] << 16); v[2 * i + 1] = __uint_as_float(r0[i] & 0xFFFF0000u); v[8 + 2 * i] = __uint_as_float(r1[i] << 16); v[9 + 2 * i] = __uint_as_float(r1[i] & 0xFFFF0000u); }
    float sum = 0.f;
#pragma unroll
    for (int i = 0; i < 16; ++i) sum += v[i];
    sum = wave_sum(sum);
    const float mean = sum * (1.0f / D);
    float sq = 0.f;
#pragma unroll
    for (int i = 0; i < 8; ++i) { const float d = v[i] - mean; sq = fmaf(d, d, sq); }
    float sq2 = 0.f;
#pragma unroll
    for (int i = 8; i < 16; ++i) { const float d = v[i] - mean; sq2 = fmaf(d, d, sq2); }
    sq += lane < 16 ? sq2 : 0.f;
    sq = wave_sum(sq);
    const float rstd = 1.0f / sqrtf(sq * (1.0f / D) + 1e-5f);
    if (lane == 0) {
      gu64* g = (gu64*)(p.ws + WS_STATS + (size_t)(256 * mt + 8 * cbi + w) * 16);
      __hip_atomic_store(g, ((unsigned long long)etag << 32) | __float_as_uint(mean), RLX_AGENT);
      __hip_atomic_store(g + 1, ((unsigned long long)etag << 32) | __float_as_uint(rstd), RLX_AGENT);
    }
  };
  const __amdgpu_buffer_rsrc_t rrow = __builtin_amdgcn_make_buffer_rsrc((void*)(p.x + (size_t)(256 * mt + 8 * cbi + w) * D), 0, D * 2, RSRC3);
  if (w < 4) {
    unsigned e_, pa_ = 0, pb_ = 0, pc_ = 0; u32x4 r0, r1;
    { const int vo = lane * 16;
      asm volatile("global_load_dword %2, %5, off sc1\n\tbuffer_load_dwordx4 %0, %3, %4, 0 offen\n\tbuffer_load_dwordx4 %1, %3, %4, 0 offen offset:1024"
                   : "=&v"(r0), "=&v"(r1), "=&v"(e_) : "v"(vo), "s"(rrow), "v"(p.ws + WS_EPOCH) : "memory"); }
    if constexpr (SSM) {
      const int ch = 64 * cj + lane;
      asm volatile("global_load_ushort %0, %3, off\n\tglobal_load_ushort %1, %4, off\n\tglobal_load_ushort %2, %5, off"
                   : "=&v"(pa_), "=&v"(pb_), "=&v"(pc_) : "v"(p.lre + ch), "v"(p.lim + ch), "v"(p.ldt + ch) : "memory");
    }
    issue_for(w, 0, 0); issue_for(w + 4, 0, 0);
    TS(6);
    issue_for(w, 1, SLOT); issue_for(w + 4, 1, SLOT);
    if constexpr (SSM) asm volatile("s_waitcnt vmcnt(24)" : "+v"(e_), "+v"(r0), "+v"(r1), "+v"(pa_), "+v"(pb_), "+v"(pc_) :: "memory"); else asm volatile("s_waitcnt vmcnt(28)" : "+v"(e_), "+v"(r0), "+v"(r1) :: "memory");
    etag = e_ + 1u;
    stats_job(r0, r1);
    if (SSM && w == 3) {
      const float a = bf2f((u16)pa_), b = bf2f((u16)pb_);
      const float dt = rbf(expf(bf2f((u16)pc_)));
      const float er = expf(dt * a);
      float sn, cn; sincosf(dt * b, &sn, &cn);
      const float lr = er * cn, li = er * sn;
      const float nr = lr - 1.0f, ni = li, den = a * a + b * b;
      const float cr = (nr * a + ni * b) / den, ci = (ni * a - nr * b) / den;
      float pr = lr, pi = li;
#pragma unroll
      for (int i = 0; i < 4; ++i) { float t = pr * pr - pi * pi; pi = 2.f * pr * pi; pr = t; }
      const float l16r = pr, l16i = pi;
#pragma unroll
      for (int i = 0; i < 4; ++i) { float t = pr * pr - pi * pi; pi = 2.f * pr * pi; pr = t; }
      float* par = (float*)(smem + LDS_PAR) + lane * 8;
      *(f32x4*)par = f32x4{lr, li, cr, ci};
      *(f32x4*)(par + 4) = f32x4{l16r, l16i, pr, pi};
    }
    if (SSM) WAIT_V(14); else WAIT_V(16);
    TS(13);
    WAIT_L0();
    BAR();
    issue_for(w, 2, 2 * SLOT); issue_for(w + 4, 2, 2 * SLOT);
  } else {
    const int hv = w - 4;
    unsigned e_; u32x4 r0, r1;
    { const int vo = lane * 16;
      asm volatile("global_load_dword %2, %5, off sc1\n\tbuffer_load_dwordx4 %0, %3, %4, 0 offen\n\tbuffer_load_dwordx4 %1, %3, %4, 0 offen offset:1024"
                   : "=&v"(r0), "=&v"(r1), "=&v"(e_) : "v"(vo), "s"(rrow), "v"(p.ws + WS_EPOCH) : "memory"); }
    const int unit = hv * 256 + blk;
    const bool has_unit = unit < 940, ffn_unit = unit < 540;
    char* rt = smem + LDS_RT + hv * 4096;
    int c0, s0u;
    if (ffn_unit) { s0u = unit / 10; c0 = 4 * (unit % 10); } else { const int u = unit - 540; c0 = u / 10; s0u = NSTEP_F + 4 * (u % 10); }
    if (has_unit) {
      if (ffn_unit) {
        const __amdgpu_buffer_rsrc_t r2 = __builtin_amdgcn_make_buffer_rsrc((void*)p.w2, 0, F * D * 2, RSRC3);
        const int vo = (lane >> 3) * 1280 + (lane & 7) * 16;
#pragma unroll
        for (int i = 0; i < 4; ++i) dma16(r2, rt + i * 1024, vo, (32 * s0u + 8 * i) * 1280 + (c0 >> 2) * 128);
      } else {
        const int vo = (lane & 15) * 1280 + (lane >> 4) * 16;
#pragma unroll
        for (int i = 0; i < 4; ++i) {
          const int sp = s0u - NSTEP_F + i, jg = sp >> 1, part = sp & 1;
          const __amdgpu_buffer_rsrc_t rc = __builtin_amdgcn_make_buffer_rsrc((void*)(part ? p.cim : p.cre), 0, D * P * 2, RSRC3);
          dma16(rc, rt + i * 1024, vo, 16 * c0 * 1280 + jg * 64);
        }
      }
    }
    asm volatile("s_waitcnt vmcnt(0)" : "+v"(e_), "+v"(r0), "+v"(r1) :: "memory");
    etag = e_ + 1u;
    u32x4 vv[4];
    {
      const unsigned rtl = lbase + LDS_RT + hv * 4096;
      if (ffn_unit) {
        const int g = lane >> 4, q = (lane >> 2) & 3, pp = lane & 3;
        const unsigned a0 = rtl + (8 * g + q) * 128 + 8 * pp;
        u32x2 l0 = trrd<0>(a0), h0 = trrd<512>(a0), l1 = trrd<32>(a0), h1 = trrd<512 + 32>(a0), l2 = trrd<64>(a0), h2 = trrd<512 + 64>(a0), l3 = trrd<96>(a0), h3 = trrd<512 + 96>(a0);
        asm volatile("s_waitcnt lgkmcnt(0)" : "+v"(l0), "+v"(h0), "+v"(l1), "+v"(h1), "+v"(l2), "+v"(h2), "+v"(l3), "+v"(h3) :: "memory");
        vv[0] = cat(l0, h0); vv[1] = cat(l1, h1); vv[2] = cat(l2, h2); vv[3] = cat(l3, h3);
      } else {
        u32x4 v0 = rd128<0>(rtl + lane * 16), v1 = rd128<1024>(rtl + lane * 16), v2 = rd128<2048>(rtl + lane * 16), v3 = rd128<3072>(rtl + lane * 16);
        asm volatile("s_waitcnt lgkmcnt(0)" : "+v"(v0), "+v"(v1), "+v"(v2), "+v"(v3) :: "memory");
        vv[0] = v0; vv[1] = v1; vv[2] = v2; vv[3] = v3;
      }
    }
    BAR();
    if (has_unit) {
#pragma unroll
      for (int i = 0; i < 4; ++i) {
        const int c = ffn_unit ? c0 + i : c0, s = ffn_unit ? s0u : s0u + i;
        st_sc1(vv[i], rW2T, lane * 16 + (((c / 5) * NSTR + s) * 5 + (c % 5)) * 1024);
      }
    }
    stats_job(r0, r1);
  }
  TS(1);
  auto hook0 = [&]() {};
  auto hook7 = [&]() { if (tid == 0) __hip_atomic_store((gu32*)(p.ws + WS_FLAGW) + blk, etag, RLX_AGENT); };
  const __amdgpu_buffer_rsrc_t rACT = __builtin_amdgcn_make_buffer_rsrc((void*)(p.ws + WS_ACT), 0, (int)ACT_BYTES, RSRC3);
  const __amdgpu_buffer_rsrc_t rFW = __builtin_amdgcn_make_buffer_rsrc((void*)(p.ws + WS_FLAGW), 0, 1024, RSRC3);
  const int mt64 = 4 * mt + wr;
  gu64* stg;
  unsigned long long sta = 0, stb = 0;
  u32x4 fw;
  auto hookA = [&]() { const int vo_ = lane * 16; asm volatile("buffer_load_dwordx4 %0, %1, %2, 0 offen sc1" : "=&v"(fw) : "v"(vo_), "s"(rFW) : "memory"); };
  auto hook8 = [&]() {
    if constexpr (SSM) asm volatile("s_waitcnt vmcnt(6)" : "+v"(fw) :: "memory"); else asm volatile("s_waitcnt vmcnt(7)" : "+v"(fw) :: "memory");
    if (w == 0) {
      unsigned spins = 0;
      while (!__all(fw.x == etag && fw.y == etag && fw.z == etag && fw.w == etag)) {
        if (++spins > SPIN_MAX) break;
        __builtin_amdgcn_s_sleep(1);
        fw = __builtin_amdgcn_raw_buffer_load_b128(rFW, lane * 16, 0, 16);
      }
      if (blk == 0 && lane == 0) __hip_atomic_store((gu32*)(p.ws + WS_EPOCH), etag, RLX_AGENT);
    }
  };
  auto hook9 = [&](auto mode_tag) {
    int tz = threadIdx.x; asm volatile("" : "+v"(tz));
    stg = (gu64*)(p.ws + WS_STATS + (size_t)(256 * mt + 64 * wr + (tz & 63)) * 16);
    sta = __hip_atomic_load(stg, RLX_AGENT); stb = __hip_atomic_load(stg + 1, RLX_AGENT);
  };
  f32x4 csa = {0.f, 0.f, 0.f, 0.f};
  float* cst = (float*)(smem + LDS_CS);
  float* stt = (float*)(smem + LDS_ST);
  auto finish_common = [&](int ntw  , int tile0  ) {
    unsigned spins = 0;
    while (!__all((unsigned)(sta >> 32) == etag && (unsigned)(stb >> 32) == etag)) {
      if (++spins > SPIN_MAX) break;
      __builtin_amdgcn_s_sleep(1);
      sta = __hip_atomic_load(stg, RLX_AGENT); stb = __hip_atomic_load(stg + 1, RLX_AGENT);
    }
    const float mean = __uint_as_float((unsigned)sta), rstd = __uint_as_float((unsigned)stb);
    if (wc == 0) *(f32x2*)(stt + (64 * wr + lane) * 2) = f32x2{rstd, -mean * rstd};
    if (SSM) { if (G == 0) {
#pragma unroll
        for (int c = 0; c < 4; ++c) cst[wr * 160 + (tile0 + c) * 16 + i16] = csa[c]; } }
    else { if (i16 < ntw) *(f32x4*)(cst + wr * 160 + (tile0 + i16) * 16 + 4 * G) = csa; }
    WAIT_L0();
    BAR();
  };
  auto cs4 = [&](int idx) { return (*(const f32x4*)(cst + idx) + *(const f32x4*)(cst + 160 + idx)) + (*(const f32x4*)(cst + 320 + idx) + *(const f32x4*)(cst + 480 + idx)); };
  auto cs1 = [&](int idx) { return (cst[idx] + cst[160 + idx]) + (cst[320 + idx] + cst[480 + idx]); };

  if (!SSM) {
    auto ffn = [&](auto mode_tag) {
      constexpr int MODE = decltype(mode_tag)::value;
      constexpr int NT = Frags<MODE>::NT, P0 = MODE == 0 ? 0 : 3;
      f32x4 acc[4][NT];
#pragma unroll
      for (int T = 0; T < 4; ++T)
#pragma unroll
        for (int c = 0; c < NT; ++c) acc[T][c] = f32x4{0.f, 0.f, 0.f, 0.f};
      main_loop<MODE>(p, cx, lbase, w, lane, acc, csa, hook0, hook7, hookA, hook8, hook9);
      TS(2);
      relane();
      finish_common(NT, 2 * P0);
      constexpr int NB = 4 * (NT / 2);
      auto ldw_part = [&](int b) {
#pragma unroll
        for (int j = 0; j < 20; ++j)
          if (j * NB / 20 == b) wv[j / 5][j % 5] = __builtin_amdgcn_raw_buffer_load_b128(p2c.rw, p2c.vo, p2c.sw + (j / 5 == 3 ? 3 : 6 + j / 5) * 5120 + (j % 5) * 1024, 16);
      };
      f32x2 st4[4];
#pragma unroll
      for (int T = 0; T < 4; ++T) st4[T] = *(const f32x2*)(stt + (64 * wr + 16 * T + i16) * 2);
#pragma unroll
      for (int Pq = 0; Pq < NT / 2; ++Pq) {
        const int ft = 5 * cj + P0 + Pq;
        const f32x4 cg = cs4((2 * P0 + 2 * Pq) * 16 + 4 * G), cu = cs4((2 * P0 + 2 * Pq + 1) * 16 + 4 * G);
        unsigned pk[4][2];
#pragma unroll
        for (int T = 0; T < 4; ++T) {
          float hv[4];
#pragma unroll
          for (int e = 0; e < 4; ++e) {
            const float g = fmaf(acc[T][2 * Pq][e], st4[T].x, st4[T].y * cg[e]);
            const float u = fmaf(acc[T][2 * Pq + 1][e], st4[T].x, st4[T].y * cu[e]);
            hv[e] = silu_f(g) * u;
          }
          pk[T][0] = pack2(hv[0], hv[1]); pk[T][1] = pack2(hv[2], hv[3]);
          ldw_part(4 * Pq + T);
          SB();
        }
        if (ft < 108) {
#pragma unroll
          for (int T2 = 0; T2 < 4; T2 += 2) {
            const auto a = __builtin_amdgcn_permlane16_swap(pk[T2][0], pk[T2 + 1][0], false, false);
            const auto b = __builtin_amdgcn_permlane16_swap(pk[T2][1], pk[T2 + 1][1], false, false);
            const u32x4 v = {a[0], b[0], a[1], b[1]};
            const int s2 = ft >> 1, gp = 2 * (ft & 1) + (G >> 1), T = T2 + (G & 1);
            st_sc1(v, rACT, T * 1024 + (gp * 16 + i16) * 16 + (mt64 * NSTR + s2) * 4096);
          }
        }
      }
    };
    if (wc == 0) ffn(std::integral_constant<int, 0>{}); else ffn(std::integral_constant<int, 1>{});
  } else {
    f32x4 acc[4][4];
#pragma unroll
    for (int T = 0; T < 4; ++T)
#pragma unroll
      for (int c = 0; c < 4; ++c) acc[T][c] = f32x4{0.f, 0.f, 0.f, 0.f};
    main_loop<2>(p, cx, lbase, w, lane, acc, csa, hook0, hook7, hookA, hook8, hook9);
    TS(2);
    relane();
    const float* par = (const float*)(smem + LDS_PAR);
    float lr[2], li[2], cr[2], ci[2];
#pragma unroll
    for (int j = 0; j < 2; ++j) {
      const f32x4 pv = *(const f32x4*)(par + (32 * wc + 16 * j + i16) * 8);
      lr[j] = pv.x; li[j] = pv.y; cr[j] = pv.z; ci[j] = pv.w;
    }
    f32x4 pv = *(const f32x4*)(par + lane * 8 + 4);
    asm volatile("" : "+v"(pv), "+v"(lr[0]), "+v"(li[0]), "+v"(cr[0]), "+v"(ci[0]), "+v"(lr[1]), "+v"(li[1]), "+v"(cr[1]), "+v"(ci[1]));
    finish_common(4, 4 * wc);
    {
      float csl[4];
#pragma unroll
      for (int c = 0; c < 4; ++c) csl[c] = cs1((4 * wc + c) * 16 + i16);
#pragma unroll
      for (int T = 0; T < 4; ++T)
#pragma unroll
        for (int e = 0; e < 4; e += 2) {
          const f32x4 sv = *(const f32x4*)(stt + (64 * wr + 16 * G + 4 * T + e) * 2);
#pragma unroll
          for (int c = 0; c < 4; ++c) {
            acc[T][c][e] = fmaf(acc[T][c][e], sv.x, sv.y * csl[c]);
            acc[T][c][e + 1] = fmaf(acc[T][c][e + 1], sv.z, sv.w * csl[c]);
          }
        }
    }
    f32x2* et = (f32x2*)(smem + LDS_ET);
    const int seg = 4 * wr + G;
#pragma unroll
    for (int j = 0; j < 2; ++j) {
      float hr = 0.f, hi = 0.f;
#pragma unroll
      for (int i = 0; i < 16; ++i) {
        const float nr = fmaf(lr[j], hr, fmaf(-li[j], hi, acc[i >> 2][2 * j][i & 3]));
        const float ni = fmaf(lr[j], hi, fmaf(li[j], hr, acc[i >> 2][2 * j + 1][i & 3]));
        hr = nr; hi = ni;
      }
      et[seg * 64 + 32 * wc + 16 * j + i16] = f32x2{hr, hi};
    }
    WAIT_L0(); BAR();
    TS(3);
    if (w == 0) {
      const unsigned tag = etag;
      float xr_ = 0.f, xi_ = 0.f;
      f32x2 ev[16];
#pragma unroll
      for (int s = 0; s < 16; ++s) ev[s] = et[s * 64 + lane];
#pragma unroll
      for (int s = 0; s < 16; ++s) {
        const float nr = fmaf(pv.x, xr_, fmaf(-pv.y, xi_, ev[s].x));
        const float ni = fmaf(pv.x, xi_, fmaf(pv.y, xr_, ev[s].y));
        xr_ = nr; xi_ = ni;
      }
      gu64* gran = (gu64*)(p.ws + WS_GRAN);
      if (mt < 7) {
        gu64* g = gran + ((size_t)(mt * 10 + cj) * 64 + lane) * 2;
        __hip_atomic_store(g, ((unsigned long long)tag << 32) | __float_as_uint(xr_), RLX_AGENT);
        __hip_atomic_store(g + 1, ((unsigned long long)tag << 32) | __float_as_uint(xi_), RLX_AGENT);
      }
      float Hr = 0.f, Hi = 0.f;
      if (mt > 0) {
        unsigned long long ga[7], gb[7];
        unsigned spins = 0;
        while (true) {
          bool ok = true;
#pragma unroll
          for (int m = 0; m < 7; ++m) {
            if (m < mt) {
              gu64* g = gran + ((size_t)(m * 10 + cj) * 64 + lane) * 2;
              ga[m] = __hip_atomic_load(g, RLX_AGENT); gb[m] = __hip_atomic_load(g + 1, RLX_AGENT);
            } else { ga[m] = gb[m] = (unsigned long long)tag << 32; }
          }
#pragma unroll
          for (int m = 0; m < 7; ++m) ok = ok && ((unsigned)(ga[m] >> 32) == tag) && ((unsigned)(gb[m] >> 32) == tag);
          if (__all(ok)) break;
          if (++spins > SPIN_MAX) break;
          __builtin_amdgcn_s_sleep(1);
        }
#pragma unroll
        for (int m = 0; m < 7; ++m) {
          if (m < mt) {
            const float sr = __uint_as_float((unsigned)ga[m]), si = __uint_as_float((unsigned)gb[m]);
            const float nr = fmaf(pv.z, Hr, fmaf(-pv.w, Hi, sr));
            const float ni = fmaf(pv.z, Hi, fmaf(pv.w, Hr, si));
            Hr = nr; Hi = ni;
          }
        }
      }
      xr_ = Hr; xi_ = Hi;
#pragma unroll
      for (int s = 0; s < 16; ++s) {
        et[s * 64 + lane] = f32x2{xr_, xi_};
        const float nr = fmaf(pv.x, xr_, fmaf(-pv.y, xi_, ev[s].x));
        const float ni = fmaf(pv.x, xi_, fmaf(pv.y, xr_, ev[s].y));
        xr_ = nr; xi_ = ni;
      }
    }
    WAIT_L0(); BAR();
    TS(4);
    p2_ldw(p2c, wv[0], 6); p2_ldw(p2c, wv[1], 7);
    SB();
    f32x2 x0[2];
#pragma unroll
    for (int j = 0; j < 2; ++j) x0[j] = et[seg * 64 + 32 * wc + 16 * j + i16];
    unsigned pre[2][8], pim[2][8];
#pragma unroll
    for (int j = 0; j < 2; ++j) {
      float hr = x0[j].x, hi = x0[j].y;
      float ore[16], oim[16];
#pragma unroll
      for (int i = 0; i < 16; ++i) {
        const float nr = fmaf(lr[j], hr, fmaf(-li[j], hi, acc[i >> 2][2 * j][i & 3]));
        const float ni = fmaf(lr[j], hi, fmaf(li[j], hr, acc[i >> 2][2 * j + 1][i & 3]));
        hr = nr; hi = ni;
        ore[i] = cr[j] * hr - ci[j] * hi;
        oim[i] = -(cr[j] * hi + ci[j] * hr);
      }
#pragma unroll
      for (int i = 0; i < 8; ++i) { pre[j][i] = pack2(ore[2 * i], ore[2 * i + 1]); pim[j][i] = pack2(oim[2 * i], oim[2 * i + 1]); }
    }
    {
      char* tp = smem + LDS_TP + w * 4608;
      const int q = (lane >> 2) & 3, pp = lane & 3;
      const unsigned tb = lbase + LDS_TP + w * 4608 + (8 * G + q) * 144 + 8 * pp;
      const int jg = 2 * cj + wc;
#pragma unroll
      for (int part = 0; part < 2; ++part) {
#pragma unroll
        for (int j = 0; j < 2; ++j) {
          char* t0 = tp + (16 * j + i16) * 144 + 32 * G;
          const unsigned* src = part ? pim[j] : pre[j];
          *(u32x4*)t0 = u32x4{src[0], src[1], src[2], src[3]};
          *(u32x4*)(t0 + 16) = u32x4{src[4], src[5], src[6], src[7]};
        }
        WAIT_L0();
        u32x2 lo[4], hi[4];
        lo[0] = trrd<0>(tb); hi[0] = trrd<576>(tb); lo[1] = trrd<32>(tb); hi[1] = trrd<576 + 32>(tb); lo[2] = trrd<64>(tb); hi[2] = trrd<576 + 64>(tb); lo[3] = trrd<96>(tb); hi[3] = trrd<576 + 96>(tb);
        asm volatile("s_waitcnt lgkmcnt(0)" : "+v"(lo[0]), "+v"(hi[0]), "+v"(lo[1]), "+v"(hi[1]), "+v"(lo[2]), "+v"(hi[2]), "+v"(lo[3]), "+v"(hi[3]) :: "memory");
        const int s2 = NSTEP_F + 2 * jg + part;
#pragma unroll
        for (int T = 0; T < 4; ++T)
          st_sc1(cat(lo[T], hi[T]), rACT, T * 1024 + lane * 16 + (mt64 * NSTR + s2) * 4096);
      }
    }
  }
  TS(5);
  WAIT_V(0);
  BAR();
  if (tid == 0) __hip_atomic_store((gu32*)(p.ws + WS_FLAGA + mt * 128) + cbi, etag, RLX_AGENT);
  asm volatile("" ::: "memory"); SB();
  if (SSM) { p2_ldw(p2c, wv[2], 8); p2_ldw(p2c, wv[3], 3); }
  p2_ldw(p2c, wv[4], 4); p2_ldw(p2c, wv[5], 5);
  SB();
  {
    char* lw = smem + LDS_W2 + w * 15360 + lane * 16;
#pragma unroll
    for (int k = 0; k < 3; ++k)
#pragma unroll
      for (int c = 0; c < 5; ++c) *(u32x4*)(lw + k * 5120 + c * 1024) = wv[k][c];
  }
  SB();
  p2_ldw(p2c, wv[0], 0); p2_ldw(p2c, wv[1], 1); p2_ldw(p2c, wv[2], 2);
  SB();
  return etag;
}

DI void p2_run(const Params& p, const char* lw  , __amdgpu_buffer_rsrc_t ra, const P2Ctx& p2c, u32x4 (&wv)[DW][5],
               int sa, int N, int w, int lane, int mt2, int shA, int shW, unsigned etag, f32x4 (&acc)[4][5]) {
  constexpr int DA = 2, NMIN = 10, NMAX = 14;
  u32x4 a[DA][4];
  const int vo = p2c.vo;
  auto lda = [&](int k, int slot) {
#pragma unroll
    for (int T = 0; T < 4; ++T) a[slot][T] = __builtin_amdgcn_raw_buffer_load_b128(ra, vo, sa + k * 4096 + T * 1024, 16);
  };
  {
    const int fi = w < 4 ? (lane < 22 ? lane : 0) : 22 + (lane < 10 ? lane : 0);
    gu32* fl = (gu32*)(p.ws + WS_FLAGA + mt2 * 128) + fi;
    unsigned spins = 0;
    while (!__all(__hip_atomic_load(fl, RLX_AGENT) == etag)) {
      if (++spins > SPIN_MAX) break;
      __builtin_amdgcn_s_sleep(1);
    }
  }
  __builtin_amdgcn_fence(__ATOMIC_ACQUIRE, "wavefront");
  TS(9);
#pragma unroll
  for (int s = 0; s < DA - 1; ++s) lda(s, s);
#pragma unroll
  for (int T = 0; T < 4; ++T)
#pragma unroll
    for (int c = 0; c < 5; ++c) asm volatile("" : "+v"(acc[T][c]));
  asm volatile("s_nop 7" ::: "memory");
  SB();
#pragma unroll
  for (int s = 0; s < NMAX; ++s) {
    if (s < NMIN || s < N) {
      if (s + DA - 1 < NMIN || s + DA - 1 < N) lda(s + DA - 1, (s + DA - 1) % DA);
      SB();
#pragma unroll
      for (int T = 0; T < 4; ++T)
#pragma unroll
        for (int c = 0; c < 5; ++c) mfma16_acc(acc[T][c], wv[s % DW][c], a[s % DA][T]);
      SB();
      if (s < 3) {
#pragma unroll
        for (int c = 0; c < 5; ++c) wv[s][c] = *(const u32x4*)(lw + s * 5120 + c * 1024);
      } else if (s + DW < NMAX) { if (s + DW < NMIN || s + DW < N) p2_ldw(p2c, wv[s % DW], s + DW); }
      SB();
    }
  }
  asm volatile("s_nop 15\n\ts_nop 7" ::: "memory");
}

DI void phase2(const Params& p, char* smem, unsigned etag, const P2Ctx& p2c, u32x4 (&wv)[DW][5], int mt64, int nt, int s0) {
  const int tid = threadIdx.x, lane = tid & 63, w = __builtin_amdgcn_readfirstlane(tid >> 6);
  TS(8);
  f32x4 acc[4][5];
#pragma unroll
  for (int T = 0; T < 4; ++T)
#pragma unroll
    for (int c = 0; c < 5; ++c) acc[T][c] = f32x4{0.f, 0.f, 0.f, 0.f};
  const __amdgpu_buffer_rsrc_t ra = __builtin_amdgcn_make_buffer_rsrc((void*)(p.ws + WS_ACT), 0, (int)ACT_BYTES, RSRC3);
  const int sa = (mt64 * NSTR + s0) * 4096;
  const int shA = nt & 3, shW = mt64 & 7;
  const int N = w < 2 ? 14 : (w < 4 ? 13 : 10);
  const char* lw = smem + LDS_W2 + w * 15360 + lane * 16;
  p2_run(p, lw, ra, p2c, wv, sa, N, w, lane, mt64 >> 2, shA, shW, etag, acc);
  auto part_addr = [&](int wv_, int t) { return t < 15 ? LDS_W2 + wv_ * (15360) + t * 1024 : wv_ * 5120 + (t - 15) * 1024; };
  {
    const int g = lane >> 4, r = lane & 15;
    const int pos = g * 16 + ((r + 4 * g) & 15);
#pragma unroll
    for (int T = 0; T < 4; ++T)
#pragma unroll
      for (int c = 0; c < 5; ++c) *(f32x4*)(smem + part_addr(w, T * 5 + c) + pos * 16) = acc[T][c];
  }
  f32x4 res[3]; u32x2 xv[3], dv[3]; unsigned long long ga[3], gb[3];
#pragma unroll
  for (int it = 0; it < 3; ++it) {
    const int item = tid + 512 * it;
    if (item < 1280) {
      const int tok = item / 20, dg = item % 20;
      const int gt = mt64 * 64 + tok, d0 = nt * 80 + dg * 4;
      gu64* sg = (gu64*)(p.ws + WS_STATS + (size_t)gt * 16);
      ga[it] = __hip_atomic_load(sg, RLX_AGENT); gb[it] = __hip_atomic_load(sg + 1, RLX_AGENT);
      xv[it] = *(const u32x2*)(p.x + (size_t)gt * D + d0);
      dv[it] = *(const u32x2*)(p.dsk + d0);
    }
  }
  __syncthreads();
  TS(10);
#pragma unroll
  for (int it = 0; it < 3; ++it) {
    const int item = tid + 512 * it;
    if (item < 1280) {
      const int tok = item / 20, dg = item % 20, c = dg >> 2, g = dg & 3, T = tok >> 4, rr = tok & 15;
      const int t = T * 5 + c, off = (g * 16 + ((rr + 4 * g) & 15)) * 16;
      const int gt = mt64 * 64 + tok, d0 = nt * 80 + dg * 4;
      const int ta = t < 15 ? LDS_W2 + t * 1024 : (t - 15) * 1024, st = t < 15 ? 15360 : 5120;
      const char* base = smem + ta + off;
      f32x4 o = *(const f32x4*)(base);
#pragma unroll
      for (int k = 1; k < 4; ++k) o += *(const f32x4*)(base + k * st);
      f32x4 y = *(const f32x4*)(base + 4 * st);
#pragma unroll
      for (int k = 5; k < 8; ++k) y += *(const f32x4*)(base + k * st);
      const float mean = __uint_as_float((unsigned)ga[it]), rstd = __uint_as_float((unsigned)gb[it]);
      const float xs[4] = {__uint_as_float(xv[it].x << 16), __uint_as_float(xv[it].x & 0xFFFF0000u), __uint_as_float(xv[it].y << 16), __uint_as_float(xv[it].y & 0xFFFF0000u)};
      const float ds[4] = {__uint_as_float(dv[it].x << 16), __uint_as_float(dv[it].x & 0xFFFF0000u), __uint_as_float(dv[it].y << 16), __uint_as_float(dv[it].y & 0xFFFF0000u)};
#pragma unroll
      for (int e = 0; e < 4; ++e) {
        const float xn = (xs[e] - mean) * rstd;
        const float yy = fmaf(2.0f, y[e], xn * ds[e]);
        res[it][e] = fmaf(yy, silu_f(o[e]), xs[e]);
      }
      *(f32x4*)(p.out + (size_t)gt * D + d0) = res[it];
    }
  }
  TS(11);
}

__global__ __launch_bounds__(512, 2) void k_fused(Params p) {
  extern __shared__ __attribute__((aligned(16))) char smem[];
  const int b = blockIdx.x, xcd = b & 7, i = b >> 3;
  if ((threadIdx.x >> 6) == 7 && (threadIdx.x & 63) < 14) {
    unsigned long long pc;
    asm volatile("s_getpc_b64 %0" : "=s"(pc));
    const char* a = (const char*)(pc & ~127ull) + (size_t)(i * 14 + (threadIdx.x & 63)) * 128;
    __builtin_amdgcn_global_load_lds((const unsigned*)a, (unsigned*)(smem + LDS_PAR + 2304), 4, 0, 0);
  }
  asm volatile("" :: "s"(p.x), "s"(p.lre), "s"(p.lim), "s"(p.bre), "s"(p.bim), "s"(p.cre), "s"(p.cim), "s"(p.ldt), "s"(p.dsk), "s"(p.w1), "s"(p.w3), "s"(p.w2), "s"(p.out), "s"(p.ws));
  const int ml = i & 3, cl = i >> 2;
  const int mt = (xcd & 1) * 4 + ml, cb = (xcd >> 1) * 8 + cl;
  const int rot = 0;
  const int w = __builtin_amdgcn_readfirstlane(threadIdx.x >> 6), lane = threadIdx.x & 63;
  const int mt64 = (xcd >> 1) * 8 + (i & 7), nt = (xcd & 1) * 4 + (i >> 3);
  const int s0 = w < 4 ? (w < 2 ? 14 * w : 28 + 13 * (w - 2)) : 54 + 10 * (w - 4);
  P2Ctx p2c;
  p2c.rw = __builtin_amdgcn_make_buffer_rsrc((void*)(p.ws + WS_W2T), 0, (int)W2T_BYTES, RSRC3);
  p2c.sw = (nt * NSTR + s0) * 5120; p2c.vo = 0;
  u32x4 wv[DW][5];
  unsigned etag;
  if (cb < 22) etag = phase1<false>(p, smem, mt, cb, b, rot, p2c, wv);
  else etag = phase1<true>(p, smem, mt, cb - 22, b, rot, p2c, wv);
  phase2(p, smem, etag, p2c, wv, mt64, nt, s0);
}
}

extern "C" void kernel_launch(void* const* d_in, const int* in_sizes, int n_in, void* d_out, int out_size, void* d_ws, size_t ws_size, hipStream_t stream) {
  using namespace s5;
  Params p;
  p.x = (const u16*)d_in[0]; p.lre = (const u16*)d_in[1]; p.lim = (const u16*)d_in[2];
  p.bre = (const u16*)d_in[3]; p.bim = (const u16*)d_in[4]; p.cre = (const u16*)d_in[5]; p.cim = (const u16*)d_in[6];
  p.ldt = (const u16*)d_in[7]; p.dsk = (const u16*)d_in[8];
  p.w1 = (const u16*)d_in[10]; p.w3 = (const u16*)d_in[11]; p.w2 = (const u16*)d_in[12];
  p.out = (float*)d_out; p.ws = (char*)d_ws;
  (void)hipFuncSetAttribute((const void*)k_fused, hipFuncAttributeMaxDynamicSharedMemorySize, LDS1_BYTES);
  k_fused<<<256, 512, LDS1_BYTES, stream>>>(p);
}
```

```cpp
#include <hip/hip_runtime.h>
#include <stdint.h>
#include <type_traits>

namespace s5 {
typedef unsigned short u16;
typedef short bf16x8 __attribute__((ext_vector_type(8)));
typedef float f32x4 __attribute__((ext_vector_type(4)));
typedef float f32x2 __attribute__((ext_vector_type(2)));
typedef unsigned u32x4 __attribute__((ext_vector_type(4)));
typedef unsigned u32x2 __attribute__((ext_vector_type(2)));
typedef __bf16 bf16x2 __attribute__((ext_vector_type(2)));
typedef __attribute__((address_space(3))) void* lds_vp;
typedef __attribute__((address_space(3))) char* lds_cp;
typedef __attribute__((address_space(1))) unsigned long long gu64;
typedef __attribute__((address_space(1))) unsigned gu32;
#define DI __device__ __forceinline__
#define RLX_AGENT __ATOMIC_RELAXED, __HIP_MEMORY_SCOPE_AGENT

constexpr int L = 2048, D = 640, P = 640, F = 1728;
constexpr int NSTEP = 94, NSTEP_F = 54;
constexpr size_t WS_EPOCH = 0;
constexpr size_t WS_FLAGW = 1024;
constexpr size_t WS_FLAGA = 4096;
constexpr size_t WS_GRAN = 8192;
constexpr size_t WS_STATS = WS_GRAN + 7 * 10 * 1024 + 2048;
constexpr size_t WS_ACT = 262144;
constexpr int NSTR = NSTEP + 1;
constexpr size_t ACT_BYTES = (size_t)32 * NSTR * 4 * 1024;
constexpr size_t WS_W2T = WS_ACT + ACT_BYTES;
constexpr size_t W2T_BYTES = (size_t)8 * NSTR * 5 * 1024;
static_assert(WS_STATS + 2048 * 16 <= WS_ACT, "ws map");
constexpr int XSLOT = 32768, WSLOT = 20480, SLOT = XSLOT + WSLOT;
constexpr int LDS_PAR = 3 * SLOT;
constexpr int LDS1_BYTES = 163840;
constexpr int LDS_CS = SLOT;
constexpr int LDS_ST = SLOT + 4096;
constexpr int LDS_ET = SLOT + 8192;
constexpr int LDS_TP = SLOT + 16384;
constexpr int LDS_W2 = 40960;
constexpr int LDS_RT = 2 * SLOT;
static_assert(LDS1_BYTES <= 163840, "lds");
constexpr int RSRC3 = 0x00020000;
constexpr unsigned SPIN_MAX = 1u << 21;

struct Params {
  const u16 *x, *lre, *lim, *bre, *bim, *cre, *cim, *ldt, *dsk, *w1, *w3, *w2;
  float* out;
  char* ws;
};

DI float bf2f(u16 v) { return __uint_as_float(((unsigned)v) << 16); }
DI float rbf(float f) { unsigned u = __float_as_uint(f); u += 0x7FFFu + ((u >> 16) & 1u); return __uint_as_float(u & 0xFFFF0000u); }
DI unsigned pack2(float a, float b) { f32x2 p = {a, b}; return __builtin_bit_cast(unsigned, __builtin_convertvector(p, bf16x2)); }
DI f32x4 mfma16(u32x4 a, u32x4 b, f32x4 c) { return __builtin_amdgcn_mfma_f32_16x16x32_bf16(__builtin_bit_cast(bf16x8, a), __builtin_bit_cast(bf16x8, b), c, 0, 0, 0); }
DI void mfma16_acc(f32x4& c, u32x4 a, u32x4 b) { asm volatile("v_mfma_f32_16x16x32_bf16 %0, %1, %2, %0" : "+v"(c) : "v"(a), "v"(b)); }
template <int OFF> DI u32x2 trrd(unsigned addr) { u32x2 r; asm volatile("ds_read_b64_tr_b16 %0, %1 offset:%2" : "=v"(r) : "v"(addr), "i"(OFF) : "memory"); return r; }
template <int OFF> DI u32x4 rd128(unsigned addr) { u32x4 r; asm volatile("ds_read_b128 %0, %1 offset:%2" : "=v"(r) : "v"(addr), "i"(OFF) : "memory"); return r; }
DI u32x4 cat(u32x2 a, u32x2 b) { return u32x4{a.x, a.y, b.x, b.y}; }
DI void dma16(__amdgpu_buffer_rsrc_t rs, char* lds, int voff, int soff) { __builtin_amdgcn_raw_ptr_buffer_load_lds(rs, (lds_vp)lds, 16, voff, soff, 0, 0); }
DI void st_sc1(u32x4 v, __amdgpu_buffer_rsrc_t rs, int off) { __builtin_amdgcn_raw_buffer_store_b128(v, rs, off, 0, 16); }
#define SB() __builtin_amdgcn_sched_barrier(0)
#define WAIT_V(n) asm volatile("s_waitcnt vmcnt(" #n ")" ::: "memory")
#define WAIT_L0() asm volatile("s_waitcnt lgkmcnt(0)" ::: "memory")
#define BAR() __builtin_amdgcn_s_barrier()
DI float wave_sum(float v) {
  v += __builtin_bit_cast(float, __builtin_amdgcn_update_dpp(0, __builtin_bit_cast(int, v), 0x111, 0xf, 0xf, true));
  v += __builtin_bit_cast(float, __builtin_amdgcn_update_dpp(0, __builtin_bit_cast(int, v), 0x112, 0xf, 0xf, true));
  v += __builtin_bit_cast(float, __builtin_amdgcn_update_dpp(0, __builtin_bit_cast(int, v), 0x114, 0xf, 0xf, true));
  v += __builtin_bit_cast(float, __builtin_amdgcn_update_dpp(0, __builtin_bit_cast(int, v), 0x118, 0xf, 0xf, true));
  const int iv = __builtin_bit_cast(int, v);
  const float a = __builtin_bit_cast(float, __builtin_amdgcn_readlane(iv, 15)), b = __builtin_bit_cast(float, __builtin_amdgcn_readlane(iv, 31));
  const float c = __builtin_bit_cast(float, __builtin_amdgcn_readlane(iv, 47)), d = __builtin_bit_cast(float, __builtin_amdgcn_readlane(iv, 63));
  return (a + b) + (c + d);
}
DI float silu_f(float g) { return g * __builtin_amdgcn_rcpf(1.0f + __builtin_amdgcn_exp2f(g * -1.44269504088896f)); }

template <int MODE> struct Frags {
  static constexpr int NT = MODE == 0 ? 6 : 4;
  u32x4 x[4];
  u32x2 lo[MODE == 2 ? 1 : NT], hi[MODE == 2 ? 1 : NT];
  u32x4 v[MODE == 2 ? NT : 1];
  template <int KK, int C> DI void rdw(unsigned aw) {
    if constexpr (MODE == 2) v[C] = rd128<C * 2048>(aw);
    else { constexpr int O = KK * 10240 + (C & 1) * 2560 + ((MODE == 0 ? 0 : 3) + (C >> 1)) * 32; lo[C] = trrd<O>(aw); hi[C] = trrd<O + 1280>(aw); }
  }
  template <int KK, int C> DI void rdw_lo(unsigned aw) {
    if constexpr (MODE == 2) v[C] = rd128<C * 2048>(aw);
    else { constexpr int O = KK * 10240 + (C & 1) * 2560 + ((MODE == 0 ? 0 : 3) + (C >> 1)) * 32; lo[C] = trrd<O>(aw); }
  }
  template <int KK, int C> DI void rdw_hi(unsigned aw) {
    if constexpr (MODE != 2) { constexpr int O = KK * 10240 + (C & 1) * 2560 + ((MODE == 0 ? 0 : 3) + (C >> 1)) * 32; hi[C] = trrd<O + 1280>(aw); }
  }
  template <int T> DI void rdx(unsigned ax) { x[T] = rd128<(MODE == 2 ? 512 : 2048) * T>(ax); }
  DI u32x4 w(int c) const { if constexpr (MODE == 2) return v[c]; else return cat(lo[c], hi[c]); }
  DI void wait() {
    if constexpr (MODE == 0) asm volatile("s_waitcnt lgkmcnt(0)" : "+v"(x[0]), "+v"(x[1]), "+v"(x[2]), "+v"(x[3]), "+v"(lo[0]), "+v"(hi[0]), "+v"(lo[1]), "+v"(hi[1]), "+v"(lo[2]), "+v"(hi[2]),
                                          "+v"(lo[3 % NT]), "+v"(hi[3 % NT]), "+v"(lo[4 % NT]), "+v"(hi[4 % NT]), "+v"(lo[5 % NT]), "+v"(hi[5 % NT]) :: "memory");
    else if constexpr (MODE == 1) asm volatile("s_waitcnt lgkmcnt(0)" : "+v"(x[0]), "+v"(x[1]), "+v"(x[2]), "+v"(x[3]), "+v"(lo[0]), "+v"(hi[0]), "+v"(lo[1]), "+v"(hi[1]), "+v"(lo[2]), "+v"(hi[2]), "+v"(lo[3]), "+v"(hi[3]) :: "memory");
    else asm volatile("s_waitcnt lgkmcnt(0)" : "+v"(x[0]), "+v"(x[1]), "+v"(x[2]), "+v"(x[3]), "+v"(v[0]), "+v"(v[1 % NT]), "+v"(v[2 % NT]), "+v"(v[3 % NT]) :: "memory");
  }
};

struct Ctx {
  __amdgpu_buffer_rsrc_t rx, rw;
  int vx[4];
  int vw[3];
  int xso_base, wso_base, wso_step;
  char* smem;
  int xdst, wdst;
};

template <int MODE, class H7, class HA, class H8, class H9>
DI void main_loop(const Params& p, const Ctx& cx, unsigned lbase, int w, int lane, f32x4 (&acc)[4][Frags<MODE>::NT], f32x4& csa, H7 hook7, HA hookA, H8 hook8, H9 hook9) {
  constexpr bool SSM = MODE == 2;
  constexpr int NT = Frags<MODE>::NT;
  const int wr = w & 3, wc = w >> 2;
  const int G = lane >> 4, i16 = lane & 15;
  unsigned ax[2], aw[2];
  if (SSM) {
    const int row = 64 * wr + 16 * (i16 >> 2) + (i16 & 3), f = ((i16 & 3) >> 1) | ((i16 >> 2) << 1);
#pragma unroll
    for (int kk = 0; kk < 2; ++kk) ax[kk] = lbase + row * 128 + (((4 * kk + G) ^ f) << 4);
    const int f2 = (i16 >> 1) & 7;
#pragma unroll
    for (int kk = 0; kk < 2; ++kk) aw[kk] = lbase + XSLOT + (64 * wc + i16) * 128 + (((4 * kk + G) ^ f2) << 4);
  } else {
    const int row = 64 * wr + i16, f = (i16 >> 1) & 7;
#pragma unroll
    for (int kk = 0; kk < 2; ++kk) ax[kk] = lbase + row * 128 + (((4 * kk + G) ^ f) << 4);
    const int q = (lane >> 2) & 3, pp = lane & 3;
    aw[0] = aw[1] = lbase + XSLOT + (G >> 1) * 5120 + (4 * (G & 1) + q) * 160 + pp * 8;
  }
  auto dma = [&](int slot, int sox, int sow, int i) {
    char* b = cx.smem + slot;
    if (i < 4) dma16(cx.rx, b + cx.xdst + i * 1024, cx.vx[i], sox + i * 10240);
    else if (SSM) { if (i < 6) dma16(cx.rw, b + XSLOT + cx.wdst + (i - 4) * 1024, cx.vw[i - 4], sow + (i - 4) * 10240); }
    else if (i < 6) dma16(cx.rw, b + XSLOT + cx.wdst + (i - 4) * 1024, cx.vw[i - 4], sow);
    else { if (lane < 32) dma16(cx.rw, b + XSLOT + cx.wdst + 2048, cx.vw[2], sow); }
  };
  Frags<MODE> fa, fb;
  fa.template rdx<0>(ax[0]); fa.template rdx<1>(ax[0]); fa.template rdx<2>(ax[0]); fa.template rdx<3>(ax[0]);
  fa.template rdw<0, 0>(aw[0]); fa.template rdw<0, 1>(aw[0]); fa.template rdw<0, 2>(aw[0]); fa.template rdw<0, 3>(aw[0]);
  if constexpr (NT > 4) { fa.template rdw<0, 4 % NT>(aw[0]); fa.template rdw<0, 5 % NT>(aw[0]); }

#define MM1(F_, C, T) acc[T][C] = SSM ? mfma16(F_.x[T], F_.w(C), acc[T][C]) : mfma16(F_.w(C), F_.x[T], acc[T][C])
#define MMG(F_, C) do { MM1(F_, C, 0); MM1(F_, C, 1); MM1(F_, C, 2); MM1(F_, C, 3); } while (0)
#define NREADS (SSM ? 8 : 4 + 2 * NT)
#define RD(N_, KK, I, WA, XA) do { \
    if constexpr ((I) < 4) N_.template rdx<(I) & 3>(XA); \
    else if constexpr (SSM) { if constexpr ((I) < 8) N_.template rdw_lo<KK, ((I) - 4) & 3>(WA); } \
    else if constexpr ((I) < 4 + 2 * NT) { if constexpr (((I) & 1) == 0) N_.template rdw_lo<KK, (((I) - 4) >> 1) % NT>(WA); else N_.template rdw_hi<KK, (((I) - 4) >> 1) % NT>(WA); } \
  } while (0)
#define GRP(F_, N_, KK, C, R0, WA, XA, EXTRA) do { \
    MM1(F_, C, 0); RD(N_, KK, (R0), WA, XA); SB(); \
    MM1(F_, C, 1); RD(N_, KK, (R0) + 1, WA, XA); SB(); \
    MM1(F_, C, 2); RD(N_, KK, (R0) + 2, WA, XA); SB(); \
    MM1(F_, C, 3); RD(N_, KK, (R0) + 3, WA, XA); EXTRA; SB(); } while (0)
#define CSEL1(F_, C) do { const unsigned v_ = (io_ == (C)) ? 0x3F803F80u : 0u; const u32x4 s_ = {v_, v_, v_, v_}; csa = SSM ? mfma16(s_, F_.w(C), csa) : mfma16(F_.w(C), s_, csa); } while (0)
#define CSELALL(F_, M) do { if (M) { int io_ = i16; asm volatile("" : "+v"(io_)); CSEL1(F_, 0); CSEL1(F_, 1); CSEL1(F_, 2); CSEL1(F_, 3); if constexpr (NT > 4) { CSEL1(F_, 4 % NT); CSEL1(F_, 5 % NT); } } SB(); } while (0)
  int slot = 0;
  auto stage = [&](const int s, auto dx_t, auto dw_t, auto bnd_t) __attribute__((always_inline)) {
    constexpr bool DX = decltype(dx_t)::value, DW = decltype(dw_t)::value;
    constexpr int BND = decltype(bnd_t)::value;
    const int nslot = slot == 2 * SLOT ? 0 : slot + SLOT;
    int sox = 0, sow = 0, pows = 0, pslot = 0;
    if constexpr (DX) { sox = cx.xso_base + (s + 3) * 128; asm volatile("" : "+s"(sox)); }
    if constexpr (DW) { pows = cx.wso_base + (s + 2) * cx.wso_step; pslot = nslot == 2 * SLOT ? 0 : nslot + SLOT; asm volatile("" : "+s"(pows), "+s"(pslot)); }
    fa.wait(); SB();
    {
      const unsigned X1 = ax[1] + slot, W1 = aw[1] + slot;
      GRP(fa, fb, 1, 0, 0, W1, X1, if constexpr (DW) dma(pslot, 0, pows, 4));
      GRP(fa, fb, 1, 1, 4, W1, X1, if constexpr (DW) dma(pslot, 0, pows, 5));
      GRP(fa, fb, 1, 2, 8, W1, X1, if constexpr (!SSM && DW) dma(pslot, 0, pows, 6));
      GRP(fa, fb, 1, 3, 12, W1, X1, );
      if constexpr (NT > 4) {
        GRP(fa, fb, 1, 4 % NT, 16, W1, X1, );
        GRP(fa, fb, 1, 5 % NT, 20, W1, X1, );
      }
      CSELALL(fa, ((2 * s) & 3) == wr);
      if constexpr (BND == 0) hook9(std::integral_constant<int, MODE>{});
    }
    fb.wait(); SB();
    const unsigned X0 = ax[0] + nslot, W0 = aw[0] + nslot;
    MMG(fb, 0); SB();
    if constexpr (BND == 1) { if (SSM) WAIT_V(6); else WAIT_V(7); BAR(); }
    if constexpr (BND == 2) { WAIT_V(0); BAR(); }
    if constexpr (BND == 3) {
      if (w < 4) { if (SSM) WAIT_V(14); else WAIT_V(16); } else WAIT_V(0);
      BAR();
    }
    GRP(fb, fa, 0, 1, 0, W0, X0, if constexpr (DX) dma(slot, sox, sow, 0));
    GRP(fb, fa, 0, 2, 4, W0, X0, if constexpr (DX) dma(slot, sox, sow, 1));
    GRP(fb, fa, 0, 3, 8, W0, X0, if constexpr (DX) dma(slot, sox, sow, 2));
    if constexpr (NT > 4) {
      GRP(fb, fa, 0, 4 % NT, 12, W0, X0, if constexpr (DX) dma(slot, sox, sow, 3));
      GRP(fb, fa, 0, 5 % NT, 16, W0, X0, );
    } else { if constexpr (DX) dma(slot, sox, sow, 3); }
    CSELALL(fb, ((2 * s + 1) & 3) == wr);
    slot = nslot;
  };
  using T_ = std::true_type; using F_ = std::false_type;
  using I0 = std::integral_constant<int, 0>; using I1 = std::integral_constant<int, 1>; using I2 = std::integral_constant<int, 2>;
  stage(0, T_{}, F_{}, std::integral_constant<int, 3>{});
  hook7();
#pragma unroll 1
  for (int s = 1; s < 6; ++s) stage(s, T_{}, T_{}, I1{});
  hookA();
  stage(6, T_{}, T_{}, I1{});
  hook8();
  stage(7, F_{}, T_{}, I1{});
  stage(8, F_{}, F_{}, I2{});
  stage(9, F_{}, F_{}, I0{});
  fa.wait();
}

constexpr int DW = 6;
struct P2Ctx { __amdgpu_buffer_rsrc_t rw; int sw, vo; };
DI void p2_ldw(const P2Ctx& c, u32x4 (&wv)[5], int k) {
#pragma unroll
  for (int j = 0; j < 5; ++j) wv[j] = __builtin_amdgcn_raw_buffer_load_b128(c.rw, c.vo, c.sw + k * 5120 + j * 1024, 16);
}
template <bool SSM>
DI unsigned phase1(const Params& p, char* smem, int mt, int cj  , int blk, P2Ctx& p2c, u32x4 (&wv)[DW][5]) {
  int tid = threadIdx.x, lane = tid & 63;
  const int w = __builtin_amdgcn_readfirstlane(tid >> 6);
  const int wr = w & 3, wc = w >> 2;
  int G = lane >> 4, i16 = lane & 15;
  auto relane = [&]() { int tz = threadIdx.x; asm volatile("" : "+v"(tz)); tid = tz; lane = tz & 63; G = lane >> 4; i16 = lane & 15; p2c.vo = lane * 16; };
  const unsigned lbase = (unsigned)(size_t)(lds_cp)smem;
  const int cbi = SSM ? 22 + cj : cj;
  unsigned etag;
  if (w == 7) {
    const char* a;
    if (lane < 32) a = p.ws + WS_ACT + ((size_t)lane * NSTR + NSTEP) * 4096;
    else if (lane < 40) a = p.ws + WS_W2T + ((size_t)(lane - 32) * NSTR + NSTEP) * 5120;
    else a = (const char*)p.dsk;
    __builtin_amdgcn_global_load_lds((const unsigned*)a, (unsigned*)(smem + LDS_PAR + 2048), 4, 0, 0);
  }
  Ctx cx;
  cx.smem = smem;
  cx.rx = __builtin_amdgcn_make_buffer_rsrc((void*)(p.x + (size_t)(256 * mt) * D), 0, 256 * D * 2, RSRC3);
#pragma unroll
  for (int i = 0; i < 4; ++i) {
    int f;
    if (SSM) f = ((lane >> 4) & 1) | ((((2 * w + (i >> 1)) & 3)) << 1);
    else f = ((i & 1) << 2) | (lane >> 4);
    cx.vx[i] = (lane >> 3) * 1280 + (((lane & 7) ^ f) << 4);
  }
  cx.xso_base = w * 32 * 1280; cx.xdst = w * 4096;
  auto wbase = [&](int v) { return SSM ? (64 * cj + 32 * (v >> 2) + 16 * ((v & 3) >> 1)) * 1280 : 16 * (v >> 1) * 3456; };
  auto wdst = [&](int v) { return SSM ? v * 2048 : (v >> 1) * 5120 + (v & 1) * 2560; };
  if (SSM) {
    cx.rw = __builtin_amdgcn_make_buffer_rsrc((void*)((w & 1) ? p.bim : p.bre), 0, P * D * 2, RSRC3);
    cx.vw[0] = (lane >> 3) * 1280 + (((lane & 7) ^ (lane >> 4)) << 4); cx.vw[1] = cx.vw[0] ^ 64; cx.vw[2] = 0;
    cx.wso_step = 128;
  } else {
    cx.rw = __builtin_amdgcn_make_buffer_rsrc((void*)((w & 1) ? p.w3 : p.w1), 0, D * F * 2, RSRC3);
#pragma unroll
    for (int i = 0; i < 3; ++i) {
      const int n = 64 * i + lane, pos = (n / 10) & 15, row = (pos & 3) | ((pos & 4) << 1) | ((pos & 8) >> 1);
      cx.vw[i] = row * 3456 + (n % 10) * 16 + cj * 160;
    }
    cx.wso_step = 64 * 3456;
  }
  cx.wso_base = wbase(w); cx.wdst = wdst(w);
  auto issue_for = [&](int v, int s, int slot) {
    const int sox = v * 32 * 1280 + s * 128, sow = wbase(v) + s * cx.wso_step;
    char* b = smem + slot;
#pragma unroll
    for (int i = 0; i < 4; ++i) dma16(cx.rx, b + v * 4096 + i * 1024, cx.vx[i], sox + i * 10240);
    dma16(cx.rw, b + XSLOT + wdst(v), cx.vw[0], sow);
    dma16(cx.rw, b + XSLOT + wdst(v) + 1024, cx.vw[1], SSM ? sow + 10240 : sow);
    if (!SSM) { if (lane < 32) dma16(cx.rw, b + XSLOT + wdst(v) + 2048, cx.vw[2], sow); }
  };
  const __amdgpu_buffer_rsrc_t rW2T = __builtin_amdgcn_make_buffer_rsrc((void*)(p.ws + WS_W2T), 0, (int)W2T_BYTES, RSRC3);
  auto stats_job = [&](const u32x4& r0, const u32x4& r1) {
    float v[16];
#pragma unroll
    for (int i = 0; i < 4; ++i) { v[2 * i] = __uint_as_float(r0[i] << 16); v[2 * i + 1] = __uint_as_float(r0[i] & 0xFFFF0000u); v[8 + 2 * i] = __uint_as_float(r1[i] << 16); v[9 + 2 * i] = __uint_as_float(r1[i] & 0xFFFF0000u); }
    float sum = 0.f;
#pragma unroll
    for (int i = 0; i < 16; ++i) sum += v[i];
    sum = wave_sum(sum);
    const float mean = sum * (1.0f / D);
    float sq = 0.f;
#pragma unroll
    for (int i = 0; i < 8; ++i) { const float d = v[i] - mean; sq = fmaf(d, d, sq); }
    float sq2 = 0.f;
#pragma unroll
    for (int i = 8; i < 16; ++i) { const float d = v[i] - mean; sq2 = fmaf(d, d, sq2); }
    sq += lane < 16 ? sq2 : 0.f;
    sq = wave_sum(sq);
    const float rstd = 1.0f / sqrtf(sq * (1.0f / D) + 1e-5f);
    if (lane == 0) {
      gu64* g = (gu64*)(p.ws + WS_STATS + (size_t)(256 * mt + 8 * cbi + w) * 16);
      __hip_atomic_store(g, ((unsigned long long)etag << 32) | __float_as_uint(mean), RLX_AGENT);
      __hip_atomic_store(g + 1, ((unsigned long long)etag << 32) | __float_as_uint(rstd), RLX_AGENT);
    }
  };
  const __amdgpu_buffer_rsrc_t rrow = __builtin_amdgcn_make_buffer_rsrc((void*)(p.x + (size_t)(256 * mt + 8 * cbi + w) * D), 0, D * 2, RSRC3);
  if (w < 4) {
    unsigned e_, pa_ = 0, pb_ = 0, pc_ = 0; u32x4 r0, r1;
    { const int vo = lane * 16;
      asm volatile("global_load_dword %2, %5, off sc1\n\tbuffer_load_dwordx4 %0, %3, %4, 0 offen\n\tbuffer_load_dwordx4 %1, %3, %4, 0 offen offset:1024"
                   : "=&v"(r0), "=&v"(r1), "=&v"(e_) : "v"(vo), "s"(rrow), "v"(p.ws + WS_EPOCH) : "memory"); }
    if constexpr (SSM) {
      const int ch = 64 * cj + lane;
      asm volatile("global_load_ushort %0, %3, off\n\tglobal_load_ushort %1, %4, off\n\tglobal_load_ushort %2, %5, off"
                   : "=&v"(pa_), "=&v"(pb_), "=&v"(pc_) : "v"(p.lre + ch), "v"(p.lim + ch), "v"(p.ldt + ch) : "memory");
    }
    issue_for(w, 0, 0); issue_for(w + 4, 0, 0);
    issue_for(w, 1, SLOT); issue_for(w + 4, 1, SLOT);
    if constexpr (SSM) asm volatile("s_waitcnt vmcnt(24)" : "+v"(e_), "+v"(r0), "+v"(r1), "+v"(pa_), "+v"(pb_), "+v"(pc_) :: "memory"); else asm volatile("s_waitcnt vmcnt(28)" : "+v"(e_), "+v"(r0), "+v"(r1) :: "memory");
    etag = e_ + 1u;
    stats_job(r0, r1);
    if (SSM && w == 3) {
      const float a = bf2f((u16)pa_), b = bf2f((u16)pb_);
      const float dt = rbf(expf(bf2f((u16)pc_)));
      const float er = expf(dt * a);
      float sn, cn; sincosf(dt * b, &sn, &cn);
      const float lr = er * cn, li = er * sn;
      const float nr = lr - 1.0f, ni = li, den = a * a + b * b;
      const float cr = (nr * a + ni * b) / den, ci = (ni * a - nr * b) / den;
      float pr = lr, pi = li;
#pragma unroll
      for (int i = 0; i < 4; ++i) { float t = pr * pr - pi * pi; pi = 2.f * pr * pi; pr = t; }
      const float l16r = pr, l16i = pi;
#pragma unroll
      for (int i = 0; i < 4; ++i) { float t = pr * pr - pi * pi; pi = 2.f * pr * pi; pr = t; }
      float* par = (float*)(smem + LDS_PAR) + lane * 8;
      *(f32x4*)par = f32x4{lr, li, cr, ci};
      *(f32x4*)(par + 4) = f32x4{l16r, l16i, pr, pi};
    }
    if (SSM) WAIT_V(14); else WAIT_V(16);
    WAIT_L0();
    BAR();
    issue_for(w, 2, 2 * SLOT); issue_for(w + 4, 2, 2 * SLOT);
  } else {
    const int hv = w - 4;
    unsigned e_; u32x4 r0, r1;
    { const int vo = lane * 16;
      asm volatile("global_load_dword %2, %5, off sc1\n\tbuffer_load_dwordx4 %0, %3, %4, 0 offen\n\tbuffer_load_dwordx4 %1, %3, %4, 0 offen offset:1024"
                   : "=&v"(r0), "=&v"(r1), "=&v"(e_) : "v"(vo), "s"(rrow), "v"(p.ws + WS_EPOCH) : "memory"); }
    const int unit = hv * 256 + blk;
    const bool has_unit = unit < 940, ffn_unit = unit < 540;
    char* rt = smem + LDS_RT + hv * 4096;
    int c0, s0u;
    if (ffn_unit) { s0u = unit / 10; c0 = 4 * (unit % 10); } else { const int u = unit - 540; c0 = u / 10; s0u = NSTEP_F + 4 * (u % 10); }
    if (has_unit) {
      if (ffn_unit) {
        const __amdgpu_buffer_rsrc_t r2 = __builtin_amdgcn_make_buffer_rsrc((void*)p.w2, 0, F * D * 2, RSRC3);
        const int vo = (lane >> 3) * 1280 + (lane & 7) * 16;
#pragma unroll
        for (int i = 0; i < 4; ++i) dma16(r2, rt + i * 1024, vo, (32 * s0u + 8 * i) * 1280 + (c0 >> 2) * 128);
      } else {
        const int vo = (lane & 15) * 1280 + (lane >> 4) * 16;
#pragma unroll
        for (int i = 0; i < 4; ++i) {
          const int sp = s0u - NSTEP_F + i, jg = sp >> 1, part = sp & 1;
          const __amdgpu_buffer_rsrc_t rc = __builtin_amdgcn_make_buffer_rsrc((void*)(part ? p.cim : p.cre), 0, D * P * 2, RSRC3);
          dma16(rc, rt + i * 1024, vo, 16 * c0 * 1280 + jg * 64);
        }
      }
    }
    asm volatile("s_waitcnt vmcnt(0)" : "+v"(e_), "+v"(r0), "+v"(r1) :: "memory");
    etag = e_ + 1u;
    u32x4 vv[4];
    {
      const unsigned rtl = lbase + LDS_RT + hv * 4096;
      if (ffn_unit) {
        const int g = lane >> 4, q = (lane >> 2) & 3, pp = lane & 3;
        const unsigned a0 = rtl + (8 * g + q) * 128 + 8 * pp;
        u32x2 l0 = trrd<0>(a0), h0 = trrd<512>(a0), l1 = trrd<32>(a0), h1 = trrd<512 + 32>(a0), l2 = trrd<64>(a0), h2 = trrd<512 + 64>(a0), l3 = trrd<96>(a0), h3 = trrd<512 + 96>(a0);
        asm volatile("s_waitcnt lgkmcnt(0)" : "+v"(l0), "+v"(h0), "+v"(l1), "+v"(h1), "+v"(l2), "+v"(h2), "+v"(l3), "+v"(h3) :: "memory");
        vv[0] = cat(l0, h0); vv[1] = cat(l1, h1); vv[2] = cat(l2, h2); vv[3] = cat(l3, h3);
      } else {
        u32x4 v0 = rd128<0>(rtl + lane * 16), v1 = rd128<1024>(rtl + lane * 16), v2 = rd128<2048>(rtl + lane * 16), v3 = rd128<3072>(rtl + lane * 16);
        asm volatile("s_waitcnt lgkmcnt(0)" : "+v"(v0), "+v"(v1), "+v"(v2), "+v"(v3) :: "memory");
        vv[0] = v0; vv[1] = v1; vv[2] = v2; vv[3] = v3;
      }
    }
    BAR();
    if (has_unit) {
#pragma unroll
      for (int i = 0; i < 4; ++i) {
        const int c = ffn_unit ? c0 + i : c0, s = ffn_unit ? s0u : s0u + i;
        st_sc1(vv[i], rW2T, lane * 16 + (((c / 5) * NSTR + s) * 5 + (c % 5)) * 1024);
      }
    }
    stats_job(r0, r1);
  }
  auto hook7 = [&]() { if (tid == 0) __hip_atomic_store((gu32*)(p.ws + WS_FLAGW) + blk, etag, RLX_AGENT); };
  const __amdgpu_buffer_rsrc_t rACT = __builtin_amdgcn_make_buffer_rsrc((void*)(p.ws + WS_ACT), 0, (int)ACT_BYTES, RSRC3);
  const __amdgpu_buffer_rsrc_t rFW = __builtin_amdgcn_make_buffer_rsrc((void*)(p.ws + WS_FLAGW), 0, 1024, RSRC3);
  const int mt64 = 4 * mt + wr;
  gu64* stg;
  unsigned long long sta = 0, stb = 0;
  u32x4 fw;
  auto hookA = [&]() { const int vo_ = lane * 16; asm volatile("buffer_load_dwordx4 %0, %1, %2, 0 offen sc1" : "=&v"(fw) : "v"(vo_), "s"(rFW) : "memory"); };
  auto hook8 = [&]() {
    if constexpr (SSM) asm volatile("s_waitcnt vmcnt(6)" : "+v"(fw) :: "memory"); else asm volatile("s_waitcnt vmcnt(7)" : "+v"(fw) :: "memory");
    if (w == 0) {
      unsigned spins = 0;
      while (!__all(fw.x == etag && fw.y == etag && fw.z == etag && fw.w == etag)) {
        if (++spins > SPIN_MAX) break;
        __builtin_amdgcn_s_sleep(1);
        fw = __builtin_amdgcn_raw_buffer_load_b128(rFW, lane * 16, 0, 16);
      }
      if (blk == 0 && lane == 0) __hip_atomic_store((gu32*)(p.ws + WS_EPOCH), etag, RLX_AGENT);
    }
  };
  auto hook9 = [&](auto mode_tag) {
    int tz = threadIdx.x; asm volatile("" : "+v"(tz));
    stg = (gu64*)(p.ws + WS_STATS + (size_t)(256 * mt + 64 * wr + (tz & 63)) * 16);
    sta = __hip_atomic_load(stg, RLX_AGENT); stb = __hip_atomic_load(stg + 1, RLX_AGENT);
  };
  f32x4 csa = {0.f, 0.f, 0.f, 0.f};
  float* cst = (float*)(smem + LDS_CS);
  float* stt = (float*)(smem + LDS_ST);
  auto finish_common = [&](int ntw  , int tile0  ) {
    unsigned spins = 0;
    while (!__all((unsigned)(sta >> 32) == etag && (unsigned)(stb >> 32) == etag)) {
      if (++spins > SPIN_MAX) break;
      __builtin_amdgcn_s_sleep(1);
      sta = __hip_atomic_load(stg, RLX_AGENT); stb = __hip_atomic_load(stg + 1, RLX_AGENT);
    }
    const float mean = __uint_as_float((unsigned)sta), rstd = __uint_as_float((unsigned)stb);
    if (wc == 0) *(f32x2*)(stt + (64 * wr + lane) * 2) = f32x2{rstd, -mean * rstd};
    if (SSM) { if (G == 0) {
#pragma unroll
        for (int c = 0; c < 4; ++c) cst[wr * 160 + (tile0 + c) * 16 + i16] = csa[c]; } }
    else { if (i16 < ntw) *(f32x4*)(cst + wr * 160 + (tile0 + i16) * 16 + 4 * G) = csa; }
    WAIT_L0();
    BAR();
  };
  auto cs4 = [&](int idx) { return (*(const f32x4*)(cst + idx) + *(const f32x4*)(cst + 160 + idx)) + (*(const f32x4*)(cst + 320 + idx) + *(const f32x4*)(cst + 480 + idx)); };
  auto cs1 = [&](int idx) { return (cst[idx] + cst[160 + idx]) + (cst[320 + idx] + cst[480 + idx]); };

  if (!SSM) {
    auto ffn = [&](auto mode_tag) {
      constexpr int MODE = decltype(mode_tag)::value;
      constexpr int NT = Frags<MODE>::NT, P0 = MODE == 0 ? 0 : 3;
      f32x4 acc[4][NT];
#pragma unroll
      for (int T = 0; T < 4; ++T)
#pragma unroll
        for (int c = 0; c < NT; ++c) acc[T][c] = f32x4{0.f, 0.f, 0.f, 0.f};
      main_loop<MODE>(p, cx, lbase, w, lane, acc, csa, hook7, hookA, hook8, hook9);
      relane();
      finish_common(NT, 2 * P0);
      constexpr int NB = 4 * (NT / 2);
      auto ldw_part = [&](int b) {
#pragma unroll
        for (int j = 0; j < 20; ++j)
          if (j * NB / 20 == b) wv[j / 5][j % 5] = __builtin_amdgcn_raw_buffer_load_b128(p2c.rw, p2c.vo, p2c.sw + (j / 5 == 3 ? 3 : 6 + j / 5) * 5120 + (j % 5) * 1024, 16);
      };
      f32x2 st4[4];
#pragma unroll
      for (int T = 0; T < 4; ++T) st4[T] = *(const f32x2*)(stt + (64 * wr + 16 * T + i16) * 2);
#pragma unroll
      for (int Pq = 0; Pq < NT / 2; ++Pq) {
        const int ft = 5 * cj + P0 + Pq;
        const f32x4 cg = cs4((2 * P0 + 2 * Pq) * 16 + 4 * G), cu = cs4((2 * P0 + 2 * Pq + 1) * 16 + 4 * G);
        unsigned pk[4][2];
#pragma unroll
        for (int T = 0; T < 4; ++T) {
          float hv[4];
#pragma unroll
          for (int e = 0; e < 4; ++e) {
            const float g = fmaf(acc[T][2 * Pq][e], st4[T].x, st4[T].y * cg[e]);
            const float u = fmaf(acc[T][2 * Pq + 1][e], st4[T].x, st4[T].y * cu[e]);
            hv[e] = silu_f(g) * u;
          }
          pk[T][0] = pack2(hv[0], hv[1]); pk[T][1] = pack2(hv[2], hv[3]);
          ldw_part(4 * Pq + T);
          SB();
        }
        if (ft < 108) {
#pragma unroll
          for (int T2 = 0; T2 < 4; T2 += 2) {
            const auto a = __builtin_amdgcn_permlane16_swap(pk[T2][0], pk[T2 + 1][0], false, false);
            const auto b = __builtin_amdgcn_permlane16_swap(pk[T2][1], pk[T2 + 1][1], false, false);
            const u32x4 v = {a[0], b[0], a[1], b[1]};
            const int s2 = ft >> 1, gp = 2 * (ft & 1) + (G >> 1), T = T2 + (G & 1);
            st_sc1(v, rACT, T * 1024 + (gp * 16 + i16) * 16 + (mt64 * NSTR + s2) * 4096);
          }
        }
      }
    };
    if (wc == 0) ffn(std::integral_constant<int, 0>{}); else ffn(std::integral_constant<int, 1>{});
  } else {
    f32x4 acc[4][4];
#pragma unroll
    for (int T = 0; T < 4; ++T)
#pragma unroll
      for (int c = 0; c < 4; ++c) acc[T][c] = f32x4{0.f, 0.f, 0.f, 0.f};
    main_loop<2>(p, cx, lbase, w, lane, acc, csa, hook7, hookA, hook8, hook9);
    relane();
    const float* par = (const float*)(smem + LDS_PAR);
    float lr[2], li[2], cr[2], ci[2];
#pragma unroll
    for (int j = 0; j < 2; ++j) {
      const f32x4 pv = *(const f32x4*)(par + (32 * wc + 16 * j + i16) * 8);
      lr[j] = pv.x; li[j] = pv.y; cr[j] = pv.z; ci[j] = pv.w;
    }
    f32x4 pv = *(const f32x4*)(par + lane * 8 + 4);
    asm volatile("" : "+v"(pv), "+v"(lr[0]), "+v"(li[0]), "+v"(cr[0]), "+v"(ci[0]), "+v"(lr[1]), "+v"(li[1]), "+v"(cr[1]), "+v"(ci[1]));
    finish_common(4, 4 * wc);
    {
      float csl[4];
#pragma unroll
      for (int c = 0; c < 4; ++c) csl[c] = cs1((4 * wc + c) * 16 + i16);
#pragma unroll
      for (int T = 0; T < 4; ++T)
#pragma unroll
        for (int e = 0; e < 4; e += 2) {
          const f32x4 sv = *(const f32x4*)(stt + (64 * wr + 16 * G + 4 * T + e) * 2);
#pragma unroll
          for (int c = 0; c < 4; ++c) {
            acc[T][c][e] = fmaf(acc[T][c][e], sv.x, sv.y * csl[c]);
            acc[T][c][e + 1] = fmaf(acc[T][c][e + 1], sv.z, sv.w * csl[c]);
          }
        }
    }
    f32x2* et = (f32x2*)(smem + LDS_ET);
    const int seg = 4 * wr + G;
#pragma unroll
    for (int j = 0; j < 2; ++j) {
      float hr = 0.f, hi = 0.f;
#pragma unroll
      for (int i = 0; i < 16; ++i) {
        const float nr = fmaf(lr[j], hr, fmaf(-li[j], hi, acc[i >> 2][2 * j][i & 3]));
        const float ni = fmaf(lr[j], hi, fmaf(li[j], hr, acc[i >> 2][2 * j + 1][i & 3]));
        hr = nr; hi = ni;
      }
      et[seg * 64 + 32 * wc + 16 * j + i16] = f32x2{hr, hi};
    }
    WAIT_L0(); BAR();
    if (w == 0) {
      const unsigned tag = etag;
      float xr_ = 0.f, xi_ = 0.f;
      f32x2 ev[16];
#pragma unroll
      for (int s = 0; s < 16; ++s) ev[s] = et[s * 64 + lane];
#pragma unroll
      for (int s = 0; s < 16; ++s) {
        const float nr = fmaf(pv.x, xr_, fmaf(-pv.y, xi_, ev[s].x));
        const float ni = fmaf(pv.x, xi_, fmaf(pv.y, xr_, ev[s].y));
        xr_ = nr; xi_ = ni;
      }
      gu64* gran = (gu64*)(p.ws + WS_GRAN);
      if (mt < 7) {
        gu64* g = gran + ((size_t)(mt * 10 + cj) * 64 + lane) * 2;
        __hip_atomic_store(g, ((unsigned long long)tag << 32) | __float_as_uint(xr_), RLX_AGENT);
        __hip_atomic_store(g + 1, ((unsigned long long)tag << 32) | __float_as_uint(xi_), RLX_AGENT);
      }
      float Hr = 0.f, Hi = 0.f;
      if (mt > 0) {
        unsigned long long ga[7], gb[7];
        unsigned spins = 0;
        while (true) {
          bool ok = true;
#pragma unroll
          for (int m = 0; m < 7; ++m) {
            if (m < mt) {
              gu64* g = gran + ((size_t)(m * 10 + cj) * 64 + lane) * 2;
              ga[m] = __hip_atomic_load(g, RLX_AGENT); gb[m] = __hip_atomic_load(g + 1, RLX_AGENT);
            } else { ga[m] = gb[m] = (unsigned long long)tag << 32; }
          }
#pragma unroll
          for (int m = 0; m < 7; ++m) ok = ok && ((unsigned)(ga[m] >> 32) == tag) && ((unsigned)(gb[m] >> 32) == tag);
          if (__all(ok)) break;
          if (++spins > SPIN_MAX) break;
          __builtin_amdgcn_s_sleep(1);
        }
#pragma unroll
        for (int m = 0; m < 7; ++m) {
          if (m < mt) {
            const float sr = __uint_as_float((unsigned)ga[m]), si = __uint_as_float((unsigned)gb[m]);
            const float nr = fmaf(pv.z, Hr, fmaf(-pv.w, Hi, sr));
            const float ni = fmaf(pv.z, Hi, fmaf(pv.w, Hr, si));
            Hr = nr; Hi = ni;
          }
        }
      }
      xr_ = Hr; xi_ = Hi;
#pragma unroll
      for (int s = 0; s < 16; ++s) {
        et[s * 64 + lane] = f32x2{xr_, xi_};
        const float nr = fmaf(pv.x, xr_, fmaf(-pv.y, xi_, ev[s].x));
        const float ni = fmaf(pv.x, xi_, fmaf(pv.y, xr_, ev[s].y));
        xr_ = nr; xi_ = ni;
      }
    }
    WAIT_L0(); BAR();
    p2_ldw(p2c, wv[0], 6); p2_ldw(p2c, wv[1], 7);
    SB();
    f32x2 x0[2];
#pragma unroll
    for (int j = 0; j < 2; ++j) x0[j] = et[seg * 64 + 32 * wc + 16 * j + i16];
    unsigned pre[2][8], pim[2][8];
#pragma unroll
    for (int j = 0; j < 2; ++j) {
      float hr = x0[j].x, hi = x0[j].y;
      float ore[16], oim[16];
#pragma unroll
      for (int i = 0; i < 16; ++i) {
        const float nr = fmaf(lr[j], hr, fmaf(-li[j], hi, acc[i >> 2][2 * j][i & 3]));
        const float ni = fmaf(lr[j], hi, fmaf(li[j], hr, acc[i >> 2][2 * j + 1][i & 3]));
        hr = nr; hi = ni;
        ore[i] = cr[j] * hr - ci[j] * hi;
        oim[i] = -(cr[j] * hi + ci[j] * hr);
      }
#pragma unroll
      for (int i = 0; i < 8; ++i) { pre[j][i] = pack2(ore[2 * i], ore[2 * i + 1]); pim[j][i] = pack2(oim[2 * i], oim[2 * i + 1]); }
    }
    {
      char* tp = smem + LDS_TP + w * 4608;
      const int q = (lane >> 2) & 3, pp = lane & 3;
      const unsigned tb = lbase + LDS_TP + w * 4608 + (8 * G + q) * 144 + 8 * pp;
      const int jg = 2 * cj + wc;
#pragma unroll
      for (int part = 0; part < 2; ++part) {
#pragma unroll
        for (int j = 0; j < 2; ++j) {
          char* t0 = tp + (16 * j + i16) * 144 + 32 * G;
          const unsigned* src = part ? pim[j] : pre[j];
          *(u32x4*)t0 = u32x4{src[0], src[1], src[2], src[3]};
          *(u32x4*)(t0 + 16) = u32x4{src[4], src[5], src[6], src[7]};
        }
        WAIT_L0();
        u32x2 lo[4], hi[4];
        lo[0] = trrd<0>(tb); hi[0] = trrd<576>(tb); lo[1] = trrd<32>(tb); hi[1] = trrd<576 + 32>(tb); lo[2] = trrd<64>(tb); hi[2] = trrd<576 + 64>(tb); lo[3] = trrd<96>(tb); hi[3] = trrd<576 + 96>(tb);
        asm volatile("s_waitcnt lgkmcnt(0)" : "+v"(lo[0]), "+v"(hi[0]), "+v"(lo[1]), "+v"(hi[1]), "+v"(lo[2]), "+v"(hi[2]), "+v"(lo[3]), "+v"(hi[3]) :: "memory");
        const int s2 = NSTEP_F + 2 * jg + part;
#pragma unroll
        for (int T = 0; T < 4; ++T)
          st_sc1(cat(lo[T], hi[T]), rACT, T * 1024 + lane * 16 + (mt64 * NSTR + s2) * 4096);
      }
    }
  }
  WAIT_V(0);
  BAR();
  if (tid == 0) __hip_atomic_store((gu32*)(p.ws + WS_FLAGA + mt * 128) + cbi, etag, RLX_AGENT);
  asm volatile("" ::: "memory"); SB();
  if (SSM) { p2_ldw(p2c, wv[2], 8); p2_ldw(p2c, wv[3], 3); }
  p2_ldw(p2c, wv[4], 4); p2_ldw(p2c, wv[5], 5);
  SB();
  {
    char* lw = smem + LDS_W2 + w * 15360 + lane * 16;
#pragma unroll
    for (int k = 0; k < 3; ++k)
#pragma unroll
      for (int c = 0; c < 5; ++c) *(u32x4*)(lw + k * 5120 + c * 1024) = wv[k][c];
  }
  SB();
  p2_ldw(p2c, wv[0], 0); p2_ldw(p2c, wv[1], 1); p2_ldw(p2c, wv[2], 2);
  SB();
  return etag;
}

DI void p2_run(const Params& p, const char* lw  , __amdgpu_buffer_rsrc_t ra, const P2Ctx& p2c, u32x4 (&wv)[DW][5],
               int sa, int N, int w, int lane, int mt2, unsigned etag, f32x4 (&acc)[4][5]) {
  constexpr int DA = 2, NMIN = 10, NMAX = 14;
  u32x4 a[DA][4];
  const int vo = p2c.vo;
  auto lda = [&](int k, int slot) {
#pragma unroll
    for (int T = 0; T < 4; ++T) a[slot][T] = __builtin_amdgcn_raw_buffer_load_b128(ra, vo, sa + k * 4096 + T * 1024, 16);
  };
  {
    const int fi = w < 4 ? (lane < 22 ? lane : 0) : 22 + (lane < 10 ? lane : 0);
    gu32* fl = (gu32*)(p.ws + WS_FLAGA + mt2 * 128) + fi;
    unsigned spins = 0;
    while (!__all(__hip_atomic_load(fl, RLX_AGENT) == etag)) {
      if (++spins > SPIN_MAX) break;
      __builtin_amdgcn_s_sleep(1);
    }
  }
  __builtin_amdgcn_fence(__ATOMIC_ACQUIRE, "wavefront");
#pragma unroll
  for (int s = 0; s < DA - 1; ++s) lda(s, s);
#pragma unroll
  for (int T = 0; T < 4; ++T)
#pragma unroll
    for (int c = 0; c < 5; ++c) asm volatile("" : "+v"(acc[T][c]));
  asm volatile("s_nop 7" ::: "memory");
  SB();
#pragma unroll
  for (int s = 0; s < NMAX; ++s) {
    if (s < NMIN || s < N) {
      if (s + DA - 1 < NMIN || s + DA - 1 < N) lda(s + DA - 1, (s + DA - 1) % DA);
      SB();
#pragma unroll
      for (int T = 0; T < 4; ++T)
#pragma unroll
        for (int c = 0; c < 5; ++c) mfma16_acc(acc[T][c], wv[s % DW][c], a[s % DA][T]);
      SB();
      if (s < 3) {
#pragma unroll
        for (int c = 0; c < 5; ++c) wv[s][c] = *(const u32x4*)(lw + s * 5120 + c * 1024);
      } else if (s + DW < NMAX) { if (s + DW < NMIN || s + DW < N) p2_ldw(p2c, wv[s % DW], s + DW); }
      SB();
    }
  }
  asm volatile("s_nop 15\n\ts_nop 7" ::: "memory");
}

DI void phase2(const Params& p, char* smem, unsigned etag, const P2Ctx& p2c, u32x4 (&wv)[DW][5], int mt64, int nt, int s0) {
  const int tid = threadIdx.x, lane = tid & 63, w = __builtin_amdgcn_readfirstlane(tid >> 6);
  f32x4 acc[4][5];
#pragma unroll
  for (int T = 0; T < 4; ++T)
#pragma unroll
    for (int c = 0; c < 5; ++c) acc[T][c] = f32x4{0.f, 0.f, 0.f, 0.f};
  const __amdgpu_buffer_rsrc_t ra = __builtin_amdgcn_make_buffer_rsrc((void*)(p.ws + WS_ACT), 0, (int)ACT_BYTES, RSRC3);
  const int sa = (mt64 * NSTR + s0) * 4096;
  const int N = w < 2 ? 14 : (w < 4 ? 13 : 10);
  const char* lw = smem + LDS_W2 + w * 15360 + lane * 16;
  p2_run(p, lw, ra, p2c, wv, sa, N, w, lane, mt64 >> 2, etag, acc);
  auto part_addr = [&](int wv_, int t) { return t < 15 ? LDS_W2 + wv_ * (15360) + t * 1024 : wv_ * 5120 + (t - 15) * 1024; };
  {
    const int g = lane >> 4, r = lane & 15;
    const int pos = g * 16 + ((r + 4 * g) & 15);
#pragma unroll
    for (int T = 0; T < 4; ++T)
#pragma unroll
      for (int c = 0; c < 5; ++c) *(f32x4*)(smem + part_addr(w, T * 5 + c) + pos * 16) = acc[T][c];
  }
  f32x4 res[3]; u32x2 xv[3], dv[3]; unsigned long long ga[3], gb[3];
#pragma unroll
  for (int it = 0; it < 3; ++it) {
    const int item = tid + 512 * it;
    if (item < 1280) {
      const int tok = item / 20, dg = item % 20;
      const int gt = mt64 * 64 + tok, d0 = nt * 80 + dg * 4;
      gu64* sg = (gu64*)(p.ws + WS_STATS + (size_t)gt * 16);
      ga[it] = __hip_atomic_load(sg, RLX_AGENT); gb[it] = __hip_atomic_load(sg + 1, RLX_AGENT);
      xv[it] = *(const u32x2*)(p.x + (size_t)gt * D + d0);
      dv[it] = *(const u32x2*)(p.dsk + d0);
    }
  }
  __syncthreads();
#pragma unroll
  for (int it = 0; it < 3; ++it) {
    const int item = tid + 512 * it;
    if (item < 1280) {
      const int tok = item / 20, dg = item % 20, c = dg >> 2, g = dg & 3, T = tok >> 4, rr = tok & 15;
      const int t = T * 5 + c, off = (g * 16 + ((rr + 4 * g) & 15)) * 16;
      const int gt = mt64 * 64 + tok, d0 = nt * 80 + dg * 4;
      const int ta = t < 15 ? LDS_W2 + t * 1024 : (t - 15) * 1024, st = t < 15 ? 15360 : 5120;
      const char* base = smem + ta + off;
      f32x4 o = *(const f32x4*)(base);
#pragma unroll
      for (int k = 1; k < 4; ++k) o += *(const f32x4*)(base + k * st);
      f32x4 y = *(const f32x4*)(base + 4 * st);
#pragma unroll
      for (int k = 5; k < 8; ++k) y += *(const f32x4*)(base + k * st);
      if ((unsigned)(ga[it] >> 32) != etag || (unsigned)(gb[it] >> 32) != etag) {
        gu64* sg = (gu64*)(p.ws + WS_STATS + (size_t)gt * 16);
        ga[it] = __hip_atomic_load(sg, RLX_AGENT); gb[it] = __hip_atomic_load(sg + 1, RLX_AGENT);
      }
      const float mean = __uint_as_float((unsigned)ga[it]), rstd = __uint_as_float((unsigned)gb[it]);
      const float xs[4] = {__uint_as_float(xv[it].x << 16), __uint_as_float(xv[it].x & 0xFFFF0000u), __uint_as_float(xv[it].y << 16), __uint_as_float(xv[it].y & 0xFFFF0000u)};
      const float ds[4] = {__uint_as_float(dv[it].x << 16), __uint_as_float(dv[it].x & 0xFFFF0000u), __uint_as_float(dv[it].y << 16), __uint_as_float(dv[it].y & 0xFFFF0000u)};
#pragma unroll
      for (int e = 0; e < 4; ++e) {
        const float xn = (xs[e] - mean) * rstd;
        const float yy = fmaf(2.0f, y[e], xn * ds[e]);
        res[it][e] = fmaf(yy, silu_f(o[e]), xs[e]);
      }
      *(f32x4*)(p.out + (size_t)gt * D + d0) = res[it];
    }
  }
}

__global__ __launch_bounds__(512, 2) void k_fused(Params p) {
  extern __shared__ __attribute__((aligned(16))) char smem[];
  const int b = blockIdx.x, xcd = b & 7, i = b >> 3;
  if ((threadIdx.x >> 6) == 7 && (threadIdx.x & 63) < 14) {
    unsigned long long pc;
    asm volatile("s_getpc_b64 %0" : "=s"(pc));
    const char* a = (const char*)(pc & ~127ull) + (size_t)(i * 14 + (threadIdx.x & 63)) * 128;
    __builtin_amdgcn_global_load_lds((const unsigned*)a, (unsigned*)(smem + LDS_PAR + 2304), 4, 0, 0);
  }
  asm volatile("" :: "s"(p.x), "s"(p.lre), "s"(p.lim), "s"(p.bre), "s"(p.bim), "s"(p.cre), "s"(p.cim), "s"(p.ldt), "s"(p.dsk), "s"(p.w1), "s"(p.w3), "s"(p.w2), "s"(p.out), "s"(p.ws));
  const int ml = i & 3, cl = i >> 2;
  const int mt = (xcd & 1) * 4 + ml, cb = (xcd >> 1) * 8 + cl;
  const int w = __builtin_amdgcn_readfirstlane(threadIdx.x >> 6), lane = threadIdx.x & 63;
  const int mt64 = (xcd >> 1) * 8 + (i & 7), nt = (xcd & 1) * 4 + (i >> 3);
  const int s0 = w < 4 ? (w < 2 ? 14 * w : 28 + 13 * (w - 2)) : 54 + 10 * (w - 4);
  P2Ctx p2c;
  p2c.rw = __builtin_amdgcn_make_buffer_rsrc((void*)(p.ws + WS_W2T), 0, (int)W2T_BYTES, RSRC3);
  p2c.sw = (nt * NSTR + s0) * 5120; p2c.vo = 0;
  u32x4 wv[DW][5];
  unsigned etag;
  if (cb < 22) etag = phase1<false>(p, smem, mt, cb, b, p2c, wv);
  else etag = phase1<true>(p, smem, mt, cb - 22, b, p2c, wv);
  phase2(p, smem, etag, p2c, wv, mt64, nt, s0);
}
}

extern "C" void kernel_launch(void* const* d_in, const int* in_sizes, int n_in, void* d_out, int out_size, void* d_ws, size_t ws_size, hipStream_t stream) {
  using namespace s5;
  Params p;
  p.x = (const u16*)d_in[0]; p.lre = (const u16*)d_in[1]; p.lim = (const u16*)d_in[2];
  p.bre = (const u16*)d_in[3]; p.bim = (const u16*)d_in[4]; p.cre = (const u16*)d_in[5]; p.cim = (const u16*)d_in[6];
  p.ldt = (const u16*)d_in[7]; p.dsk = (const u16*)d_in[8];
  p.w1 = (const u16*)d_in[10]; p.w3 = (const u16*)d_in[11]; p.w2 = (const u16*)d_in[12];
  p.out = (float*)d_out; p.ws = (char*)d_ws;
  (void)hipFuncSetAttribute((const void*)k_fused, hipFuncAttributeMaxDynamicSharedMemorySize, LDS1_BYTES);
  k_fused<<<256, 512, LDS1_BYTES, stream>>>(p);
}
```
